```python
import torch

def _softmax(x, axis=-1):
    return torch.softmax(x, dim=axis)
def _gelu(x):
    return torch.nn.functional.gelu(x, approximate="tanh")
def _silu(x):
    return torch.nn.functional.silu(x)
def _sigmoid(x):
    return torch.sigmoid(x)
def _layernorm(x, axis=-1, eps=1e-5):
    m = x.mean(dim=axis, keepdim=True)
    v = x.var(dim=axis, keepdim=True, unbiased=False)
    return (x - m) / torch.sqrt(v + eps)
def _scatter_add(tgt, idx, src, axis=0):
    return tgt.clone().index_add_(axis, idx, src)
def _segment_sum(src, seg, n):
    out = torch.zeros((n,) + src.shape[1:], dtype=src.dtype)
    return out.index_add_(0, seg, src)
def _pad(x, pw):
    flat = tuple(v for pair in reversed(pw) for v in pair)
    return torch.nn.functional.pad(x, flat)

kernel_name = 'gated_mix__grouped_kv_mqa__dense_activation_gelu__prompt'

def setup_inputs():

    _w = (lambda a: (lambda t: t.to(torch.bfloat16) if t.is_floating_point() else t)(torch.as_tensor(a)))
    def _A_setup():
        B, S, D, H = 1, 2048, 448, 16
        Dh = D // H
        return {
            "x": _w((lambda *s: torch.randn(*s, dtype=torch.bfloat16) * 0.05)(B, S, D)),
            "wq": _w((lambda *s: torch.randn(*s, dtype=torch.bfloat16) * 1.6)(D, H * Dh)),
            "wk": _w((lambda *s: torch.randn(*s, dtype=torch.bfloat16) * 1.6)(D, Dh)),
            "wv": _w((lambda *s: torch.randn(*s, dtype=torch.bfloat16) * 0.05)(D, Dh)),
            "wo": _w((lambda *s: torch.randn(*s, dtype=torch.bfloat16) * 0.05)(H * Dh, D)),
        }
    _A_in = _A_setup()

    def _B_setup():
        B, S, D, F = 1, 2048, 448, 1792
        return {
            "x": _w((lambda *s: torch.randn(*s, dtype=torch.bfloat16) * 0.05)(B, S, D)),
            "w1": _w((lambda *s: torch.randn(*s, dtype=torch.bfloat16) * 3.2)(D, F) * (D ** -0.5)),
            "b1": _w((lambda *s: torch.randn(*s, dtype=torch.bfloat16) * 3.2)(F) * 0.01),
            "w2": _w((lambda *s: torch.randn(*s, dtype=torch.bfloat16) * 0.4)(F, D) * (F ** -0.5)),
            "b2": _w((lambda *s: torch.randn(*s, dtype=torch.bfloat16) * 0.8)(D) * 0.01),
        }
    _B_in = _B_setup()

    return {
        "A_x": _A_in["x"],
        "A_wq": _A_in["wq"],
        "A_wk": _A_in["wk"],
        "A_wv": _A_in["wv"],
        "A_wo": _A_in["wo"],
        "B_x": _B_in["x"],
        "B_w1": _B_in["w1"],
        "B_b1": _B_in["b1"],
        "B_w2": _B_in["w2"],
        "B_b2": _B_in["b2"],
        "gate_w": _w((lambda *s: torch.randn(*s, dtype=torch.bfloat16) * 0.05)(448) * 16.0),
    }

def reference(A_x, A_wq, A_wk, A_wv, A_wo, B_x, B_w1, B_b1, B_w2, B_b2, gate_w):
    def _A_ref(x, wq, wk, wv, wo):
        B, S, D = x.shape
        H = 16
        Dh = D // H
        q = (lambda x, *d: x.permute(*d))((x @ wq).reshape(B, S, H, Dh), 0, 2, 1, 3)
        k = x @ wk
        v = x @ wv
        scores = torch.einsum("bhqd,bkd->bhqk", q, k) * (Dh ** -0.5)
        p = _softmax(scores, axis=-1)
        o = torch.einsum("bhqk,bkd->bhqd", p, v)
        return (lambda x, *d: x.permute(*d))(o, 0, 2, 1, 3).reshape(B, S, H * Dh) @ wo

    def _B_ref(x, w1, b1, w2, b2):
        h = torch.einsum("bsd,df->bsf", x, w1) + b1
        h = _gelu(h)
        return torch.einsum("bsf,fd->bsd", h, w2) + b2

    x = A_x
    g = _sigmoid((x * gate_w).sum(-1, keepdims=True))
    a = _A_ref(x, A_wq, A_wk, A_wv, A_wo)
    b = _B_ref(x, B_w1, B_b1, B_w2, B_b2)
    return g * a + (1 - g) * b
```

```cpp
#include <hip/hip_runtime.h>
#include <hip/hip_bf16.h>
typedef __hip_bfloat16 bf16;
#define S_ 2048
#define D_ 448
#define H_ 16
#define DH 28
#define F_ 1792
__device__ inline float b2f(bf16 x) { return __bfloat162float(x); }
__device__ inline float rb(float x) { return __bfloat162float(__float2bfloat16(x)); }

__global__ void gemm_naive(const bf16* A, const bf16* B, float* C, int M, int N, int K, const bf16* bias, int act) {
    int n = blockIdx.x * 64 + (threadIdx.x & 63), m = blockIdx.y * 4 + (threadIdx.x >> 6);
    if (n >= N) return;
    float s = 0;
    for (int k = 0; k < K; k++) s += b2f(A[m * K + k]) * b2f(B[k * N + n]);
    if (bias) s += b2f(bias[n]);
    if (act) { float x = rb(s); float u = 0.7978845608028654f * (x + 0.044715f * x * x * x); s = 0.5f * x * (1.f + tanhf(u)); }
    C[m * N + n] = s;
}
__global__ void tobf(const float* a, bf16* b, int n) { int i = blockIdx.x * 256 + threadIdx.x; if (i < n) b[i] = __float2bfloat16(a[i]); }
__global__ void attn_naive(const float* q, const float* k, const float* v, bf16* o) {
    int t = blockIdx.x, h = blockIdx.y;
    __shared__ float qs[DH]; __shared__ float p[S_]; __shared__ float red[256];
    if (threadIdx.x < DH) qs[threadIdx.x] = q[t * D_ + h * DH + threadIdx.x];
    __syncthreads();
    float mx = -1e30f;
    for (int j = threadIdx.x; j < S_; j += 256) { float s = 0; for (int d = 0; d < DH; d++) s += qs[d] * k[j * DH + d]; s *= 0.1889822365046136f; p[j] = s; mx = fmaxf(mx, s); }
    red[threadIdx.x] = mx; __syncthreads();
    for (int o2 = 128; o2 > 0; o2 >>= 1) { if (threadIdx.x < o2) red[threadIdx.x] = fmaxf(red[threadIdx.x], red[threadIdx.x + o2]); __syncthreads(); }
    mx = red[0]; __syncthreads();
    float sum = 0;
    for (int j = threadIdx.x; j < S_; j += 256) { float e = __expf(p[j] - mx); p[j] = e; sum += e; }
    red[threadIdx.x] = sum; __syncthreads();
    for (int o2 = 128; o2 > 0; o2 >>= 1) { if (threadIdx.x < o2) red[threadIdx.x] += red[threadIdx.x + o2]; __syncthreads(); }
    sum = red[0]; __syncthreads();
    int d = threadIdx.x & 31, part = threadIdx.x >> 5; float acc = 0;
    if (d < DH) for (int j = part; j < S_; j += 8) acc += p[j] * v[j * DH + d];
    red[threadIdx.x] = acc; __syncthreads();
    if (threadIdx.x < DH) { float s = 0; for (int i = 0; i < 8; i++) s += red[i * 32 + threadIdx.x]; o[t * D_ + h * DH + threadIdx.x] = __float2bfloat16(s / sum); }
}
__global__ void combine(const bf16* x, const bf16* gw, const float* a, const float* b, bf16* out) {
    int t = blockIdx.x; __shared__ float red[64]; __shared__ float g;
    float s = 0; for (int i = threadIdx.x; i < D_; i += 64) s += b2f(x[t * D_ + i]) * b2f(gw[i]);
    red[threadIdx.x] = s; __syncthreads();
    if (threadIdx.x == 0) { float z = 0; for (int i = 0; i < 64; i++) z += red[i]; g = 1.f / (1.f + __expf(-z)); }
    __syncthreads();
    for (int i = threadIdx.x; i < D_; i += 64) out[t * D_ + i] = __float2bfloat16(g * a[t * D_ + i] + (1.f - g) * b[t * D_ + i]);
}
extern "C" void kernel_launch(void* const* d_in, const int* in_sizes, int n_in, void* d_out, int out_size, void* d_ws, size_t ws_size, hipStream_t stream) {
    const bf16 *Ax = (const bf16*)d_in[0], *wq = (const bf16*)d_in[1], *wk = (const bf16*)d_in[2], *wv = (const bf16*)d_in[3], *wo = (const bf16*)d_in[4];
    const bf16 *Bx = (const bf16*)d_in[5], *w1 = (const bf16*)d_in[6], *b1 = (const bf16*)d_in[7], *w2 = (const bf16*)d_in[8], *b2 = (const bf16*)d_in[9], *gw = (const bf16*)d_in[10];
    float* ws = (float*)d_ws;
    float* q = ws; float* k = q + S_ * D_; float* v = k + S_ * DH; float* hf = v + S_ * DH; float* a = hf + S_ * F_; float* b = a + S_ * D_;
    bf16* o = (bf16*)(b + S_ * D_); bf16* hb = o + S_ * D_;
    gemm_naive<<<dim3(7, S_ / 4), 256, 0, stream>>>(Ax, wq, q, S_, D_, D_, nullptr, 0);
    gemm_naive<<<dim3(1, S_ / 4), 256, 0, stream>>>(Ax, wk, k, S_, DH, D_, nullptr, 0);
    gemm_naive<<<dim3(1, S_ / 4), 256, 0, stream>>>(Ax, wv, v, S_, DH, D_, nullptr, 0);
    attn_naive<<<dim3(S_, H_), 256, 0, stream>>>(q, k, v, o);
    gemm_naive<<<dim3(7, S_ / 4), 256, 0, stream>>>(o, wo, a, S_, D_, D_, nullptr, 0);
    gemm_naive<<<dim3(28, S_ / 4), 256, 0, stream>>>(Ax, w1, hf, S_, F_, D_, b1, 1);
    tobf<<<S_ * F_ / 256, 256, 0, stream>>>(hf, hb, S_ * F_);
    gemm_naive<<<dim3(7, S_ / 4), 256, 0, stream>>>(hb, w2, b, S_, D_, F_, b2, 0);
    combine<<<S_, 64, 0, stream>>>(Ax, gw, a, b, (bf16*)d_out);
}
```

```cpp
#include <hip/hip_runtime.h>
#include <hip/hip_bf16.h>

typedef short bf16x8 __attribute__((ext_vector_type(8)));
typedef short s16x4 __attribute__((ext_vector_type(4)));
typedef __bf16 bf16x2 __attribute__((ext_vector_type(2)));
typedef float f32x16 __attribute__((ext_vector_type(16)));
typedef float f32x4 __attribute__((ext_vector_type(4)));
typedef unsigned u32x4 __attribute__((ext_vector_type(4)));
typedef unsigned u32x2 __attribute__((ext_vector_type(2)));
typedef short v4i16_t __attribute__((ext_vector_type(4)));
#define LDS_AS __attribute__((address_space(3)))
#define DI __device__ __forceinline__

constexpr int S_ = 2048, D_ = 448, F_ = 1792;
constexpr size_t WS_QF = 0;
constexpr size_t WS_KF = 2u << 20;
constexpr size_t WS_VF = WS_KF + 131072;
constexpr size_t WS_G = WS_VF + 131072;
constexpr size_t WS_FLAG = WS_KF + 524288;
constexpr size_t WS_OF = 3u << 20;
constexpr size_t WS_HF = 5u << 20;

#define MFMA(a, b, c) __builtin_amdgcn_mfma_f32_32x32x16_bf16(a, b, c, 0, 0, 0)

typedef float f32x2 __attribute__((ext_vector_type(2)));
DI unsigned cvtpk(float lo, float hi) { f32x2 v = {lo, hi}; return __builtin_bit_cast(unsigned, __builtin_convertvector(v, bf16x2)); }
DI void glds16(unsigned voff, const void* sbase, unsigned lds_dst) {
    unsigned keep;
    asm volatile("s_mov_b32 %0, m0\n\ts_mov_b32 m0, %3\n\ts_nop 0\n\tglobal_load_lds_dwordx4 %1, %2\n\ts_mov_b32 m0, %0"
                 : "=&s"(keep) : "v"(voff), "s"(sbase), "s"(lds_dst) : "memory");
}
DI bf16x8 lds_b128(unsigned a) { return *(const LDS_AS bf16x8*)(size_t)a; }
DI s16x4 lds_tr(unsigned a) { return __builtin_bit_cast(s16x4, __builtin_amdgcn_ds_read_tr16_b64_v4i16((LDS_AS v4i16_t*)(size_t)a)); }
DI bf16x8 lds_tr2(unsigned a0, unsigned a1) { s16x4 lo = lds_tr(a0), hi = lds_tr(a1); return __builtin_shufflevector(lo, hi, 0, 1, 2, 3, 4, 5, 6, 7); }
DI float dot2(unsigned a, unsigned b, float c) { return __builtin_amdgcn_fdot2_f32_bf16(__builtin_bit_cast(bf16x2, a), __builtin_bit_cast(bf16x2, b), c, false); }
DI float bf2f(unsigned short v) { return __uint_as_float((unsigned)v << 16); }
DI u32x4 pack8(const f32x16& a, int s) {
    u32x4 r;
    r[0] = cvtpk(a[8 * s + 0], a[8 * s + 1]); r[1] = cvtpk(a[8 * s + 2], a[8 * s + 3]);
    r[2] = cvtpk(a[8 * s + 4], a[8 * s + 5]); r[3] = cvtpk(a[8 * s + 6], a[8 * s + 7]);
    return r;
}
DI float gelu_tanh(float x) {
    const float c1 = -2.0f * 1.4426950408889634f * 0.7978845608028654f, c2 = c1 * 0.044715f;
    float t = x * x;
    float e = __builtin_amdgcn_exp2f(x * __builtin_fmaf(t, c2, c1));
    return x * __builtin_amdgcn_rcpf(1.0f + e);
}

constexpr unsigned P1_SLOT = 36864, P1_W = 16384, P1_GW = 4 * P1_SLOT;
constexpr unsigned P1_LDS = P1_GW + 1024;

struct Params {
    const unsigned short *x, *wq, *wk, *wv, *wo, *w1, *b1, *w2, *b2, *gw;
    unsigned short* out;
    char* ws;
};

DI void phase1(const Params& p, unsigned lds0, int g, int j, int w, int lane) {
    const int h = lane >> 5, l31 = lane & 31;
    const bool odd = j & 1;
    const int pp = j >> 1;
    const int fb0 = 7 * pp + (odd ? 4 : 0);
    const int kt = pp & 3, isV = pp >> 2;
    const bool isA = w < 4;
    const int tb = w & 3;
    const int T = 4 * g + tb;
    const int fbase = odd ? fb0 - 1 : fb0;
    f32x16 acc[3];
    float z = 0.f;
    if (isA) {
        unsigned xoff[4];
#pragma unroll
        for (int i = 0; i < 4; i++) {
            const int row = 32 * tb + 8 * i + (lane >> 3);
            const int c = (lane & 7) ^ ((row >> 1) & 7);
            xoff[i] = row * 896 + c * 16;
        }
        const char* xb = (const char*)p.x + (size_t)g * 128 * 896;
#pragma unroll
        for (int b = 0; b < 3; b++)
#pragma unroll
            for (int r = 0; r < 16; r++) acc[b][r] = 0.f;
        {
            const unsigned short* b1 = p.b1 + 4 * h;
#pragma unroll
            for (int b = 1; b < 3; b++) {
                if (b == 2 || !odd) {
                    const int f0 = 32 * (fbase + b - 1);
#pragma unroll
                    for (int q = 0; q < 4; q++) {
                        u32x2 v = *(const u32x2*)(b1 + f0 + 8 * q);
                        acc[b][4 * q + 0] = __uint_as_float(v[0] << 16); acc[b][4 * q + 1] = __uint_as_float(v[0] & 0xffff0000u);
                        acc[b][4 * q + 2] = __uint_as_float(v[1] << 16); acc[b][4 * q + 3] = __uint_as_float(v[1] & 0xffff0000u);
                    }
                }
            }
        }
        asm volatile("s_waitcnt vmcnt(0)" ::: "memory");
        if (w == 0) glds16((lane < 56 ? lane : 55) * 16, p.gw, lds0 + P1_GW);
#pragma unroll
        for (int it = 0; it < 4; it++)
#pragma unroll
            for (int i = 0; i < 4; i++) glds16(xoff[i], xb + it * 128, lds0 + it * P1_SLOT + (32 * tb + 8 * i) * 128);
        const unsigned xrd = lds0 + (32 * tb + l31) * 128;
        const int xsw = (l31 >> 1) & 7;
        const unsigned wrd = lds0 + P1_W + (8 * h + ((lane & 15) >> 2)) * 64 + ((lane >> 4) & 1) * 32 + (lane & 3) * 8;
        const bool doKV = odd && (tb == kt);
        unsigned xa[4];
#pragma unroll
        for (int s = 0; s < 4; s++) xa[s] = xrd + (((2 * s + h) ^ xsw) << 4);
        unsigned gwa = lds0 + P1_GW + h * 16;
#pragma unroll 1
        for (int it = 0; it < 7; it++) {
            if (it < 4) asm volatile("s_waitcnt vmcnt(12)\n\ts_barrier" ::: "memory");
            else if (it == 4) asm volatile("s_waitcnt vmcnt(8)\n\ts_barrier" ::: "memory");
            else if (it == 5) asm volatile("s_waitcnt vmcnt(4)\n\ts_barrier" ::: "memory");
            else asm volatile("s_waitcnt vmcnt(0)\n\ts_barrier" ::: "memory");
            const unsigned sl = (it & 3) * P1_SLOT;
            const unsigned wa = wrd + sl;
#pragma unroll
            for (int s = 0; s < 4; s++) {
                bf16x8 xf = lds_b128(xa[s] + sl);
                bf16x8 w0 = lds_tr2(wa + s * 1024, wa + s * 1024 + 256);
                bf16x8 w2 = lds_tr2(wa + 2 * 4096 + s * 1024, wa + 2 * 4096 + s * 1024 + 256);
                u32x4 gwv = *(const LDS_AS u32x4*)(size_t)(gwa + s * 32);
                acc[0] = MFMA(w0, xf, acc[0]);
                acc[2] = MFMA(w2, xf, acc[2]);
                if (!odd) {
                    bf16x8 w1 = lds_tr2(wa + 4096 + s * 1024, wa + 4096 + s * 1024 + 256);
                    acc[1] = MFMA(w1, xf, acc[1]);
                } else if (doKV) {
                    bf16x8 w1 = lds_tr2(wa + 4096 + s * 1024, wa + 4096 + s * 1024 + 256);
                    if (isV) acc[1] = MFMA(xf, w1, acc[1]); else acc[1] = MFMA(w1, xf, acc[1]);
                }
                u32x4 xu = __builtin_bit_cast(u32x4, xf);
                z = dot2(xu[0], gwv[0], z); z = dot2(xu[1], gwv[1], z); z = dot2(xu[2], gwv[2], z); z = dot2(xu[3], gwv[3], z);
            }
            gwa += 128;
            if (it < 3) {
                asm volatile("s_waitcnt lgkmcnt(0)\n\ts_barrier" ::: "memory");
#pragma unroll
                for (int i = 0; i < 4; i++) glds16(xoff[i], xb + (it + 4) * 128, lds0 + sl + (32 * tb + 8 * i) * 128);
            }
        }
        {
            const float qs = 1.4426950408889634f * 0.1889822365046136f;
            f32x16 q = acc[0] * qs;
            u32x4 q0 = pack8(q, 0), q1 = pack8(q, 1);
            if (h == 0) { q1[2] = 0; q1[3] = 0; }
            u32x4* dst = (u32x4*)(p.ws + WS_QF + ((size_t)(T * 16 + j) * 2) * 1024) + lane;
            dst[0] = q0; dst[64] = q1;
        }
        if (doKV) {
            u32x4 a0 = pack8(acc[1], 0), a1 = pack8(acc[1], 1);
            if (isV) {
                if (l31 == 24) { a0 = (u32x4){0x3f803f80u, 0x3f803f80u, 0x3f803f80u, 0x3f803f80u}; a1 = a0; }
                else if (l31 > 24 && l31 < 28) { a0 = (u32x4){0, 0, 0, 0}; a1 = a0; }
            } else if (h == 0) { a1[2] = 0x3f803f80u; a1[3] = 0x00003f80u; }
            u32x4* dst = (u32x4*)(p.ws + (isV ? WS_VF : WS_KF) + (size_t)T * 2048) + lane;
            dst[0] = a0; dst[64] = a1;
        }
#pragma unroll
        for (int b = 1; b < 3; b++) {
            if (b == 2 || !odd) {
                f32x16 a = acc[b];
#pragma unroll
                for (int r = 0; r < 16; r++) a[r] = gelu_tanh(a[r]);
                u32x4* dst = (u32x4*)(p.ws + WS_HF + ((size_t)T * 112 + 2 * (fbase + b - 1)) * 1024) + lane;
                dst[0] = pack8(a, 0); dst[64] = pack8(a, 1);
            }
        }
        z += __shfl_xor(z, 32);
        if (j == 0 && h == 0) ((float*)(p.ws + WS_G))[T * 32 + l31] = __builtin_amdgcn_rcpf(1.0f + __builtin_amdgcn_exp2f(-1.4426950408889634f * z));
    } else {
        const int r = 16 * tb + (lane >> 2), c = lane & 3;
        unsigned woff[5];
        const char* wb[5];
        unsigned wstep[5];
        const unsigned c28 = c == 3 ? 40 : c * 16;
        woff[0] = r * 896 + c28; wb[0] = (const char*)p.wq + j * 56; wstep[0] = 64 * 896;
        if (odd) { woff[1] = r * 56 + c28; wb[1] = (const char*)(isV ? p.wv : p.wk); wstep[1] = 64 * 56; }
        else { woff[1] = r * 3584 + c * 16; wb[1] = (const char*)p.w1 + (size_t)fbase * 64; wstep[1] = 64 * 3584; }
#pragma unroll
        for (int b = 2; b < 5; b++) { woff[b] = r * 3584 + c * 16; wb[b] = (const char*)p.w1 + (size_t)(fbase + b - 1) * 64; wstep[b] = 64 * 3584; }
        {
            const unsigned short* b1 = p.b1 + 4 * h;
#pragma unroll
            for (int b = 0; b < 2; b++) {
                const int f0 = 32 * (fbase + b + 2);
#pragma unroll
                for (int q = 0; q < 4; q++) {
                    u32x2 v = *(const u32x2*)(b1 + f0 + 8 * q);
                    acc[b][4 * q + 0] = __uint_as_float(v[0] << 16); acc[b][4 * q + 1] = __uint_as_float(v[0] & 0xffff0000u);
                    acc[b][4 * q + 2] = __uint_as_float(v[1] << 16); acc[b][4 * q + 3] = __uint_as_float(v[1] & 0xffff0000u);
                }
            }
        }
        asm volatile("s_waitcnt vmcnt(0)" ::: "memory");
#pragma unroll
        for (int it = 0; it < 4; it++)
#pragma unroll
            for (int b = 0; b < 5; b++) glds16(woff[b], wb[b] + (size_t)it * wstep[b], lds0 + it * P1_SLOT + P1_W + b * 4096 + tb * 1024);
        const unsigned xrd = lds0 + (32 * tb + l31) * 128;
        const int xsw = (l31 >> 1) & 7;
        const unsigned wrd = lds0 + P1_W + (8 * h + ((lane & 15) >> 2)) * 64 + ((lane >> 4) & 1) * 32 + (lane & 3) * 8;
        unsigned xa[4];
#pragma unroll
        for (int s = 0; s < 4; s++) xa[s] = xrd + (((2 * s + h) ^ xsw) << 4);
#pragma unroll 1
        for (int it = 0; it < 7; it++) {
            if (it < 4) asm volatile("s_waitcnt vmcnt(15)\n\ts_barrier" ::: "memory");
            else if (it == 4) asm volatile("s_waitcnt vmcnt(10)\n\ts_barrier" ::: "memory");
            else if (it == 5) asm volatile("s_waitcnt vmcnt(5)\n\ts_barrier" ::: "memory");
            else asm volatile("s_waitcnt vmcnt(0)\n\ts_barrier" ::: "memory");
            const unsigned sl = (it & 3) * P1_SLOT;
            const unsigned wa = wrd + sl;
#pragma unroll
            for (int s = 0; s < 4; s++) {
                bf16x8 xf = lds_b128(xa[s] + sl);
                bf16x8 w3 = lds_tr2(wa + 3 * 4096 + s * 1024, wa + 3 * 4096 + s * 1024 + 256);
                bf16x8 w4 = lds_tr2(wa + 4 * 4096 + s * 1024, wa + 4 * 4096 + s * 1024 + 256);
                acc[0] = MFMA(w3, xf, acc[0]);
                acc[1] = MFMA(w4, xf, acc[1]);
            }
            if (it < 3) {
                asm volatile("s_waitcnt lgkmcnt(0)\n\ts_barrier" ::: "memory");
#pragma unroll
                for (int b = 0; b < 5; b++) glds16(woff[b], wb[b] + (size_t)(it + 4) * wstep[b], lds0 + sl + P1_W + b * 4096 + tb * 1024);
            }
        }
#pragma unroll
        for (int b = 0; b < 2; b++) {
            f32x16 a = acc[b];
#pragma unroll
            for (int r2 = 0; r2 < 16; r2++) a[r2] = gelu_tanh(a[r2]);
            u32x4* dst = (u32x4*)(p.ws + WS_HF + ((size_t)T * 112 + 2 * (fbase + b + 2)) * 1024) + lane;
            dst[0] = pack8(a, 0); dst[64] = pack8(a, 1);
        }
    }
}


constexpr unsigned P2_RING = 24576;
constexpr unsigned P2_MRG = 4 * P2_RING;
constexpr unsigned P2_MAX = P2_MRG + 49152;
constexpr unsigned P2_CTR = P2_MAX + 2048;
constexpr unsigned P2_LDS = P2_CTR + 64;
#define SB __builtin_amdgcn_sched_barrier(0)
#define EX2(x) __builtin_amdgcn_exp2f(x)

DI void lds_sync4(unsigned ctr, unsigned target, int lane) {
    asm volatile("s_waitcnt lgkmcnt(0)" ::: "memory");
    LDS_AS unsigned* c = (LDS_AS unsigned*)(size_t)ctr;
    if (lane == 0) __hip_atomic_fetch_add(c, 1u, __ATOMIC_RELAXED, __HIP_MEMORY_SCOPE_WORKGROUP);
    for (int spin = 0; spin < (1 << 22); spin++) {
        unsigned v = __builtin_amdgcn_readfirstlane(__hip_atomic_load(c, __ATOMIC_RELAXED, __HIP_MEMORY_SCOPE_WORKGROUP));
        if (v >= target) break;
        __builtin_amdgcn_s_sleep(1);
    }
    asm volatile("" ::: "memory");
}
DI bf16x8 ldg8(const char* base, unsigned off) { return *(const bf16x8*)(base + off); }
DI void st_sc1(void* ptr, u32x4 v) { asm volatile("global_store_dwordx4 %0, %1, off sc1" ::"v"(ptr), "v"(v) : "memory"); }

DI void attn_step(f32x16& S, bf16x8 kn0, bf16x8 kn1, bf16x8 qn0, bf16x8 qn1, bf16x8 v0c, bf16x8 v1p, f32x16& Oc, f32x16& Op, u32x4& P1) {
    const f32x16 zero = {};
    f32x16 Sn = MFMA(kn0, qn0, zero); SB;
    u32x4 p0, p1;
    p0[0] = cvtpk(EX2(S[0]), EX2(S[1])); p0[1] = cvtpk(EX2(S[2]), EX2(S[3])); SB;
    Sn = MFMA(kn1, qn1, Sn); SB;
    p0[2] = cvtpk(EX2(S[4]), EX2(S[5])); p0[3] = cvtpk(EX2(S[6]), EX2(S[7])); SB;
    Op = MFMA(v1p, __builtin_bit_cast(bf16x8, P1), Op); SB;
    p1[0] = cvtpk(EX2(S[8]), EX2(S[9])); p1[1] = cvtpk(EX2(S[10]), EX2(S[11])); SB;
    Oc = MFMA(v0c, __builtin_bit_cast(bf16x8, p0), Oc); SB;
    p1[2] = cvtpk(EX2(S[12]), EX2(S[13])); p1[3] = cvtpk(EX2(S[14]), EX2(S[15])); SB;
    P1 = p1; S = Sn;
}
DI float max16(const f32x16& s) {
    float a = __builtin_fmaxf(__builtin_fmaxf(s[0], s[1]), s[2]), b = __builtin_fmaxf(__builtin_fmaxf(s[3], s[4]), s[5]);
#pragma unroll
    for (int r = 6; r < 14; r += 4) { a = __builtin_fmaxf(__builtin_fmaxf(a, s[r]), s[r + 1]); b = __builtin_fmaxf(__builtin_fmaxf(b, s[r + 2]), s[r + 3]); }
    a = __builtin_fmaxf(__builtin_fmaxf(a, s[14]), s[15]);
    return __builtin_fmaxf(a, b);
}

DI void attn_wave(const Params& p, unsigned lds0, int g, int j, int w, int lane) {
    const int h = lane >> 5, l31 = lane & 31;
    const char* qb_ = p.ws + WS_QF + ((size_t)(4 * g) * 16 + j) * 2048;
    const char* kb = p.ws + WS_KF;
    const char* vb = p.ws + WS_VF;
    const unsigned lo = lane * 16;
    bf16x8 q[4][2];
#pragma unroll
    for (int i = 0; i < 4; i++) { q[i][0] = ldg8(qb_, lo + i * 32768); q[i][1] = ldg8(qb_, lo + i * 32768 + 1024); }
    const float gtok = ((const float*)(p.ws + WS_G))[(4 * g + w) * 32 + l31];
    const f32x16 zero = {};
    unsigned nsync = 0;
    for (int attempt = 0; attempt < 2; attempt++) {
        float m[4];
        if (attempt == 0) {
            bf16x8 k0 = ldg8(kb, lo), k1 = ldg8(kb, lo + 1024);
#pragma unroll
            for (int i = 0; i < 4; i++) { f32x16 s = MFMA(k0, q[i][0], zero); s = MFMA(k1, q[i][1], s); m[i] = max16(s); }
        } else {
#pragma unroll
            for (int i = 0; i < 4; i++) {
                u32x4 t = __builtin_bit_cast(u32x4, q[i][1]);
                if (h == 0) { t[2] = 0; t[3] = 0; }
                q[i][1] = __builtin_bit_cast(bf16x8, t);
                m[i] = -INFINITY;
            }
#pragma unroll 1
            for (int t = 0; t < 16; t++) {
                bf16x8 k0 = ldg8(kb, lo + (16 * w + t) * 2048), k1 = ldg8(kb, lo + (16 * w + t) * 2048 + 1024);
#pragma unroll
                for (int i = 0; i < 4; i++) { f32x16 s = MFMA(k0, q[i][0], zero); s = MFMA(k1, q[i][1], s); m[i] = __builtin_fmaxf(m[i], max16(s)); }
            }
        }
#pragma unroll
        for (int i = 0; i < 4; i++) {
            auto rr = __builtin_amdgcn_permlane32_swap(__float_as_uint(m[i]), __float_as_uint(m[i]), false, false);
            m[i] = __builtin_fmaxf(__uint_as_float(rr[0]), __uint_as_float(rr[1]));
        }
        if (attempt == 1) {
            LDS_AS float* mx = (LDS_AS float*)(size_t)(lds0 + P2_MAX);
            if (h == 0) {
#pragma unroll
                for (int i = 0; i < 4; i++) mx[(w * 4 + i) * 32 + l31] = m[i];
            }
            nsync += 4; lds_sync4(lds0 + P2_CTR, nsync, lane);
#pragma unroll
            for (int i = 0; i < 4; i++)
#pragma unroll
                for (int u = 0; u < 4; u++) m[i] = __builtin_fmaxf(m[i], mx[(u * 4 + i) * 32 + l31]);
        }
#pragma unroll
        for (int i = 0; i < 4; i++) {
            const float nm = -m[i];
            const unsigned a = cvtpk(nm, 0.f) & 0xffffu;
            const float r1 = nm - __uint_as_float(a << 16);
            const unsigned b = cvtpk(r1, 0.f) & 0xffffu;
            const float r2 = r1 - __uint_as_float(b << 16);
            const unsigned c = cvtpk(r2, 0.f) & 0xffffu;
            u32x4 t = __builtin_bit_cast(u32x4, q[i][1]);
            if (h == 0) { t[2] = a | (b << 16); t[3] = c; }
            q[i][1] = __builtin_bit_cast(bf16x8, t);
        }
        f32x16 O[4];
#pragma unroll
        for (int i = 0; i < 4; i++) O[i] = zero;
        const char* kw = kb + (size_t)(16 * w) * 2048;
        const char* vw = vb + (size_t)(16 * w) * 2048;
        bf16x8 kA0 = ldg8(kw, lo), kA1 = ldg8(kw, lo + 1024), vA0 = ldg8(vw, lo), vA1 = ldg8(vw, lo + 1024);
        bf16x8 kB0 = ldg8(kw, lo + 2048), kB1 = ldg8(kw, lo + 3072), vB0 = vA0, vB1 = vA1;
        f32x16 S = MFMA(kA0, q[0][0], zero); S = MFMA(kA1, q[0][1], S);
        u32x4 P1 = {0, 0, 0, 0};
#pragma unroll 1
        for (int t = 0; t < 16; t += 2) {
            attn_step(S, kA0, kA1, q[1][0], q[1][1], vA0, vB1, O[0], O[3], P1);
            vB0 = ldg8(vw, lo + (t + 1) * 2048); vB1 = ldg8(vw, lo + (t + 1) * 2048 + 1024); SB;
            attn_step(S, kA0, kA1, q[2][0], q[2][1], vA0, vA1, O[1], O[0], P1);
            attn_step(S, kA0, kA1, q[3][0], q[3][1], vA0, vA1, O[2], O[1], P1);
            { const int tn = t + 2 < 16 ? t + 2 : 15; kA0 = ldg8(kw, lo + tn * 2048); kA1 = ldg8(kw, lo + tn * 2048 + 1024); SB; }
            attn_step(S, kB0, kB1, q[0][0], q[0][1], vA0, vA1, O[3], O[2], P1);
            attn_step(S, kB0, kB1, q[1][0], q[1][1], vB0, vA1, O[0], O[3], P1);
            { const int tn = t + 2 < 16 ? t + 2 : 15; vA0 = ldg8(vw, lo + tn * 2048); vA1 = ldg8(vw, lo + tn * 2048 + 1024); SB; }
            attn_step(S, kB0, kB1, q[2][0], q[2][1], vB0, vB1, O[1], O[0], P1);
            attn_step(S, kB0, kB1, q[3][0], q[3][1], vB0, vB1, O[2], O[1], P1);
            { const int tn = t + 3 < 16 ? t + 3 : 15; kB0 = ldg8(kw, lo + tn * 2048); kB1 = ldg8(kw, lo + tn * 2048 + 1024); SB; }
            attn_step(S, kA0, kA1, q[0][0], q[0][1], vB0, vB1, O[3], O[2], P1);
        }
        O[3] = MFMA(vB1, __builtin_bit_cast(bf16x8, P1), O[3]);
#pragma unroll
        for (int i = 0; i < 4; i++) {
            if (i != w) {
                const int slot = i - (i > w ? 1 : 0);
                const unsigned a = lds0 + P2_MRG + (w * 3 + slot) * 4096 + lo;
#pragma unroll
                for (int rg = 0; rg < 4; rg++) {
                    f32x4 v = {O[i][4 * rg], O[i][4 * rg + 1], O[i][4 * rg + 2], O[i][4 * rg + 3]};
                    *(LDS_AS f32x4*)(size_t)(a + rg * 1024) = v;
                }
            }
        }
        nsync += 4; lds_sync4(lds0 + P2_CTR, nsync, lane);
        f32x16 o = zero;
#pragma unroll
        for (int i = 0; i < 4; i++) if (i == w) o = O[i];
#pragma unroll
        for (int u = 0; u < 4; u++) {
            if (u != w) {
                const int slot = w - (w > u ? 1 : 0);
                const unsigned a = lds0 + P2_MRG + (u * 3 + slot) * 4096 + lo;
#pragma unroll
                for (int rg = 0; rg < 4; rg++) {
                    f32x4 v = *(const LDS_AS f32x4*)(size_t)(a + rg * 1024);
                    o[4 * rg] += v[0]; o[4 * rg + 1] += v[1]; o[4 * rg + 2] += v[2]; o[4 * rg + 3] += v[3];
                }
            }
        }
        auto rr = __builtin_amdgcn_permlane32_swap(__float_as_uint(o[12]), __float_as_uint(o[12]), false, false);
        const float l = __uint_as_float(rr[0]);
        if (attempt == 0) {
            bool ok = l < 1.152921504606846976e18f;
#pragma unroll
            for (int r = 0; r < 16; r++) ok = ok && (__builtin_fabsf(o[r]) < 3.0e38f);
            LDS_AS unsigned* badf = (LDS_AS unsigned*)(size_t)(lds0 + P2_CTR + 8);
            if (__builtin_amdgcn_ballot_w64(!ok) != 0 && lane == 0) __hip_atomic_store(badf, 1u, __ATOMIC_RELAXED, __HIP_MEMORY_SCOPE_WORKGROUP);
            nsync += 4; lds_sync4(lds0 + P2_CTR, nsync, lane);
            const unsigned bad = __builtin_amdgcn_readfirstlane(__hip_atomic_load(badf, __ATOMIC_RELAXED, __HIP_MEMORY_SCOPE_WORKGROUP));
            if (bad) continue;
        }
        const float sc = gtok * __builtin_amdgcn_rcpf(l);
#pragma unroll
        for (int r = 0; r < 16; r++) o[r] *= sc;
        u32x4 o0 = pack8(o, 0), o1 = pack8(o, 1);
        if (h == 0) { o1[2] = 0; o1[3] = 0; }
        char* dst = p.ws + WS_OF + ((size_t)(4 * g + w) * 32 + 2 * j) * 1024 + lo;
        st_sc1(dst, o0); st_sc1(dst + 1024, o1);
        break;
    }
    asm volatile("s_waitcnt vmcnt(0)" ::: "memory");
    nsync += 4; lds_sync4(lds0 + P2_CTR, nsync, lane);
    if (w == 0 && lane == 0) __hip_atomic_fetch_add((unsigned*)(p.ws + WS_FLAG + g * 256), 1u, __ATOMIC_RELAXED, __HIP_MEMORY_SCOPE_AGENT);
}

DI void ffn2_wave(const Params& p, unsigned lds0, int g, int j, int wb, int lane) {
    const int h = lane >> 5, l31 = lane & 31;
    const unsigned ring = lds0 + wb * P2_RING;
    const int th = j & 1, cs = j >> 1, n0 = 56 * cs, T0 = 4 * g + 2 * th;
    const int r = lane >> 2, c = lane & 3;
    const unsigned woff0 = r * 896 + c * 16, woff1 = r * 896 + 64 + (c == 3 ? 2 : c) * 16;
    const char* wsrc = (const char*)p.w2 + n0 * 2 + (size_t)(28 * wb * 16) * 896;
    const char* hsrc = p.ws + WS_HF + ((size_t)T0 * 112 + 28 * wb) * 1024;
    const unsigned lo = lane * 16;
    const float g0 = ((const float*)(p.ws + WS_G))[T0 * 32 + l31], g1 = ((const float*)(p.ws + WS_G))[T0 * 32 + 32 + l31];
    f32x16 acc[4];
#pragma unroll
    for (int i = 0; i < 4; i++) acc[i] = (f32x16){};
#define ISSUE(st, so) do { glds16(woff0, wsrc + (size_t)(st) * (16 * 896), (so)); glds16(woff1, wsrc + (size_t)(st) * (16 * 896), (so) + 1024); \
        glds16(lo, hsrc + (size_t)(st) * 1024, (so) + 2048); glds16(lo, hsrc + (size_t)(st) * 1024 + 112 * 1024, (so) + 3072); } while (0)
    const unsigned wrd = (4 * h + ((lane & 15) >> 2)) * 64 + ((lane >> 4) & 1) * 32 + (lane & 3) * 8;
#define COMPUTE(so) do { bf16x8 w0 = lds_tr2((so) + wrd, (so) + wrd + 512), w1 = lds_tr2((so) + wrd + 1024, (so) + wrd + 1536); \
        bf16x8 h0 = lds_b128((so) + 2048 + lo), h1 = lds_b128((so) + 3072 + lo); \
        acc[0] = MFMA(w0, h0, acc[0]); acc[1] = MFMA(w0, h1, acc[1]); acc[2] = MFMA(w1, h0, acc[2]); acc[3] = MFMA(w1, h1, acc[3]); } while (0)
    asm volatile("s_waitcnt vmcnt(0)" ::: "memory");
#pragma unroll
    for (int st = 0; st < 5; st++) ISSUE(st, ring + st * 4096);
    unsigned so = ring, si = ring + 5 * 4096;
#pragma unroll 1
    for (int st = 0; st < 23; st++) {
        asm volatile("s_waitcnt lgkmcnt(0)" ::: "memory");
        ISSUE(st + 5, si);
        asm volatile("s_waitcnt vmcnt(20)" ::: "memory");
        COMPUTE(so);
        so = (so == ring + 5 * 4096) ? ring : so + 4096;
        si = (si == ring + 5 * 4096) ? ring : si + 4096;
    }
    asm volatile("s_waitcnt vmcnt(16)" ::: "memory"); COMPUTE(ring + 5 * 4096);
    asm volatile("s_waitcnt vmcnt(12)" ::: "memory"); COMPUTE(ring);
    asm volatile("s_waitcnt vmcnt(8)" ::: "memory"); COMPUTE(ring + 4096);
    asm volatile("s_waitcnt vmcnt(4)" ::: "memory"); COMPUTE(ring + 2 * 4096);
    asm volatile("s_waitcnt vmcnt(0)" ::: "memory"); COMPUTE(ring + 3 * 4096);
    asm volatile("s_waitcnt lgkmcnt(0)" ::: "memory");
    {
        const char* wos = (const char*)p.wo + n0 * 2 + (size_t)(28 * 4 * wb) * 896;
        const unsigned adj = (r >= 8) ? 4 * 896 : 0;
#pragma unroll
        for (int st = 0; st < 8; st++) {
            const int hd = st >> 1, s = st & 1;
            const unsigned ro = (28 * hd + 16 * s) * 896;
            glds16(woff0 + ro - (s ? adj : 0), wos, ring + st * 2048);
            glds16(woff1 + ro - (s ? adj : 0), wos, ring + st * 2048 + 1024);
        }
    }
    const float f0 = 1.0f - g0, f1 = 1.0f - g1;
    acc[0] *= f0; acc[2] *= f0; acc[1] *= f1; acc[3] *= f1;
    {
        unsigned* flag = (unsigned*)(p.ws + WS_FLAG + g * 256);
        for (int spin = 0; spin < (1 << 22); spin++) {
            unsigned v = __builtin_amdgcn_readfirstlane(__hip_atomic_load(flag, __ATOMIC_RELAXED, __HIP_MEMORY_SCOPE_AGENT));
            if (v >= 16) break;
            __builtin_amdgcn_s_sleep(2);
        }
        __builtin_amdgcn_fence(__ATOMIC_ACQUIRE, "agent");
        asm volatile("s_waitcnt vmcnt(0)" ::: "memory");
    }
    {
        const char* osrc = p.ws + WS_OF + ((size_t)T0 * 32 + 8 * wb) * 1024;
        bf16x8 o0[8], o1[8];
#pragma unroll
        for (int st = 0; st < 8; st++) { o0[st] = ldg8(osrc, lo + st * 1024); o1[st] = ldg8(osrc, lo + st * 1024 + 32768); }
#pragma unroll
        for (int st = 0; st < 8; st++) {
            const unsigned so2 = ring + st * 2048;
            bf16x8 w0 = lds_tr2(so2 + wrd, so2 + wrd + 512), w1 = lds_tr2(so2 + wrd + 1024, so2 + wrd + 1536);
            acc[0] = MFMA(w0, o0[st], acc[0]); acc[1] = MFMA(w0, o1[st], acc[1]); acc[2] = MFMA(w1, o0[st], acc[2]); acc[3] = MFMA(w1, o1[st], acc[3]);
        }
    }
    asm volatile("s_waitcnt lgkmcnt(0)" ::: "memory");
#pragma unroll
    for (int i = 0; i < 4; i++) {
        if (i != wb) {
            const unsigned a = ring + i * 4096 + lo;
#pragma unroll
            for (int rg = 0; rg < 4; rg++) {
                f32x4 v = {acc[i][4 * rg], acc[i][4 * rg + 1], acc[i][4 * rg + 2], acc[i][4 * rg + 3]};
                *(LDS_AS f32x4*)(size_t)(a + rg * 1024) = v;
            }
        }
    }
    lds_sync4(lds0 + P2_CTR + 4, 4, lane);
    f32x16 o = {};
#pragma unroll
    for (int i = 0; i < 4; i++) if (i == wb) o = acc[i];
#pragma unroll
    for (int u = 0; u < 4; u++) {
        if (u != wb) {
            const unsigned a = lds0 + u * P2_RING + wb * 4096 + lo;
#pragma unroll
            for (int rg = 0; rg < 4; rg++) {
                f32x4 v = *(const LDS_AS f32x4*)(size_t)(a + rg * 1024);
                o[4 * rg] += v[0]; o[4 * rg + 1] += v[1]; o[4 * rg + 2] += v[2]; o[4 * rg + 3] += v[3];
            }
        }
    }
    {
        const int nb = wb >> 1, T = T0 + (wb & 1);
        const float fg = (wb & 1) ? f1 : f0;
        const int nc = n0 + 32 * nb + 4 * h;
        unsigned short* orow = p.out + (size_t)(T * 32 + l31) * D_ + nc;
#pragma unroll
        for (int rg = 0; rg < 4; rg++) {
            if (rg < 3 || nb == 0) {
                u32x2 bv = *(const u32x2*)(p.b2 + nc + 8 * rg);
                float v0 = o[4 * rg] + fg * __uint_as_float(bv[0] << 16), v1 = o[4 * rg + 1] + fg * __uint_as_float(bv[0] & 0xffff0000u);
                float v2 = o[4 * rg + 2] + fg * __uint_as_float(bv[1] << 16), v3 = o[4 * rg + 3] + fg * __uint_as_float(bv[1] & 0xffff0000u);
                u32x2 ov = {cvtpk(v0, v1), cvtpk(v2, v3)};
                *(u32x2*)(orow + 8 * rg) = ov;
            }
        }
    }
}

__global__ void __launch_bounds__(512, 2) k2(Params p) {
    extern __shared__ char smem[];
    const unsigned lds0 = (unsigned)(size_t)smem;
    const int b = blockIdx.x, xcd = b & 7, idx = b >> 3;
    const int g = xcd * 2 + (idx >> 4), j = idx & 15;
    const int w = __builtin_amdgcn_readfirstlane(threadIdx.x >> 6), lane = threadIdx.x & 63;
    if (threadIdx.x < 4) *(LDS_AS unsigned*)(size_t)(lds0 + P2_CTR + 4 * threadIdx.x) = 0;
    __syncthreads();
    if (w < 4) attn_wave(p, lds0, g, j, w, lane);
    else ffn2_wave(p, lds0, g, j, w - 4, lane);
}

__global__ void __launch_bounds__(512, 2) k1(Params p) {
    extern __shared__ char smem[];
    const unsigned lds0 = (unsigned)(size_t)smem;
    const int b = blockIdx.x, xcd = b & 7, idx = b >> 3;
    const int g = xcd * 2 + (idx >> 4), j = idx & 15;
    const int w = __builtin_amdgcn_readfirstlane(threadIdx.x >> 6), lane = threadIdx.x & 63;
    if (b < 16 && threadIdx.x == 0) *(unsigned*)(p.ws + WS_FLAG + b * 256) = 0;
    phase1(p, lds0, g, j, w, lane);
}

extern "C" void kernel_launch(void* const* d_in, const int* in_sizes, int n_in, void* d_out, int out_size, void* d_ws, size_t ws_size, hipStream_t stream) {
    Params p;
    p.x = (const unsigned short*)d_in[0]; p.wq = (const unsigned short*)d_in[1]; p.wk = (const unsigned short*)d_in[2];
    p.wv = (const unsigned short*)d_in[3]; p.wo = (const unsigned short*)d_in[4]; p.w1 = (const unsigned short*)d_in[6];
    p.b1 = (const unsigned short*)d_in[7]; p.w2 = (const unsigned short*)d_in[8]; p.b2 = (const unsigned short*)d_in[9];
    p.gw = (const unsigned short*)d_in[10]; p.out = (unsigned short*)d_out; p.ws = (char*)d_ws;
    hipFuncSetAttribute((const void*)k1, hipFuncAttributeMaxDynamicSharedMemorySize, P1_LDS);
    k1<<<256, 512, P1_LDS, stream>>>(p);
    hipFuncSetAttribute((const void*)k2, hipFuncAttributeMaxDynamicSharedMemorySize, P2_LDS);
    k2<<<256, 512, P2_LDS, stream>>>(p);
}
```

```cpp
#include <hip/hip_runtime.h>
#include <hip/hip_bf16.h>

typedef short bf16x8 __attribute__((ext_vector_type(8)));
typedef short s16x4 __attribute__((ext_vector_type(4)));
typedef __bf16 bf16x2 __attribute__((ext_vector_type(2)));
typedef float f32x16 __attribute__((ext_vector_type(16)));
typedef float f32x4 __attribute__((ext_vector_type(4)));
typedef float f32x2 __attribute__((ext_vector_type(2)));
typedef unsigned u32x4 __attribute__((ext_vector_type(4)));
typedef unsigned u32x2 __attribute__((ext_vector_type(2)));
typedef short v4i16_t __attribute__((ext_vector_type(4)));
typedef unsigned long long u64;
#define LDS_AS __attribute__((address_space(3)))
#define DI __device__ __forceinline__

constexpr int S_ = 2048, D_ = 448, F_ = 1792;
constexpr size_t WS_EPOCH = 0;
constexpr size_t WS_FKV = 4096;
constexpr size_t WS_FH = 8192;
constexpr size_t WS_FO = 16384;
constexpr size_t WS_KV = 1u << 20;
constexpr size_t WS_OF = 2u << 20;
constexpr size_t WS_HF = 4u << 20;

#define STAMP(i) do {} while (0)
#define MFMA(a, b, c) __builtin_amdgcn_mfma_f32_32x32x16_bf16(a, b, c, 0, 0, 0)
#define SB __builtin_amdgcn_sched_barrier(0)
#define EX2(x) __builtin_amdgcn_exp2f(x)
constexpr int SPIN_CAP = 1 << 21;
#define ROT1 0
#define ROT2 (4 * cs + th + 2 * (g & 1))
#define POLL_SLEEP 1
#define B_PRIO 0

DI unsigned cvtpk(float lo, float hi) { f32x2 v = {lo, hi}; return __builtin_bit_cast(unsigned, __builtin_convertvector(v, bf16x2)); }
DI void glds16(unsigned voff, const void* sbase, unsigned lds_dst) {
    unsigned keep;
    asm volatile("s_mov_b32 %0, m0\n\ts_mov_b32 m0, %3\n\ts_nop 0\n\tglobal_load_lds_dwordx4 %1, %2\n\ts_mov_b32 m0, %0"
                 : "=&s"(keep) : "v"(voff), "s"(sbase), "s"(lds_dst) : "memory");
}
DI void glds16_sc1(unsigned voff, const void* sbase, unsigned lds_dst) {
    unsigned keep;
    asm volatile("s_mov_b32 %0, m0\n\ts_mov_b32 m0, %3\n\ts_nop 0\n\tglobal_load_lds_dwordx4 %1, %2 sc1\n\ts_mov_b32 m0, %0"
                 : "=&s"(keep) : "v"(voff), "s"(sbase), "s"(lds_dst) : "memory");
}
DI bf16x8 lds_b128(unsigned a) { return *(const LDS_AS bf16x8*)(size_t)a; }
DI s16x4 lds_tr(unsigned a) { return __builtin_bit_cast(s16x4, __builtin_amdgcn_ds_read_tr16_b64_v4i16((LDS_AS v4i16_t*)(size_t)a)); }
DI bf16x8 lds_tr2(unsigned a0, unsigned a1) { s16x4 lo = lds_tr(a0), hi = lds_tr(a1); return __builtin_shufflevector(lo, hi, 0, 1, 2, 3, 4, 5, 6, 7); }
DI float dot2(unsigned a, unsigned b, float c) { return __builtin_amdgcn_fdot2_f32_bf16(__builtin_bit_cast(bf16x2, a), __builtin_bit_cast(bf16x2, b), c, false); }
DI u32x4 pack8(const f32x16& a, int s) {
    u32x4 r;
    r[0] = cvtpk(a[8 * s + 0], a[8 * s + 1]); r[1] = cvtpk(a[8 * s + 2], a[8 * s + 3]);
    r[2] = cvtpk(a[8 * s + 4], a[8 * s + 5]); r[3] = cvtpk(a[8 * s + 6], a[8 * s + 7]);
    return r;
}
DI float gelu_tanh(float x) {
    const float c1 = -2.0f * 1.4426950408889634f * 0.7978845608028654f, c2 = c1 * 0.044715f;
    float t = x * x;
    float e = EX2(x * __builtin_fmaf(t, c2, c1));
    return x * __builtin_amdgcn_rcpf(1.0f + e);
}
DI __amdgpu_buffer_rsrc_t ws_rsrc(const char* ws) { return __builtin_amdgcn_make_buffer_rsrc((void*)ws, 0, 16u << 20, 0x00020000); }
DI void st_sc1(__amdgpu_buffer_rsrc_t r, unsigned off, u32x4 v) { __builtin_amdgcn_raw_buffer_store_b128(v, r, off, 0, 16); }
DI void flag_set(char* ws, size_t off, u64 tag) { __hip_atomic_store((u64*)(ws + off), tag, __ATOMIC_RELAXED, __HIP_MEMORY_SCOPE_AGENT); }
DI void flags_wait(const char* ws, unsigned off, bool on, u64 tag) {
#pragma unroll 1
    for (int spin = 0; spin < SPIN_CAP; spin++) {
        u64 v = tag;
        if (on) v = __hip_atomic_load((const u64*)(ws + off), __ATOMIC_RELAXED, __HIP_MEMORY_SCOPE_AGENT);
        if (__builtin_amdgcn_ballot_w64(v != tag) == 0) break;
        __builtin_amdgcn_s_sleep(POLL_SLEEP);
    }
    asm volatile("s_waitcnt vmcnt(0)" ::: "memory");
}
DI u64 uniform64(u64 v) {
    asm volatile("" : "+v"(v) :: "memory");
    const unsigned lo = __builtin_amdgcn_readfirstlane((unsigned)v), hi = __builtin_amdgcn_readfirstlane((unsigned)(v >> 32));
    return ((u64)hi << 32) | lo;
}
DI unsigned lds_ld(unsigned a) { return __hip_atomic_load((LDS_AS unsigned*)(size_t)a, __ATOMIC_RELAXED, __HIP_MEMORY_SCOPE_WORKGROUP); }
DI void lds_st(unsigned a, unsigned v) { __hip_atomic_store((LDS_AS unsigned*)(size_t)a, v, __ATOMIC_RELAXED, __HIP_MEMORY_SCOPE_WORKGROUP); }
DI void lds_sync4(unsigned ctr, unsigned target, int lane) {
    asm volatile("s_waitcnt lgkmcnt(0)" ::: "memory");
    LDS_AS unsigned* c = (LDS_AS unsigned*)(size_t)ctr;
    if (lane == 0) __hip_atomic_fetch_add(c, 1u, __ATOMIC_RELAXED, __HIP_MEMORY_SCOPE_WORKGROUP);
#pragma unroll 1
    for (int spin = 0; spin < SPIN_CAP; spin++) {
        unsigned v = __builtin_amdgcn_readfirstlane(__hip_atomic_load(c, __ATOMIC_RELAXED, __HIP_MEMORY_SCOPE_WORKGROUP));
        if (v >= target) break;
        __builtin_amdgcn_s_sleep(1);
    }
    asm volatile("" ::: "memory");
}

struct Params {
    const unsigned short *x, *wq, *wk, *wv, *wo, *w1, *b1, *w2, *b2, *gw;
    unsigned short* out;
    char* ws;
};

constexpr unsigned X_SLOT = 16384, W_0 = 65536, W_SLOT = 20480, W_Q = 0, W_R1 = 4096, W_R2 = 12288;
constexpr unsigned L_WO = 0, L_MRG = 57344, L_BST = 106496, B_AREA = 12288;
constexpr unsigned P1_GW = L_BST + 4 * B_AREA, P1_B1 = P1_GW + 1024, L_G = P1_B1 + 1024;
constexpr unsigned L_CTR = L_G + 512;
constexpr unsigned LDS_BYTES = L_CTR + 64;
static_assert(W_0 + 2 * W_SLOT <= L_BST && L_MRG + 12 * 4096 <= L_BST && LDS_BYTES <= 163840, "LDS map");

struct P1Frag { bf16x8 x, w[4]; u32x4 gw; };
template <int MODE> DI void p1_load(P1Frag& f, unsigned xa, unsigned wP, unsigned wS, unsigned gwa, int s) {
    f.x = lds_b128(xa);
    if (MODE <= 1) { f.w[0] = lds_tr2(wS + W_Q + s * 1024, wS + W_Q + s * 1024 + 256); f.gw = *(const LDS_AS u32x4*)(size_t)(gwa + s * 32); }
    if (MODE == 1) { f.w[1] = lds_tr2(wS + W_R1 + s * 1024, wS + W_R1 + s * 1024 + 256); f.w[2] = lds_tr2(wS + W_R1 + 4096 + s * 1024, wS + W_R1 + 4096 + s * 1024 + 256); }
    if (MODE == 2) { f.w[2] = lds_tr2(wP + W_R1 + s * 2048, wP + W_R1 + s * 2048 + 256); f.w[3] = lds_tr2(wP + W_R1 + s * 2048 + 512, wP + W_R1 + s * 2048 + 768); }
    if (MODE >= 2) { f.w[0] = lds_tr2(wP + W_R2 + s * 2048, wP + W_R2 + s * 2048 + 256); f.w[1] = lds_tr2(wP + W_R2 + s * 2048 + 512, wP + W_R2 + s * 2048 + 768); }
}
template <int MODE> DI void p1_mma(const P1Frag& f, f32x16 (&acc)[4], float (&z)[4]) {
    acc[0] = MFMA(f.w[0], f.x, acc[0]);
    if (MODE >= 1) acc[1] = MFMA(f.w[1], f.x, acc[1]);
    if (MODE == 1) acc[2] = MFMA(f.x, f.w[2], acc[2]);
    if (MODE == 2) { acc[2] = MFMA(f.w[2], f.x, acc[2]); acc[3] = MFMA(f.w[3], f.x, acc[3]); }
    if (MODE <= 1) {
        u32x4 xu = __builtin_bit_cast(u32x4, f.x);
        z[0] = dot2(xu[0], f.gw[0], z[0]); z[1] = dot2(xu[1], f.gw[1], z[1]); z[2] = dot2(xu[2], f.gw[2], z[2]); z[3] = dot2(xu[3], f.gw[3], z[3]);
    }
}
template <int N> DI void wait_vm_bar() { asm volatile("s_waitcnt vmcnt(%0) lgkmcnt(0)\n\ts_barrier" ::"n"(N) : "memory"); }
DI void wait_bar() { asm volatile("s_waitcnt lgkmcnt(0)\n\ts_barrier" ::: "memory"); }
struct P1Addr { unsigned xa[4], wP, wS, gwa; };
DI P1Addr p1_addr(unsigned lds0, int tb, int lane) {
    P1Addr a;
    const int h = lane >> 5, l31 = lane & 31;
    const unsigned xrd = lds0 + (32 * tb + l31) * 128;
    const int xsw = (l31 >> 1) & 7;
#pragma unroll
    for (int s = 0; s < 4; s++) a.xa[s] = xrd + (((2 * s + h) ^ xsw) << 4);
    const unsigned wl = ((lane & 15) >> 2) * 64 + ((lane >> 4) & 1) * 32 + (lane & 3) * 8;
    a.wP = lds0 + W_0 + h * 1024 + wl; a.wS = lds0 + W_0 + h * 512 + wl;
    a.gwa = lds0 + P1_GW + h * 16;
    return a;
}
#define P1_HEAD(MODE, sx, sw, gw_, H0, H1, H2) \
        SB; p1_load<MODE>(f1, a.xa[1] + (sx), a.wP + (sw), a.wS + (sw), gw_, 1); H0; SB; p1_mma<MODE>(f0, acc, z); \
        SB; p1_load<MODE>(f0, a.xa[2] + (sx), a.wP + (sw), a.wS + (sw), gw_, 2); H1; SB; p1_mma<MODE>(f1, acc, z); \
        SB; p1_load<MODE>(f1, a.xa[3] + (sx), a.wP + (sw), a.wS + (sw), gw_, 3); H2; SB; p1_mma<MODE>(f0, acc, z); SB;
DI int rot7(int it, int rot) { const int r = it + rot; return r >= 7 ? r - 7 : r; }
template <int MODE, typename Dma>
DI void p1_loop_a(f32x16 (&acc)[4], float (&z)[4], unsigned lds0, int tb, int lane, int rot, Dma&& dma) {
    const P1Addr a = p1_addr(lds0, tb, lane);
    unsigned gwa = a.gwa + rot * 128;
    P1Frag f0, f1;
    wait_vm_bar<8>();
    p1_load<MODE>(f0, a.xa[0], a.wP, a.wS, gwa, 0);
#define P1A_TAIL(NW) wait_vm_bar<NW>(); { const unsigned nx = ((it + 1) & 3) * X_SLOT, nw = ((it + 1) & 1) * W_SLOT; gwa = a.gwa + rot7(it + 1, rot) * 128; p1_load<MODE>(f0, a.xa[0] + nx, a.wP + nw, a.wS + nw, gwa, 0); } SB; p1_mma<MODE>(f1, acc, z); SB;
#pragma unroll 1
    for (int it = 0; it < 4; it++) {
        const unsigned sx = (it & 3) * X_SLOT, sw = (it & 1) * W_SLOT;
        P1_HEAD(MODE, sx, sw, gwa, dma(it + 3, 0), dma(it + 3, 1), { dma(it + 3, 2); dma(it + 3, 3); })
        P1A_TAIL(8)
    }
    { const int it = 4; P1_HEAD(MODE, (it & 3) * X_SLOT, (it & 1) * W_SLOT, gwa, {}, {}, {}) P1A_TAIL(4) }
    { const int it = 5; P1_HEAD(MODE, (it & 3) * X_SLOT, (it & 1) * W_SLOT, gwa, {}, {}, {}) P1A_TAIL(0) }
    { const int it = 6; P1_HEAD(MODE, (it & 3) * X_SLOT, (it & 1) * W_SLOT, gwa, {}, {}, {}) p1_mma<MODE>(f1, acc, z); }
}
template <int MODE, typename Ld, typename St>
DI void p1_loop_b(f32x16 (&acc)[4], unsigned lds0, int tb, int lane, Ld&& ld, St&& st) {
    const P1Addr a = p1_addr(lds0, tb, lane);
    float z[4];
    P1Frag f0, f1;
    u32x4 R[2][5];
    ld(R[0], 0); ld(R[1], 1);
    st(R[0], 0); ld(R[0], 2);
    wait_bar();
    p1_load<MODE>(f0, a.xa[0], a.wP, a.wS, 0, 0);
#pragma unroll
    for (int it = 0; it < 7; it++) {
        const unsigned sx = (it & 3) * X_SLOT, sw = (it & 1) * W_SLOT;
        P1_HEAD(MODE, sx, sw, 0, { if (it + 1 < 7) st(R[(it + 1) & 1], it + 1); }, { if (it + 3 < 7) ld(R[(it + 1) & 1], it + 3); }, {})
        if (it < 6) {
            wait_bar();
            const unsigned nx = ((it + 1) & 3) * X_SLOT, nw = ((it + 1) & 1) * W_SLOT;
            p1_load<MODE>(f0, a.xa[0] + nx, a.wP + nw, a.wS + nw, 0, 0);
        }
        SB; p1_mma<MODE>(f1, acc, z); SB;
    }
}
DI void p1_gelu_store(const Params& p, const f32x16& acc, unsigned b1a, int T, int fb, int lane) {
    f32x16 a;
#pragma unroll
    for (int q = 0; q < 4; q++) {
        u32x2 v = *(const LDS_AS u32x2*)(size_t)(b1a + q * 16);
        a[4 * q + 0] = acc[4 * q + 0] + __uint_as_float(v[0] << 16); a[4 * q + 1] = acc[4 * q + 1] + __uint_as_float(v[0] & 0xffff0000u);
        a[4 * q + 2] = acc[4 * q + 2] + __uint_as_float(v[1] << 16); a[4 * q + 3] = acc[4 * q + 3] + __uint_as_float(v[1] & 0xffff0000u);
    }
#pragma unroll
    for (int r = 0; r < 16; r++) a[r] = gelu_tanh(a[r]);
    const unsigned dst = (unsigned)WS_HF + (T * 112 + 2 * fb) * 1024 + lane * 16;
    const __amdgpu_buffer_rsrc_t r_ = ws_rsrc(p.ws);
    st_sc1(r_, dst, pack8(a, 0)); st_sc1(r_, dst + 1024, pack8(a, 1));
}

DI float max16(const f32x16& s) {
    float a = __builtin_fmaxf(__builtin_fmaxf(s[0], s[1]), s[2]), b = __builtin_fmaxf(__builtin_fmaxf(s[3], s[4]), s[5]);
#pragma unroll
    for (int r = 6; r < 14; r += 4) { a = __builtin_fmaxf(__builtin_fmaxf(a, s[r]), s[r + 1]); b = __builtin_fmaxf(__builtin_fmaxf(b, s[r + 2]), s[r + 3]); }
    a = __builtin_fmaxf(__builtin_fmaxf(a, s[14]), s[15]);
    return __builtin_fmaxf(a, b);
}
DI void set_max(bf16x8& q1, float m, int h) {
    auto rr = __builtin_amdgcn_permlane32_swap(__float_as_uint(m), __float_as_uint(m), false, false);
    const float nm = -__builtin_fmaxf(__uint_as_float(rr[0]), __uint_as_float(rr[1]));
    const unsigned a = cvtpk(nm, 0.f) & 0xffffu;
    const float r1 = nm - __uint_as_float(a << 16);
    const unsigned b = cvtpk(r1, 0.f) & 0xffffu;
    const float r2 = r1 - __uint_as_float(b << 16);
    const unsigned c = cvtpk(r2, 0.f) & 0xffffu;
    u32x4 t = __builtin_bit_cast(u32x4, q1);
    if (h == 0) { t[2] = a | (b << 16); t[3] = c; }
    q1 = __builtin_bit_cast(bf16x8, t);
}
DI bf16x8 ld_sc1s(__amdgpu_buffer_rsrc_t r, unsigned off, unsigned soff) { return __builtin_bit_cast(bf16x8, __builtin_amdgcn_raw_buffer_load_b128(r, off, soff, 16)); }
DI bf16x8 ld_sc1(__amdgpu_buffer_rsrc_t r, unsigned off) { return __builtin_bit_cast(bf16x8, __builtin_amdgcn_raw_buffer_load_b128(r, off, 0, 16)); }

DI void a_wave(const Params& p, unsigned lds0, int g, int j, int w, int lane, u64 epoch_v) {
    const int h = lane >> 5, l31 = lane & 31;
    const bool light = j >= 12;
    const int lp = j - 12;
    const int fb0 = light ? 48 + 2 * lp : 4 * j;
    const int T = 4 * g + w;
    f32x16 acc[4];
#pragma unroll
    for (int b = 0; b < 4; b++) acc[b] = (f32x16){};
    float z[4] = {0.f, 0.f, 0.f, 0.f};
    STAMP(0);
    unsigned xoff[4];
#pragma unroll
    for (int i = 0; i < 4; i++) {
        const int row = 32 * w + 8 * i + (lane >> 3);
        const int c = (lane & 7) ^ ((row >> 1) & 7);
        xoff[i] = row * 896 + c * 16;
    }
    const char* xb = (const char*)p.x + (size_t)g * 128 * 896;
    if (w == 0) glds16((lane < 56 ? lane : 55) * 16, p.gw, lds0 + P1_GW);
    if (w == 1) glds16((lane < (light ? 8 : 16) ? lane : 0) * 16, p.b1 + 32 * fb0, lds0 + P1_B1);
    const int rot = ROT1;
    auto dma = [&](int it, int i) { glds16(xoff[i], xb + rot7(it, rot) * 128, lds0 + (it & 3) * X_SLOT + (32 * w + 8 * i) * 128); };
#pragma unroll
    for (int it = 0; it < 3; it++)
#pragma unroll
        for (int i = 0; i < 4; i++) dma(it, i);
    const bool doKV = light && (w == lp);
    if (!doKV) p1_loop_a<0>(acc, z, lds0, w, lane, rot, dma); else p1_loop_a<1>(acc, z, lds0, w, lane, rot, dma);
    STAMP(1);
    const u64 tag = uniform64(epoch_v) + 1;
    const __amdgpu_buffer_rsrc_t wsr = ws_rsrc(p.ws);
    if (doKV) {
        u32x4 a0 = pack8(acc[1], 0), a1 = pack8(acc[1], 1);
        if (h == 0) { a1[2] = 0x3f803f80u; a1[3] = 0x00003f80u; }
        const unsigned dst = (unsigned)WS_KV + T * 4096 + lane * 16;
        st_sc1(wsr, dst, a0); st_sc1(wsr, dst + 1024, a1);
        a0 = pack8(acc[2], 0); a1 = pack8(acc[2], 1);
        if (l31 == 24) { a0 = (u32x4){0x3f803f80u, 0x3f803f80u, 0x3f803f80u, 0x3f803f80u}; a1 = a0; }
        else if (l31 > 24 && l31 < 28) { a0 = (u32x4){0, 0, 0, 0}; a1 = a0; }
        st_sc1(wsr, dst + 2048, a0); st_sc1(wsr, dst + 3072, a1);
        STAMP(9);
        asm volatile("s_waitcnt vmcnt(0)" ::: "memory");
        STAMP(10);
        if (lane == 0) flag_set(p.ws, WS_FKV + T * 8, tag);
    }
    float zz = (z[0] + z[1]) + (z[2] + z[3]);
    zz += __shfl_xor(zz, 32);
    const float gtok = __builtin_amdgcn_rcpf(1.0f + EX2(-1.4426950408889634f * zz));
    if (h == 0) *(LDS_AS float*)(size_t)(lds0 + L_G + (32 * w + l31) * 4) = gtok;
    bf16x8 q0[4], q1[4];
    bf16x8 qa, qb;
    {
        const float qs = 1.4426950408889634f * 0.1889822365046136f;
        f32x16 q = acc[0] * qs;
        u32x4 a0 = pack8(q, 0), a1 = pack8(q, 1);
        if (h == 0) { a1[2] = 0; a1[3] = 0; }
        qa = __builtin_bit_cast(bf16x8, a0); qb = __builtin_bit_cast(bf16x8, a1);
    }
    wait_bar();
    STAMP(2);
    const f32x16 zero = {};
    const int t0 = (4 * g + 4 * j + 16 * w) & 63;
    flags_wait(p.ws, WS_FKV + ((t0 + lane) & 63) * 8, lane < 16, tag);
    STAMP(3);
    bf16x8 K0[4], K1[4], V0[4], V1[4];
    const unsigned kvb = (unsigned)WS_KV + lane * 16;
#define KVLOAD(set, vo) do { const unsigned o_ = (vo); K0[set] = ld_sc1(wsr, o_); K1[set] = ld_sc1(wsr, o_ + 1024); V0[set] = ld_sc1(wsr, o_ + 2048); V1[set] = ld_sc1(wsr, o_ + 3072); } while (0)
    KVLOAD(0, kvb + t0 * 4096); KVLOAD(1, kvb + (t0 + 1) * 4096); KVLOAD(2, kvb + (t0 + 2) * 4096);
    V1[3] = (bf16x8){}; K0[3] = V1[3]; K1[3] = V1[3]; V0[3] = V1[3];
    f32x16 S = MFMA(K0[0], qa, zero); S = MFMA(K1[0], qb, S);
    set_max(qb, max16(S), h);
    const unsigned lo = lane * 16;
    *(LDS_AS bf16x8*)(size_t)(lds0 + L_MRG + w * 2048 + lo) = qa;
    *(LDS_AS bf16x8*)(size_t)(lds0 + L_MRG + w * 2048 + 1024 + lo) = qb;
    lds_sync4(lds0 + L_CTR + 0, 4, lane);
#pragma unroll
    for (int b = 0; b < 4; b++) { q0[b] = lds_b128(lds0 + L_MRG + b * 2048 + lo); q1[b] = lds_b128(lds0 + L_MRG + b * 2048 + 1024 + lo); }
    asm volatile("s_waitcnt lgkmcnt(0)" ::: "memory");
    if (lane == 0) __hip_atomic_fetch_add((LDS_AS unsigned*)(size_t)(lds0 + L_CTR + 4), 1u, __ATOMIC_RELAXED, __HIP_MEMORY_SCOPE_WORKGROUP);
    STAMP(4);
    S = MFMA(K0[0], q0[0], zero); S = MFMA(K1[0], q1[0], S);
    f32x16 O[4];
#pragma unroll
    for (int b = 0; b < 4; b++) O[b] = zero;
    u32x4 P1 = {0, 0, 0, 0};
#define ASTEP(c, b) do { \
        constexpr int pc = (b) == 0 ? ((c) + 3) & 3 : (c), pb = ((b) + 3) & 3, nc = (b) == 3 ? ((c) + 1) & 3 : (c), nb = ((b) + 1) & 3; \
        O[pb] = MFMA(V1[pc], __builtin_bit_cast(bf16x8, P1), O[pb]); SB; \
        u32x4 p0, p1; \
        p0[0] = cvtpk(EX2(S[0]), EX2(S[1])); p0[1] = cvtpk(EX2(S[2]), EX2(S[3])); SB; \
        f32x16 Sn = MFMA(K0[nc], q0[nb], zero); SB; \
        if ((b) == 1) { KVLOAD(pc3(c), vnext); } \
        p0[2] = cvtpk(EX2(S[4]), EX2(S[5])); p0[3] = cvtpk(EX2(S[6]), EX2(S[7])); SB; \
        Sn = MFMA(K1[nc], q1[nb], Sn); SB; \
        p1[0] = cvtpk(EX2(S[8]), EX2(S[9])); p1[1] = cvtpk(EX2(S[10]), EX2(S[11])); SB; \
        O[b] = MFMA(V0[c], __builtin_bit_cast(bf16x8, p0), O[b]); SB; \
        p1[2] = cvtpk(EX2(S[12]), EX2(S[13])); p1[3] = cvtpk(EX2(S[14]), EX2(S[15])); SB; \
        P1 = p1; S = Sn; } while (0)
#define pc3(c) (((c) + 3) & 3)
#define ATILE(c) do { \
        const int tn = tt + (c) + 3;     \
        const unsigned vnext = lo + (tn < 16 ? (unsigned)WS_KV + ((t0 + tn) & 63) * 4096u : 0x7f000000u); \
        ASTEP(c, 0); ASTEP(c, 1); ASTEP(c, 2); ASTEP(c, 3); } while (0)
#pragma unroll 1
    for (int tt = 0; tt < 16; tt += 4) {
        ATILE(0); ATILE(1); ATILE(2); ATILE(3);
        if (tt == 4) STAMP(8);
    }
    O[3] = MFMA(V1[3], __builtin_bit_cast(bf16x8, P1), O[3]);
    STAMP(5);
    {
#pragma unroll 1
        for (int spin = 0; spin < SPIN_CAP; spin++) {
            if ((unsigned)__builtin_amdgcn_readfirstlane(lds_ld(lds0 + L_CTR + 4)) >= 4u) break;
            __builtin_amdgcn_s_sleep(1);
        }
        asm volatile("" ::: "memory");
    }
#pragma unroll
    for (int b = 0; b < 4; b++) {
        if (b != w) {
            const unsigned a = lds0 + L_MRG + (b * 3 + ((w - b - 1) & 3)) * 4096 + lo;
#pragma unroll
            for (int rg = 0; rg < 4; rg++) {
                f32x4 v = {O[b][4 * rg], O[b][4 * rg + 1], O[b][4 * rg + 2], O[b][4 * rg + 3]};
                *(LDS_AS f32x4*)(size_t)(a + rg * 1024) = v;
            }
        }
    }
    f32x16 o = zero;
#pragma unroll
    for (int b = 0; b < 4; b++) if (b == w) { o = O[b]; qa = q0[b]; qb = q1[b]; }
    lds_sync4(lds0 + L_CTR + 8, 4, lane);
#pragma unroll
    for (int u = 0; u < 3; u++) {
        const unsigned a = lds0 + L_MRG + (w * 3 + u) * 4096 + lo;
#pragma unroll
        for (int rg = 0; rg < 4; rg++) {
            f32x4 v = *(const LDS_AS f32x4*)(size_t)(a + rg * 1024);
            o[4 * rg] += v[0]; o[4 * rg + 1] += v[1]; o[4 * rg + 2] += v[2]; o[4 * rg + 3] += v[3];
        }
    }
    STAMP(6);
    auto rr = __builtin_amdgcn_permlane32_swap(__float_as_uint(o[12]), __float_as_uint(o[12]), false, false);
    float l = __uint_as_float(rr[0]);
    {
        bool ok = l < 1.152921504606846976e18f && l > 8.673617379884035e-19f;
#pragma unroll
        for (int r = 0; r < 16; r++) ok = ok && (__builtin_fabsf(o[r]) < 3.0e38f);
        if (__builtin_amdgcn_ballot_w64(!ok) != 0) {
            u32x4 tq = __builtin_bit_cast(u32x4, qb);
            if (h == 0) { tq[2] = 0; tq[3] = 0; }
            qb = __builtin_bit_cast(bf16x8, tq);
            float m = -INFINITY;
#pragma unroll 1
            for (int t = 0; t < 64; t++) {
                bf16x8 k0 = ld_sc1(wsr, kvb + t * 4096), k1 = ld_sc1(wsr, kvb + t * 4096 + 1024);
                f32x16 s = MFMA(k0, qa, zero); s = MFMA(k1, qb, s);
                m = __builtin_fmaxf(m, max16(s));
            }
            set_max(qb, m, h);
            o = zero;
#pragma unroll 1
            for (int t = 0; t < 64; t++) {
                bf16x8 k0 = ld_sc1(wsr, kvb + t * 4096), k1 = ld_sc1(wsr, kvb + t * 4096 + 1024);
                bf16x8 v0 = ld_sc1(wsr, kvb + t * 4096 + 2048), v1 = ld_sc1(wsr, kvb + t * 4096 + 3072);
                f32x16 s = MFMA(k0, qa, zero); s = MFMA(k1, qb, s);
#pragma unroll
                for (int r = 0; r < 16; r++) s[r] = EX2(s[r]);
                o = MFMA(v0, __builtin_bit_cast(bf16x8, pack8(s, 0)), o); o = MFMA(v1, __builtin_bit_cast(bf16x8, pack8(s, 1)), o);
            }
            auto r2 = __builtin_amdgcn_permlane32_swap(__float_as_uint(o[12]), __float_as_uint(o[12]), false, false);
            l = __uint_as_float(r2[0]);
        }
    }
    const float sc = gtok * __builtin_amdgcn_rcpf(l);
#pragma unroll
    for (int r = 0; r < 16; r++) o[r] *= sc;
    u32x4 o0 = pack8(o, 0), o1 = pack8(o, 1);
    if (h == 0) { o1[2] = 0; o1[3] = 0; }
    const unsigned dst = (unsigned)WS_OF + (T * 32 + 2 * j) * 1024 + lo;
    st_sc1(wsr, dst, o0); st_sc1(wsr, dst + 1024, o1);
    asm volatile("s_waitcnt vmcnt(0)" ::: "memory");
    if (lane == 0) flag_set(p.ws, WS_FO + (T * 16 + j) * 8, tag);
    STAMP(7);
}

DI void b_wave(const Params& p, unsigned lds0, int g, int j, int wb, int lane, u64 epoch_v) {
    const int h = lane >> 5, l31 = lane & 31;
    const bool light = j >= 12;
    const int lp = j - 12;
    const int fbA = 4 * j, fbB = light ? 48 + 2 * lp : 4 * j + 2, fb0 = light ? fbB : fbA;
    const unsigned lo = lane * 16;
    f32x16 acc[4];
#pragma unroll
    for (int b = 0; b < 4; b++) acc[b] = (f32x16){};
    STAMP(0);
    __builtin_amdgcn_s_setprio(B_PRIO);
    {
        const int tb = wb;
        const int r16 = 16 * tb + (lane >> 2), c = lane & 3;
        const unsigned c28 = c == 3 ? 40 : c * 16;
        const unsigned qoff = r16 * 896 + c28 + j * 56;
        const unsigned poff = (16 * tb + ((lane & 31) >> 2)) * 3584 + (lane >> 5) * 64 + c * 16;
        const unsigned kvoff = r16 * 56 + c28;
        const __amdgpu_buffer_rsrc_t rq = __builtin_amdgcn_make_buffer_rsrc((void*)p.wq, 0, 448 * 896, 0x00020000);
        const __amdgpu_buffer_rsrc_t rw1 = __builtin_amdgcn_make_buffer_rsrc((void*)p.w1, 0, 448 * 3584, 0x00020000);
        const __amdgpu_buffer_rsrc_t r1a = light ? __builtin_amdgcn_make_buffer_rsrc((void*)p.wk, 0, 448 * 56, 0x00020000) : rw1;
        const __amdgpu_buffer_rsrc_t r1b = light ? __builtin_amdgcn_make_buffer_rsrc((void*)p.wv, 0, 448 * 56, 0x00020000) : rw1;
        const unsigned r1off0 = light ? kvoff : poff + fbA * 64, r1off1 = light ? kvoff : poff + fbA * 64 + 8 * 3584;
        const unsigned r1step = light ? 64 * 56 : 64 * 3584;
        const unsigned r1d0 = light ? tb * 1024 : tb * 2048, r1d1 = light ? 4096 + tb * 1024 : tb * 2048 + 1024;
        const unsigned r2off = poff + fbB * 64;
        const int rot = ROT1;
        auto ld = [&](u32x4 (&R)[5], int it0) {
            const int it = rot7(it0, rot);
            R[0] = __builtin_amdgcn_raw_buffer_load_b128(rq, qoff, it * (64 * 896), 0);
            R[1] = __builtin_amdgcn_raw_buffer_load_b128(r1a, r1off0, it * r1step, 0);
            R[2] = __builtin_amdgcn_raw_buffer_load_b128(r1b, r1off1, it * r1step, 0);
            R[3] = __builtin_amdgcn_raw_buffer_load_b128(rw1, r2off, it * (64 * 3584), 0);
            R[4] = __builtin_amdgcn_raw_buffer_load_b128(rw1, r2off + 8 * 3584, it * (64 * 3584), 0);
        };
        auto st = [&](const u32x4 (&R)[5], int it) {
            const unsigned sl = lds0 + W_0 + (it & 1) * W_SLOT + lo;
            *(LDS_AS u32x4*)(size_t)(sl + W_Q + tb * 1024) = R[0];
            *(LDS_AS u32x4*)(size_t)(sl + W_R1 + r1d0) = R[1];
            *(LDS_AS u32x4*)(size_t)(sl + W_R1 + r1d1) = R[2];
            *(LDS_AS u32x4*)(size_t)(sl + W_R2 + tb * 2048) = R[3];
            *(LDS_AS u32x4*)(size_t)(sl + W_R2 + tb * 2048 + 1024) = R[4];
        };
        if (!light) p1_loop_b<2>(acc, lds0, tb, lane, ld, st); else p1_loop_b<3>(acc, lds0, tb, lane, ld, st);
    }
    STAMP(1);
    const u64 tag = uniform64(epoch_v) + 1;
    wait_bar();
    STAMP(2);
    const bool tile = j < 14;
    const unsigned stg = lds0 + L_BST + wb * B_AREA;
    const int th = j & 1, cs = j >> 1, n0 = 64 * cs, T0 = 4 * g + 2 * th;
    const unsigned woff = ((lane & 31) >> 2) * 896 + (lane >> 5) * 64 + (lane & 3) * 16;
    const __amdgpu_buffer_rsrc_t rw2 = __builtin_amdgcn_make_buffer_rsrc((void*)p.w2, 0, 1792 * 896, 0x00020000);
    const __amdgpu_buffer_rsrc_t wsr = ws_rsrc(p.ws);
    const unsigned w2v = woff + n0 * 2, w2s = 28 * wb * 16 * 896;
    constexpr int NB = 7;
    u32x4 Wr[NB][2];
    const int rot2 = ROT2;
#define ROT28(st_) (((st_) + rot2 >= 28) ? (st_) + rot2 - 28 : (st_) + rot2)
#define WLOAD(st_) do { const unsigned so_ = w2s + ROT28(st_) * (16 * 896); Wr[(st_) % NB][0] = __builtin_amdgcn_raw_buffer_load_b128(rw2, w2v, so_, 0); \
        Wr[(st_) % NB][1] = __builtin_amdgcn_raw_buffer_load_b128(rw2, w2v + 8 * 896, so_, 0); } while (0)
    STAMP(10);
    {
        const int T = 4 * g + wb;
        const unsigned bo = lds0 + P1_B1 + (fbB - fb0) * 64 + h * 8;
        p1_gelu_store(p, acc[0], bo, T, fbB, lane);
        p1_gelu_store(p, acc[1], bo + 64, T, fbB + 1, lane);
        STAMP(11);
        if (!light) {
            p1_gelu_store(p, acc[2], lds0 + P1_B1 + h * 8, T, fbA, lane);
            p1_gelu_store(p, acc[3], lds0 + P1_B1 + 64 + h * 8, T, fbA + 1, lane);
        }
        STAMP(12);
        asm volatile("s_waitcnt vmcnt(0)" ::: "memory");
        STAMP(13);
        if (lane == 0) flag_set(p.ws, WS_FH + (T * 16 + j) * 8, tag);
    }
    STAMP(3);
    if (!tile) {
        if (g == 0 && j == 15 && wb == 0) {
#pragma unroll 1
            for (int spin = 0; spin < SPIN_CAP; spin++) {
                bool bad = false;
#pragma unroll
                for (int i = 0; i < 16; i++) bad = bad || (__hip_atomic_load((const u64*)(p.ws + WS_FH + (i * 64 + lane) * 8), __ATOMIC_RELAXED, __HIP_MEMORY_SCOPE_AGENT) != tag);
                if (__builtin_amdgcn_ballot_w64(bad) == 0) break;
                __builtin_amdgcn_s_sleep(8);
            }
            if (lane == 0) __hip_atomic_store((u64*)(p.ws + WS_EPOCH), tag, __ATOMIC_RELAXED, __HIP_MEMORY_SCOPE_AGENT);
        }
        return;
    }
#pragma unroll
    for (int st = 0; st < NB; st++) WLOAD(st);
    const int Tm = T0 + (wb & 1);
    const float gm = *(const LDS_AS float*)(size_t)(lds0 + L_G + (64 * th + 32 * (wb & 1) + l31) * 4);
    const int nc = n0 + 32 * (wb >> 1) + 4 * h;
    u32x2 b2v[4];
#pragma unroll
    for (int rg = 0; rg < 4; rg++) b2v[rg] = *(const u32x2*)(p.b2 + nc + 8 * rg);
    flags_wait(p.ws, WS_FH + (T0 * 16 + (lane & 31)) * 8, true, tag);
    STAMP(4);
#pragma unroll
    for (int i = 0; i < 4; i++) acc[i] = (f32x16){};
    {
        const unsigned wrd = stg + (4 * h + ((lane & 15) >> 2)) * 64 + ((lane >> 4) & 1) * 32 + (lane & 3) * 8;
        const unsigned hv = (unsigned)WS_HF + (T0 * 112 + 28 * wb) * 1024 + lo;
        bf16x8 H0[NB], H1[NB], wf0[2], wf1[2];
#define HLOAD(st_) do { const unsigned so_ = ROT28(st_) * 1024; H0[(st_) % NB] = ld_sc1s(wsr, hv, so_); H1[(st_) % NB] = ld_sc1s(wsr, hv + 112 * 1024, so_); } while (0)
#define WWRITE(st_) do { *(LDS_AS u32x4*)(size_t)(stg + ((st_) % 3) * 2048 + lo) = Wr[(st_) % NB][0]; *(LDS_AS u32x4*)(size_t)(stg + ((st_) % 3) * 2048 + 1024 + lo) = Wr[(st_) % NB][1]; } while (0)
#define WREAD(st_) do { wf0[(st_) & 1] = lds_tr2(wrd + ((st_) % 3) * 2048, wrd + ((st_) % 3) * 2048 + 1024); wf1[(st_) & 1] = lds_tr2(wrd + ((st_) % 3) * 2048 + 512, wrd + ((st_) % 3) * 2048 + 1536); } while (0)
#pragma unroll
        for (int st = 0; st < NB; st++) HLOAD(st);
        WWRITE(0); WLOAD(NB); WWRITE(1); WLOAD(NB + 1);
        WREAD(0);
#pragma unroll
        for (int st = 0; st < 28; st++) {
            SB;
            if (st + 2 < 28) WWRITE(st + 2);
            if (st + 1 < 28) WREAD(st + 1);
            if (st + 2 + NB < 28) WLOAD(st + 2 + NB);
            SB;
            acc[0] = MFMA(wf0[st & 1], H0[st % NB], acc[0]); acc[1] = MFMA(wf0[st & 1], H1[st % NB], acc[1]);
            acc[2] = MFMA(wf1[st & 1], H0[st % NB], acc[2]); acc[3] = MFMA(wf1[st & 1], H1[st % NB], acc[3]);
            SB;
            if (st + NB < 28) HLOAD(st + NB);
        }
    }
    STAMP(5);
    {
        const char* wos = (const char*)p.wo + n0 * 2;
#pragma unroll
        for (int i = 0; i < 14; i++) glds16(woff, wos + (size_t)(14 * wb + i) * (8 * 896), lds0 + L_WO + (14 * wb + i) * 1024);
    }
    {
        const float g0 = *(const LDS_AS float*)(size_t)(lds0 + L_G + (64 * th + l31) * 4), g1 = *(const LDS_AS float*)(size_t)(lds0 + L_G + (64 * th + 32 + l31) * 4);
        const float f0 = 1.0f - g0, f1 = 1.0f - g1;
        acc[0] *= f0; acc[2] *= f0; acc[1] *= f1; acc[3] *= f1;
    }
    flags_wait(p.ws, WS_FO + ((T0 + ((lane >> 2) & 1)) * 16 + 4 * wb + (lane & 3)) * 8, true, tag);
    STAMP(7);
    {
        const unsigned ob = (unsigned)WS_OF + (T0 * 32 + 8 * wb) * 1024 + lo;
        bf16x8 o0[8], o1[8];
#pragma unroll
        for (int st = 0; st < 8; st++) { o0[st] = ld_sc1(wsr, ob + st * 1024); o1[st] = ld_sc1(wsr, ob + st * 1024 + 32768); }
        const unsigned lp16 = lds0 + L_WO + 14 * wb * 1024 + ((lane & 15) >> 2) * 64 + ((lane >> 4) & 1) * 32 + (lane & 3) * 8;
        const unsigned wA = lp16 + h * 256, wB = lp16 + h * 768;
#pragma unroll
        for (int st = 0; st < 8; st++) {
            const int hd = st >> 1, s = st & 1;
            const int r1 = 28 * hd + 16 * s, r2 = 28 * hd + (s ? 20 : 8);
            const unsigned a1 = ((r1 & 4) ? wB : wA) + (r1 >> 3) * 1024 + (r1 & 7) * 64, a2 = ((r2 & 4) ? wB : wA) + (r2 >> 3) * 1024 + (r2 & 7) * 64;
            const bf16x8 w0 = lds_tr2(a1, a2), w1 = lds_tr2(a1 + 512, a2 + 512);
            acc[0] = MFMA(w0, o0[st], acc[0]); acc[1] = MFMA(w0, o1[st], acc[1]); acc[2] = MFMA(w1, o0[st], acc[2]); acc[3] = MFMA(w1, o1[st], acc[3]);
        }
    }
    STAMP(8);
    asm volatile("s_waitcnt lgkmcnt(0)" ::: "memory");
#pragma unroll
    for (int i = 0; i < 4; i++) {
        if (i != wb) {
            const unsigned a = stg + ((i - wb - 1) & 3) * 4096 + lo;
#pragma unroll
            for (int rg = 0; rg < 4; rg++) {
                f32x4 v = {acc[i][4 * rg], acc[i][4 * rg + 1], acc[i][4 * rg + 2], acc[i][4 * rg + 3]};
                *(LDS_AS f32x4*)(size_t)(a + rg * 1024) = v;
            }
        }
    }
    f32x16 o = {};
#pragma unroll
    for (int i = 0; i < 4; i++) if (i == wb) o = acc[i];
    lds_sync4(lds0 + L_CTR + 12, 4, lane);
#pragma unroll
    for (int u = 0; u < 4; u++) {
        if (u != wb) {
            const unsigned a = lds0 + L_BST + u * B_AREA + ((wb - u - 1) & 3) * 4096 + lo;
#pragma unroll
            for (int rg = 0; rg < 4; rg++) {
                f32x4 v = *(const LDS_AS f32x4*)(size_t)(a + rg * 1024);
                o[4 * rg] += v[0]; o[4 * rg + 1] += v[1]; o[4 * rg + 2] += v[2]; o[4 * rg + 3] += v[3];
            }
        }
    }
    {
        const float fg = 1.0f - gm;
        unsigned short* orow = p.out + (size_t)(Tm * 32 + l31) * D_ + nc;
#pragma unroll
        for (int rg = 0; rg < 4; rg++) {
            const u32x2 bv = b2v[rg];
            float v0 = o[4 * rg] + fg * __uint_as_float(bv[0] << 16), v1 = o[4 * rg + 1] + fg * __uint_as_float(bv[0] & 0xffff0000u);
            float v2 = o[4 * rg + 2] + fg * __uint_as_float(bv[1] << 16), v3 = o[4 * rg + 3] + fg * __uint_as_float(bv[1] & 0xffff0000u);
            u32x2 ov = {cvtpk(v0, v1), cvtpk(v2, v3)};
            *(u32x2*)(orow + 8 * rg) = ov;
        }
    }
    STAMP(9);
}

__global__ void __launch_bounds__(512, 2) gated_mix_fused(Params p) {
    extern __shared__ char smem[];
    const unsigned lds0 = (unsigned)(size_t)smem;
    const int b = blockIdx.x, xcd = b & 7, idx = b >> 3;
    const int g = xcd * 2 + (idx >> 4), j = idx & 15;
    const int w = __builtin_amdgcn_readfirstlane(threadIdx.x >> 6), lane = threadIdx.x & 63;
    const u64 epoch = __hip_atomic_load((const u64*)(p.ws + WS_EPOCH), __ATOMIC_RELAXED, __HIP_MEMORY_SCOPE_AGENT);
    if (w == 0 && lane < 8) *(LDS_AS unsigned*)(size_t)(lds0 + L_CTR + 4 * lane) = 0;
    if (w < 4) a_wave(p, lds0, g, j, w, lane, epoch);
    else b_wave(p, lds0, g, j, w - 4, lane, epoch);
}

extern "C" void kernel_launch(void* const* d_in, const int* in_sizes, int n_in, void* d_out, int out_size, void* d_ws, size_t ws_size, hipStream_t stream) {
    Params p;
    p.x = (const unsigned short*)d_in[0]; p.wq = (const unsigned short*)d_in[1]; p.wk = (const unsigned short*)d_in[2];
    p.wv = (const unsigned short*)d_in[3]; p.wo = (const unsigned short*)d_in[4]; p.w1 = (const unsigned short*)d_in[6];
    p.b1 = (const unsigned short*)d_in[7]; p.w2 = (const unsigned short*)d_in[8]; p.b2 = (const unsigned short*)d_in[9];
    p.gw = (const unsigned short*)d_in[10]; p.out = (unsigned short*)d_out; p.ws = (char*)d_ws;
    (void)hipFuncSetAttribute((const void*)gated_mix_fused, hipFuncAttributeMaxDynamicSharedMemorySize, LDS_BYTES);
    gated_mix_fused<<<256, 512, LDS_BYTES, stream>>>(p);
}
```

```cpp
#include <hip/hip_runtime.h>
#include <hip/hip_bf16.h>

typedef short bf16x8 __attribute__((ext_vector_type(8)));
typedef short s16x4 __attribute__((ext_vector_type(4)));
typedef __bf16 bf16x2 __attribute__((ext_vector_type(2)));
typedef float f32x16 __attribute__((ext_vector_type(16)));
typedef float f32x4 __attribute__((ext_vector_type(4)));
typedef float f32x2 __attribute__((ext_vector_type(2)));
typedef unsigned u32x4 __attribute__((ext_vector_type(4)));
typedef unsigned u32x2 __attribute__((ext_vector_type(2)));
typedef short v4i16_t __attribute__((ext_vector_type(4)));
typedef unsigned long long u64;
#define LDS_AS __attribute__((address_space(3)))
#define DI __device__ __forceinline__

constexpr int S_ = 2048, D_ = 448, F_ = 1792;
constexpr size_t WS_EPOCH = 0;
constexpr size_t WS_FKV = 65536, FKV_COPY = 1280;
constexpr size_t WS_FH = 163840;
constexpr size_t WS_FO = 294912;
constexpr size_t WS_KV = 1u << 20;
constexpr size_t WS_OF = 2u << 20;
constexpr size_t WS_HF = 4u << 20;

#define STAMP(i) do {} while (0)
#define MFMA(a, b, c) __builtin_amdgcn_mfma_f32_32x32x16_bf16(a, b, c, 0, 0, 0)
#define SB __builtin_amdgcn_sched_barrier(0)
#define EX2(x) __builtin_amdgcn_exp2f(x)
constexpr int SPIN_CAP = 1 << 21;
#define POLL_SLEEP 2
#define G_PRIO 3
#define F_PRIO 0
#define KV_PRIO 3
#define A1_PRIO 2
#define A2_PRIO 0
#define B_PRIO 2

DI unsigned cvtpk(float lo, float hi) { f32x2 v = {lo, hi}; return __builtin_bit_cast(unsigned, __builtin_convertvector(v, bf16x2)); }
DI void glds16(unsigned voff, const void* sbase, unsigned lds_dst) {
    unsigned keep;
    asm volatile("s_mov_b32 %0, m0\n\ts_mov_b32 m0, %3\n\ts_nop 0\n\tglobal_load_lds_dwordx4 %1, %2\n\ts_mov_b32 m0, %0"
                 : "=&s"(keep) : "v"(voff), "s"(sbase), "s"(lds_dst) : "memory");
}
DI void glds16_sc1(unsigned voff, const void* sbase, unsigned lds_dst) {
    unsigned keep;
    asm volatile("s_mov_b32 %0, m0\n\ts_mov_b32 m0, %3\n\ts_nop 0\n\tglobal_load_lds_dwordx4 %1, %2 sc1\n\ts_mov_b32 m0, %0"
                 : "=&s"(keep) : "v"(voff), "s"(sbase), "s"(lds_dst) : "memory");
}
DI bf16x8 lds_b128(unsigned a) { return *(const LDS_AS bf16x8*)(size_t)a; }
DI s16x4 lds_tr(unsigned a) { return __builtin_bit_cast(s16x4, __builtin_amdgcn_ds_read_tr16_b64_v4i16((LDS_AS v4i16_t*)(size_t)a)); }
DI bf16x8 lds_tr2(unsigned a0, unsigned a1) { s16x4 lo = lds_tr(a0), hi = lds_tr(a1); return __builtin_shufflevector(lo, hi, 0, 1, 2, 3, 4, 5, 6, 7); }
DI float dot2(unsigned a, unsigned b, float c) { return __builtin_amdgcn_fdot2_f32_bf16(__builtin_bit_cast(bf16x2, a), __builtin_bit_cast(bf16x2, b), c, false); }
DI u32x4 pack8(const f32x16& a, int s) {
    u32x4 r;
    r[0] = cvtpk(a[8 * s + 0], a[8 * s + 1]); r[1] = cvtpk(a[8 * s + 2], a[8 * s + 3]);
    r[2] = cvtpk(a[8 * s + 4], a[8 * s + 5]); r[3] = cvtpk(a[8 * s + 6], a[8 * s + 7]);
    return r;
}
DI float gelu_tanh(float x) {
    const float c1 = -2.0f * 1.4426950408889634f * 0.7978845608028654f, c2 = c1 * 0.044715f;
    float t = x * x;
    float e = EX2(x * __builtin_fmaf(t, c2, c1));
    return x * __builtin_amdgcn_rcpf(1.0f + e);
}
DI __amdgpu_buffer_rsrc_t ws_rsrc(const char* ws) { return __builtin_amdgcn_make_buffer_rsrc((void*)ws, 0, 16u << 20, 0x00020000); }
DI void st_sc1(__amdgpu_buffer_rsrc_t r, unsigned off, u32x4 v) { __builtin_amdgcn_raw_buffer_store_b128(v, r, off, 0, 16); }
DI void flag_set(char* ws, size_t off, u64 tag) { __hip_atomic_store((u64*)(ws + off), tag, __ATOMIC_RELAXED, __HIP_MEMORY_SCOPE_AGENT); }
DI void flags_wait(const char* ws, unsigned off, bool on, u64 tag) {
#pragma unroll 1
    for (int spin = 0; spin < SPIN_CAP; spin++) {
        u64 v = tag;
        if (on) v = __hip_atomic_load((const u64*)(ws + off), __ATOMIC_RELAXED, __HIP_MEMORY_SCOPE_AGENT);
        if (__builtin_amdgcn_ballot_w64(v != tag) == 0) break;
        __builtin_amdgcn_s_sleep(POLL_SLEEP);
    }
    asm volatile("s_waitcnt vmcnt(0)" ::: "memory");
}
DI u64 uniform64(u64 v) {
    asm volatile("" : "+v"(v) :: "memory");
    const unsigned lo = __builtin_amdgcn_readfirstlane((unsigned)v), hi = __builtin_amdgcn_readfirstlane((unsigned)(v >> 32));
    return ((u64)hi << 32) | lo;
}
DI unsigned lds_ld(unsigned a) { return __hip_atomic_load((LDS_AS unsigned*)(size_t)a, __ATOMIC_RELAXED, __HIP_MEMORY_SCOPE_WORKGROUP); }
DI void lds_st(unsigned a, unsigned v) { __hip_atomic_store((LDS_AS unsigned*)(size_t)a, v, __ATOMIC_RELAXED, __HIP_MEMORY_SCOPE_WORKGROUP); }
DI void lds_sync4(unsigned ctr, unsigned target, int lane) {
    asm volatile("s_waitcnt lgkmcnt(0)" ::: "memory");
    LDS_AS unsigned* c = (LDS_AS unsigned*)(size_t)ctr;
    if (lane == 0) __hip_atomic_fetch_add(c, 1u, __ATOMIC_RELAXED, __HIP_MEMORY_SCOPE_WORKGROUP);
#pragma unroll 1
    for (int spin = 0; spin < SPIN_CAP; spin++) {
        unsigned v = __builtin_amdgcn_readfirstlane(__hip_atomic_load(c, __ATOMIC_RELAXED, __HIP_MEMORY_SCOPE_WORKGROUP));
        if (v >= target) break;
        __builtin_amdgcn_s_sleep(1);
    }
    asm volatile("" ::: "memory");
}

struct Params {
    const unsigned short *x, *wq, *wk, *wv, *wo, *w1, *b1, *w2, *b2, *gw;
    unsigned short* out;
    char* ws;
};

constexpr unsigned X_SLOT = 16384, L_WQ = 114688, L_RING = 143360, L_QX = L_WQ;
constexpr unsigned L_WO = 0, WO_SLICE = 14336, L_MRG = 57344, L_RED = 106496, B_AREA = 12288;
constexpr unsigned P1_GW = 159744, L_G = P1_GW + 1024;
constexpr unsigned L_CTR = L_G + 512;
constexpr unsigned C_QW = 0, C_QR = 4, C_OW = 8, C_RED = 12, C_AP = 16, C_B1 = 20, L_XF = L_CTR + 32, L_XE = L_CTR + 48;
constexpr unsigned LDS_BYTES = L_CTR + 64;
static_assert(L_RING + 4 * 4096 <= P1_GW && L_RED + 4 * B_AREA <= P1_GW && L_MRG + 12 * 4096 <= L_RED && LDS_BYTES <= 163840 && (L_XF & 15) == 0, "LDS map");

template <int N> DI void wait_vm() { asm volatile("s_waitcnt vmcnt(%0)" ::"n"(N) : "memory"); }
DI void wait_bar() { asm volatile("s_waitcnt lgkmcnt(0)\n\ts_barrier" ::: "memory"); }
DI u32x4 lds_flags(unsigned a) { u32x4 v; asm volatile("ds_read_b128 %0, %1" : "=v"(v) : "v"(a) : "memory"); return v; }
DI void lds_wait_ge(unsigned a, unsigned need) {
#pragma unroll 1
    for (int spin = 0; spin < SPIN_CAP; spin++) {
        if ((unsigned)__builtin_amdgcn_readfirstlane(lds_ld(a)) >= need) break;
        __builtin_amdgcn_s_sleep(1);
    }
    asm volatile("" ::: "memory");
}
DI void x_wait(unsigned xf, unsigned need, int only) {
#pragma unroll 1
    for (int spin = 0; spin < SPIN_CAP; spin++) {
        unsigned m;
        if (only < 0) {
            const unsigned a = lds_ld(xf), b = lds_ld(xf + 4), c = lds_ld(xf + 8), d = lds_ld(xf + 12);
            m = min(min(a, b), min(c, d));
        } else m = lds_ld(xf + 4 * only);
        if ((unsigned)__builtin_amdgcn_readfirstlane(m) >= need) break;
        __builtin_amdgcn_s_sleep(1);
    }
    asm volatile("" ::: "memory");
}
DI unsigned x_addr(unsigned lds0, int lane, int s) {
    const int h = lane >> 5, l31 = lane & 31;
    return lds0 + l31 * 128 + (((2 * s + h) ^ ((l31 >> 1) & 7)) << 4);
}
DI unsigned w_lane(int lane) { return (lane >> 5) * 512 + ((lane & 15) >> 2) * 64 + ((lane >> 4) & 1) * 32 + (lane & 3) * 8; }
#define NR1 8
template <typename Ld>
DI void proj_loop(f32x16 (&acc)[4], unsigned lds0, unsigned ring, int lane, bool light, Ld&& ld, unsigned long long* ptb) {
    constexpr int NR = NR1;
    u32x4 R[NR];
#define W1_DELAY 4
    if (light) x_wait(lds0 + L_XE, 1, -1);
    else if (W1_DELAY > 0) x_wait(lds0 + L_XF, W1_DELAY, -1);
#pragma unroll
    for (int s = 0; s < NR; s++) R[s] = ld(s);
    unsigned xa[4];
#pragma unroll
    for (int s = 0; s < 4; s++) xa[s] = x_addr(lds0, lane, s);
    const unsigned wr = ring + lane * 16, rd = ring + w_lane(lane), xf = lds0 + L_XF;
    bf16x8 xf_[3][4], wf[3];
#define PWRITE(s_) do { *(LDS_AS u32x4*)(size_t)(wr + ((s_) & 3) * 1024) = R[(s_) % NR]; if ((s_) + NR < 28) R[(s_) % NR] = ld((s_) + NR); } while (0)
#define PREADW(s_) do { wf[(s_) % 3] = lds_tr2(rd + ((s_) & 3) * 1024, rd + ((s_) & 3) * 1024 + 256); } while (0)
#define PREADX(s_, t_) do { xf_[(s_) % 3][t_] = lds_b128(xa[(s_) & 3] + ((s_) >> 2) * X_SLOT + (t_) * 4096); } while (0)
    PWRITE(0); PWRITE(1); PWRITE(2);
    x_wait(xf, 1, -1);
    PREADW(0); PREADX(0, 0); PREADX(0, 1); PREADX(0, 2); PREADX(0, 3);
    PREADW(1); PREADX(1, 0); PREADX(1, 1); PREADX(1, 2); PREADX(1, 3);
#pragma unroll
    for (int s = 0; s < 28; s++) {
        SB;
        if ((s & 3) == 2 && s + 2 < 28) x_wait(xf, (s + 2) / 4 + 1, -1);
        acc[0] = MFMA(wf[s % 3], xf_[s % 3][0], acc[0]); SB;
        if (s + 2 < 28) { PREADW(s + 2); PREADX(s + 2, 0); } SB;
        acc[1] = MFMA(wf[s % 3], xf_[s % 3][1], acc[1]); SB;
        if (s + 2 < 28) { PREADX(s + 2, 1); PREADX(s + 2, 2); } SB;
        acc[2] = MFMA(wf[s % 3], xf_[s % 3][2], acc[2]); SB;
        if (s + 2 < 28) PREADX(s + 2, 3);
        if (s + 3 < 28) PWRITE(s + 3);
        SB;
        acc[3] = MFMA(wf[s % 3], xf_[s % 3][3], acc[3]);
    }
    SB;
#undef PWRITE
#undef PREADW
#undef PREADX
}
DI void gelu_store(const Params& p, const f32x16& acc, const u32x2 (&bv)[4], int T, int fb, int lane) {
    f32x16 a;
#pragma unroll
    for (int q = 0; q < 4; q++) {
        a[4 * q + 0] = acc[4 * q + 0] + __uint_as_float(bv[q][0] << 16); a[4 * q + 1] = acc[4 * q + 1] + __uint_as_float(bv[q][0] & 0xffff0000u);
        a[4 * q + 2] = acc[4 * q + 2] + __uint_as_float(bv[q][1] << 16); a[4 * q + 3] = acc[4 * q + 3] + __uint_as_float(bv[q][1] & 0xffff0000u);
    }
#pragma unroll
    for (int r = 0; r < 16; r++) a[r] = gelu_tanh(a[r]);
    const unsigned dst = (unsigned)WS_HF + (T * 112 + 2 * fb) * 1024 + lane * 16;
    const __amdgpu_buffer_rsrc_t r_ = ws_rsrc(p.ws);
    st_sc1(r_, dst, pack8(a, 0)); st_sc1(r_, dst + 1024, pack8(a, 1));
}

DI float max16(const f32x16& s) {
    float a = __builtin_fmaxf(__builtin_fmaxf(s[0], s[1]), s[2]), b = __builtin_fmaxf(__builtin_fmaxf(s[3], s[4]), s[5]);
#pragma unroll
    for (int r = 6; r < 14; r += 4) { a = __builtin_fmaxf(__builtin_fmaxf(a, s[r]), s[r + 1]); b = __builtin_fmaxf(__builtin_fmaxf(b, s[r + 2]), s[r + 3]); }
    a = __builtin_fmaxf(__builtin_fmaxf(a, s[14]), s[15]);
    return __builtin_fmaxf(a, b);
}
DI void set_max(bf16x8& q1, float m, int h) {
    auto rr = __builtin_amdgcn_permlane32_swap(__float_as_uint(m), __float_as_uint(m), false, false);
    const float nm = -__builtin_fmaxf(__uint_as_float(rr[0]), __uint_as_float(rr[1]));
    const unsigned a = cvtpk(nm, 0.f) & 0xffffu;
    const float r1 = nm - __uint_as_float(a << 16);
    const unsigned b = cvtpk(r1, 0.f) & 0xffffu;
    const float r2 = r1 - __uint_as_float(b << 16);
    const unsigned c = cvtpk(r2, 0.f) & 0xffffu;
    u32x4 t = __builtin_bit_cast(u32x4, q1);
    if (h == 0) { t[2] = a | (b << 16); t[3] = c; }
    q1 = __builtin_bit_cast(bf16x8, t);
}
DI bf16x8 ld_sc1s(__amdgpu_buffer_rsrc_t r, unsigned off, unsigned soff) { return __builtin_bit_cast(bf16x8, __builtin_amdgcn_raw_buffer_load_b128(r, off, soff, 16)); }
DI bf16x8 ld_sc1(__amdgpu_buffer_rsrc_t r, unsigned off) { return __builtin_bit_cast(bf16x8, __builtin_amdgcn_raw_buffer_load_b128(r, off, 0, 16)); }

DI void a_wave(const Params& p, unsigned lds0, int g, int j, int w, int lane, u64 epoch_v) {
    const int h = lane >> 5, l31 = lane & 31;
    const int T = 4 * g + w;
    f32x16 accq = {};
    STAMP(0);
    __builtin_amdgcn_s_setprio(A1_PRIO);
    unsigned xoff[4];
#pragma unroll
    for (int i = 0; i < 4; i++) {
        const int row = 32 * w + 8 * i + (lane >> 3);
        const int c = (lane & 7) ^ ((row >> 1) & 7);
        xoff[i] = row * 896 + c * 16;
    }
    const char* xb = (const char*)p.x + (size_t)g * 128 * 896;
    const unsigned qoff = (16 * w + (lane >> 2)) * 896 + ((lane & 3) == 3 ? 40 : (lane & 3) * 16);
    const char* qb_ = (const char*)p.wq + j * 56;
    glds16((lane < 56 ? lane : 55) * 16, p.gw, lds0 + P1_GW);
    auto dma = [&](int c) {
#pragma unroll
        for (int i = 0; i < 4; i++) glds16(xoff[i], xb + c * 128, lds0 + c * X_SLOT + (32 * w + 8 * i) * 128);
        glds16(qoff, qb_ + c * (64 * 896), lds0 + L_WQ + c * 4096 + w * 1024);
    };
    const bool light = j >= 12;
    if (light) {
        const int row = 32 * (j - 12) + 8 * w + (lane >> 3);
        const unsigned eo = row * 896 + (((lane & 7) ^ ((row >> 1) & 7)) << 4);
#pragma unroll
        for (int c = 0; c < 7; c++) glds16(eo, xb + c * 128, lds0 + c * X_SLOT + (32 * (j - 12) + 8 * w) * 128);
    }
    if (light) {
        dma(0);
        wait_vm<5>();
        if (lane == 0) lds_st(lds0 + L_XE + 4 * w, 1);
    } else dma(0);
    dma(1); dma(2);
    u64 tag;
    const __amdgpu_buffer_rsrc_t wsr = ws_rsrc(p.ws);
    const int t0 = (4 * g + 4 * j + 16 * w) & 63;
    bf16x8 K0[4], K1[4], V0[4], V1[4];
    const unsigned kvb = (unsigned)WS_KV + lane * 16;
#define KVLOAD(set, vo) do { const unsigned o_ = (vo); K0[set] = ld_sc1(wsr, o_); K1[set] = ld_sc1(wsr, o_ + 1024); V0[set] = ld_sc1(wsr, o_ + 2048); V1[set] = ld_sc1(wsr, o_ + 3072); } while (0)
    {
        unsigned xa[4];
#pragma unroll
        for (int s = 0; s < 4; s++) xa[s] = x_addr(lds0, lane, s) + w * 4096;
        const unsigned wa = lds0 + L_WQ + w_lane(lane);
        unsigned short gwv[7];
        auto publish = [&](int c) {
            if (lane < 16) *(LDS_AS unsigned short*)(size_t)(lds0 + L_WQ + c * 4096 + w * 1024 + lane * 64 + 48) = gwv[c];
            asm volatile("s_waitcnt lgkmcnt(0)" ::: "memory");
            if (lane == 0) lds_st(lds0 + L_XF + 4 * w, c + 1);
        };
        auto qchunk = [&](int c) {
            bf16x8 xf[4], wf[4];
#pragma unroll
            for (int s = 0; s < 4; s++) { xf[s] = lds_b128(xa[s] + c * X_SLOT); wf[s] = lds_tr2(wa + c * 4096 + s * 1024, wa + c * 4096 + s * 1024 + 256); }
#pragma unroll
            for (int s = 0; s < 4; s++) accq = MFMA(wf[s], xf[s], accq);
        };
#define TC(c)
        wait_vm<10>(); TC(0);
#pragma unroll
        for (int c = 0; c < 7; c++) gwv[c] = *(const LDS_AS unsigned short*)(size_t)(lds0 + P1_GW + (64 * c + 16 * w + (lane & 15)) * 2);
        publish(0); dma(3);
        wait_vm<10>(); TC(1); publish(1); dma(4);
        wait_vm<10>(); TC(2); publish(2); dma(5);
        wait_vm<10>(); TC(3); publish(3); dma(6);
        wait_vm<10>(); TC(4); publish(4);
        x_wait(lds0 + L_XF, 2, -1); qchunk(0); qchunk(1); SB;
        wait_vm<5>(); TC(5); publish(5);
        x_wait(lds0 + L_XF, 4, -1); qchunk(2); qchunk(3); SB;
        wait_vm<0>(); TC(6); publish(6);
        tag = uniform64(epoch_v) + 1;
        const unsigned fo = WS_FKV + (g * 4 + (j >> 2)) * FKV_COPY + ((t0 + (lane >> 1)) & 63) * 16 + (lane & 1) * 8;
        u64 fv = tag;
        if (lane < 32) fv = __hip_atomic_load((const u64*)(p.ws + fo), __ATOMIC_RELAXED, __HIP_MEMORY_SCOPE_AGENT);
        x_wait(lds0 + L_XF, 6, -1); TC(8); qchunk(4); qchunk(5); SB; TC(9);
        x_wait(lds0 + L_XF, 7, -1); TC(11); qchunk(6); SB; TC(12);
        asm volatile("" : "+v"(fv) :: "memory");
        if (__builtin_amdgcn_ballot_w64(fv != tag) != 0) flags_wait(p.ws, fo, lane < 32, tag);
        TC(10);
        KVLOAD(0, kvb + t0 * 4096); KVLOAD(1, kvb + (t0 + 1) * 4096); KVLOAD(2, kvb + (t0 + 2) * 4096);
        SB;
    }
    const float zz = __uint_as_float(__builtin_amdgcn_permlane32_swap(__float_as_uint(accq[12]), __float_as_uint(accq[12]), false, false)[0]);
    const float gtok = __builtin_amdgcn_rcpf(1.0f + EX2(-1.4426950408889634f * zz));
    if (h == 0) *(LDS_AS float*)(size_t)(lds0 + L_G + (32 * w + l31) * 4) = gtok;
    bf16x8 q0[4], q1[4];
    bf16x8 qa, qb;
    {
        const float qs = 1.4426950408889634f * 0.1889822365046136f;
        f32x16 q = accq * qs;
        u32x4 a0 = pack8(q, 0), a1 = pack8(q, 1);
        if (h == 0) { a1[2] = 0; a1[3] = 0; }
        qa = __builtin_bit_cast(bf16x8, a0); qb = __builtin_bit_cast(bf16x8, a1);
    }
    asm volatile("s_waitcnt lgkmcnt(0)" ::: "memory");
    if (lane == 0) __hip_atomic_fetch_add((LDS_AS unsigned*)(size_t)(lds0 + L_CTR + C_AP), 1u, __ATOMIC_RELAXED, __HIP_MEMORY_SCOPE_WORKGROUP);
    const f32x16 zero = {};
    V1[3] = (bf16x8){}; K0[3] = V1[3]; K1[3] = V1[3]; V0[3] = V1[3];
    f32x16 S = MFMA(K0[0], qa, zero); S = MFMA(K1[0], qb, S);
    set_max(qb, max16(S), h);
    const unsigned lo = lane * 16;
    *(LDS_AS bf16x8*)(size_t)(lds0 + L_QX + w * 2048 + lo) = qa;
    *(LDS_AS bf16x8*)(size_t)(lds0 + L_QX + w * 2048 + 1024 + lo) = qb;
    lds_sync4(lds0 + L_CTR + C_QW, 4, lane);
#pragma unroll
    for (int b = 0; b < 4; b++) { q0[b] = lds_b128(lds0 + L_QX + b * 2048 + lo); q1[b] = lds_b128(lds0 + L_QX + b * 2048 + 1024 + lo); }
    asm volatile("s_waitcnt lgkmcnt(0)" ::: "memory");
    if (lane == 0) __hip_atomic_fetch_add((LDS_AS unsigned*)(size_t)(lds0 + L_CTR + C_QR), 1u, __ATOMIC_RELAXED, __HIP_MEMORY_SCOPE_WORKGROUP);
    TC(13);
    __builtin_amdgcn_s_setprio(A2_PRIO);
    S = MFMA(K0[0], q0[0], zero); S = MFMA(K1[0], q1[0], S);
    f32x16 O[4];
#pragma unroll
    for (int b = 0; b < 4; b++) O[b] = zero;
    u32x4 P1 = {0, 0, 0, 0};
#define ASTEP(c, b) do { \
        constexpr int pc = (b) == 0 ? ((c) + 3) & 3 : (c), pb = ((b) + 3) & 3, nc = (b) == 3 ? ((c) + 1) & 3 : (c), nb = ((b) + 1) & 3; \
        O[pb] = MFMA(V1[pc], __builtin_bit_cast(bf16x8, P1), O[pb]); SB; \
        u32x4 p0, p1; \
        p0[0] = cvtpk(EX2(S[0]), EX2(S[1])); p0[1] = cvtpk(EX2(S[2]), EX2(S[3])); SB; \
        f32x16 Sn = MFMA(K0[nc], q0[nb], zero); SB; \
        if ((b) == 1) { KVLOAD(pc3(c), vnext); } \
        p0[2] = cvtpk(EX2(S[4]), EX2(S[5])); p0[3] = cvtpk(EX2(S[6]), EX2(S[7])); SB; \
        Sn = MFMA(K1[nc], q1[nb], Sn); SB; \
        p1[0] = cvtpk(EX2(S[8]), EX2(S[9])); p1[1] = cvtpk(EX2(S[10]), EX2(S[11])); SB; \
        O[b] = MFMA(V0[c], __builtin_bit_cast(bf16x8, p0), O[b]); SB; \
        p1[2] = cvtpk(EX2(S[12]), EX2(S[13])); p1[3] = cvtpk(EX2(S[14]), EX2(S[15])); SB; \
        P1 = p1; S = Sn; } while (0)
#define pc3(c) (((c) + 3) & 3)
#define ATILE(c) do { \
        const int tn = tt + (c) + 3;     \
        const unsigned vnext = lo + (tn < 16 ? (unsigned)WS_KV + ((t0 + tn) & 63) * 4096u : 0x7f000000u); \
        ASTEP(c, 0); ASTEP(c, 1); ASTEP(c, 2); ASTEP(c, 3); } while (0)
#pragma unroll 1
    for (int tt = 0; tt < 16; tt += 4) {
        ATILE(0); ATILE(1); ATILE(2); ATILE(3);
    }
    O[3] = MFMA(V1[3], __builtin_bit_cast(bf16x8, P1), O[3]);
    STAMP(5);
    lds_wait_ge(lds0 + L_CTR + C_B1, 4);
#pragma unroll
    for (int b = 0; b < 4; b++) {
        if (b != w) {
            const unsigned a = lds0 + L_MRG + (b * 3 + ((w - b - 1) & 3)) * 4096 + lo;
#pragma unroll
            for (int rg = 0; rg < 4; rg++) {
                f32x4 v = {O[b][4 * rg], O[b][4 * rg + 1], O[b][4 * rg + 2], O[b][4 * rg + 3]};
                *(LDS_AS f32x4*)(size_t)(a + rg * 1024) = v;
            }
        }
    }
    f32x16 o = zero;
#pragma unroll
    for (int b = 0; b < 4; b++) if (b == w) { o = O[b]; qa = q0[b]; qb = q1[b]; }
    lds_sync4(lds0 + L_CTR + C_OW, 4, lane);
#pragma unroll
    for (int u = 0; u < 3; u++) {
        const unsigned a = lds0 + L_MRG + (w * 3 + u) * 4096 + lo;
#pragma unroll
        for (int rg = 0; rg < 4; rg++) {
            f32x4 v = *(const LDS_AS f32x4*)(size_t)(a + rg * 1024);
            o[4 * rg] += v[0]; o[4 * rg + 1] += v[1]; o[4 * rg + 2] += v[2]; o[4 * rg + 3] += v[3];
        }
    }
    STAMP(6);
    auto rr = __builtin_amdgcn_permlane32_swap(__float_as_uint(o[12]), __float_as_uint(o[12]), false, false);
    float l = __uint_as_float(rr[0]);
    {
        bool ok = l < 1.152921504606846976e18f && l > 8.673617379884035e-19f;
#pragma unroll
        for (int r = 0; r < 16; r++) ok = ok && (__builtin_fabsf(o[r]) < 3.0e38f);
        if (__builtin_amdgcn_ballot_w64(!ok) != 0) {
            u32x4 tq = __builtin_bit_cast(u32x4, qb);
            if (h == 0) { tq[2] = 0; tq[3] = 0; }
            qb = __builtin_bit_cast(bf16x8, tq);
            float m = -INFINITY;
#pragma unroll 1
            for (int t = 0; t < 64; t++) {
                bf16x8 k0 = ld_sc1(wsr, kvb + t * 4096), k1 = ld_sc1(wsr, kvb + t * 4096 + 1024);
                f32x16 s = MFMA(k0, qa, zero); s = MFMA(k1, qb, s);
                m = __builtin_fmaxf(m, max16(s));
            }
            set_max(qb, m, h);
            o = zero;
#pragma unroll 1
            for (int t = 0; t < 64; t++) {
                bf16x8 k0 = ld_sc1(wsr, kvb + t * 4096), k1 = ld_sc1(wsr, kvb + t * 4096 + 1024);
                bf16x8 v0 = ld_sc1(wsr, kvb + t * 4096 + 2048), v1 = ld_sc1(wsr, kvb + t * 4096 + 3072);
                f32x16 s = MFMA(k0, qa, zero); s = MFMA(k1, qb, s);
#pragma unroll
                for (int r = 0; r < 16; r++) s[r] = EX2(s[r]);
                o = MFMA(v0, __builtin_bit_cast(bf16x8, pack8(s, 0)), o); o = MFMA(v1, __builtin_bit_cast(bf16x8, pack8(s, 1)), o);
            }
            auto r2 = __builtin_amdgcn_permlane32_swap(__float_as_uint(o[12]), __float_as_uint(o[12]), false, false);
            l = __uint_as_float(r2[0]);
        }
    }
    const float sc = gtok * __builtin_amdgcn_rcpf(l);
#pragma unroll
    for (int r = 0; r < 16; r++) o[r] *= sc;
    u32x4 o0 = pack8(o, 0), o1 = pack8(o, 1);
    if (h == 0) { o1[2] = 0; o1[3] = 0; }
    const unsigned dst = (unsigned)WS_OF + (T * 32 + 2 * j) * 1024 + lo;
    st_sc1(wsr, dst, o0); st_sc1(wsr, dst + 1024, o1);
    asm volatile("s_waitcnt vmcnt(0)" ::: "memory");
    if (lane < 7) flag_set(p.ws, WS_FO + (T * 7 + lane) * 128 + j * 8, tag);
    STAMP(7);
}

DI void b_wave(const Params& p, unsigned lds0, int g, int j, int wb, int lane, u64 epoch_v) {
    const int h = lane >> 5, l31 = lane & 31;
    const bool light = j >= 12;
    const int lp = j - 12;
    const int fb = light ? 48 + 2 * lp + wb : 4 * j + wb;
    const unsigned lo = lane * 16;
    f32x16 acc[4];
#pragma unroll
    for (int b = 0; b < 4; b++) acc[b] = (f32x16){};
    STAMP(0);
    __builtin_amdgcn_s_setprio(B_PRIO);
    const __amdgpu_buffer_rsrc_t wsr = ws_rsrc(p.ws);
    const unsigned ring = lds0 + L_RING + wb * 4096;
    u64 tag;
    if (light && wb >= 2) {
        __builtin_amdgcn_s_setprio(KV_PRIO);
        const int T = 4 * g + lp;
        const __amdgpu_buffer_rsrc_t rk = __builtin_amdgcn_make_buffer_rsrc((void*)(wb == 2 ? p.wk : p.wv), 0, 448 * 56, 0x00020000);
        const unsigned off = (lane >> 2) * 56 + ((lane & 3) == 3 ? 40 : (lane & 3) * 16);
        const unsigned dst = (unsigned)WS_KV + T * 4096 + lo;
        u32x4 R[28];
#pragma unroll
        for (int s = 0; s < 28; s++) R[s] = __builtin_amdgcn_raw_buffer_load_b128(rk, off, s * (16 * 56), 0);
        const unsigned wr = ring + lo, rd = ring + w_lane(lane);
        bf16x8 wf[28];
#pragma unroll
        for (int q = 0; q < 28; q += 4) {
#pragma unroll
            for (int u = 0; u < 4; u++) *(LDS_AS u32x4*)(size_t)(wr + u * 1024) = R[q + u];
#pragma unroll
            for (int u = 0; u < 4; u++) wf[q + u] = lds_tr2(rd + u * 1024, rd + u * 1024 + 256);
            SB;
        }
        STAMP(10);
        x_wait(lds0 + L_XE, 1, -1);
        STAMP(11);
        f32x16 a2 = {};
        {
            unsigned xa[4];
#pragma unroll
            for (int s = 0; s < 4; s++) xa[s] = x_addr(lds0, lane, s) + lp * 4096;
            bf16x8 xf[2][4];
#define KVX(q_) _Pragma("unroll") for (int u = 0; u < 4; u++) xf[(q_) & 1][u] = lds_b128(xa[u] + (q_) * X_SLOT)
            KVX(0); KVX(1);
            if (wb == 2) {
#pragma unroll
                for (int q = 0; q < 7; q++) {
                    SB;
#pragma unroll
                    for (int u = 0; u < 4; u++) { if (u & 1) a2 = MFMA(wf[4 * q + u], xf[q & 1][u], a2); else acc[0] = MFMA(wf[4 * q + u], xf[q & 1][u], acc[0]); }
                    SB;
                    if (q + 2 < 7) { KVX(q + 2); }
                }
            } else {
#pragma unroll
                for (int q = 0; q < 7; q++) {
                    SB;
#pragma unroll
                    for (int u = 0; u < 4; u++) { if (u & 1) a2 = MFMA(xf[q & 1][u], wf[4 * q + u], a2); else acc[0] = MFMA(xf[q & 1][u], wf[4 * q + u], acc[0]); }
                    SB;
                    if (q + 2 < 7) { KVX(q + 2); }
                }
            }
        }
        acc[0] += a2;
        STAMP(13);
        {
            u32x4 a0 = pack8(acc[0], 0), a1 = pack8(acc[0], 1);
            if (wb == 2) {
                if (h == 0) { a1[2] = 0x3f803f80u; a1[3] = 0x00003f80u; }
                st_sc1(wsr, dst, a0); st_sc1(wsr, dst + 1024, a1);
            } else {
                if (l31 == 24) { a0 = (u32x4){0x3f803f80u, 0x3f803f80u, 0x3f803f80u, 0x3f803f80u}; a1 = a0; }
                else if (l31 > 24 && l31 < 28) { a0 = (u32x4){0, 0, 0, 0}; a1 = a0; }
                st_sc1(wsr, dst + 2048, a0); st_sc1(wsr, dst + 3072, a1);
            }
        }
        STAMP(1);
        if (lane == 0) __hip_atomic_fetch_add((LDS_AS unsigned*)(size_t)(lds0 + L_CTR + C_B1), 1u, __ATOMIC_RELAXED, __HIP_MEMORY_SCOPE_WORKGROUP);
        tag = uniform64(epoch_v) + 1;
        asm volatile("s_waitcnt vmcnt(0)" ::: "memory");
        STAMP(14);
        flag_set(p.ws, WS_FKV + lane * FKV_COPY + T * 16 + (wb & 1) * 8, tag);
        STAMP(15);
    } else {
        const __amdgpu_buffer_rsrc_t rw1 = __builtin_amdgcn_make_buffer_rsrc((void*)p.w1, 0, 448 * 3584, 0x00020000);
        const unsigned off = (lane >> 2) * 3584 + fb * 64 + (lane & 3) * 16;
        u32x2 bv[4];
#pragma unroll
        for (int q = 0; q < 4; q++) bv[q] = *(const u32x2*)(p.b1 + fb * 32 + 4 * h + 8 * q);
        auto ld = [&](int s) { return __builtin_amdgcn_raw_buffer_load_b128(rw1, off, s * (16 * 3584), 0); };
        unsigned long long ptb[8];
        proj_loop(acc, lds0, ring, lane, light, ld, ptb);
        STAMP(1);
        if (lane == 0) __hip_atomic_fetch_add((LDS_AS unsigned*)(size_t)(lds0 + L_CTR + C_B1), 1u, __ATOMIC_RELAXED, __HIP_MEMORY_SCOPE_WORKGROUP);
        tag = uniform64(epoch_v) + 1;
        __builtin_amdgcn_s_setprio(G_PRIO);
#pragma unroll
        for (int t = 0; t < 4; t++) gelu_store(p, acc[t], bv, 4 * g + t, fb, lane);
        __builtin_amdgcn_s_setprio(F_PRIO);
        STAMP(12);
        asm volatile("s_waitcnt vmcnt(0)" ::: "memory");
        if (lane < 14) flag_set(p.ws, WS_FH + (g * 14 + lane) * 512 + fb * 8, tag);
        STAMP(3);

    }
    const bool tile = j < 14;
    if (!tile) {
        if (g == 0 && j == 15 && wb == 0) {
#pragma unroll 1
            for (int stage = 0; stage < 3; stage++) {
                const char* base = p.ws + (stage == 0 ? WS_FKV + lane * 8 : stage == 1 ? WS_FH + lane * 8 : WS_FO + (lane >> 4) * (7 * 128) + (lane & 15) * 8);
                const int n = stage == 0 ? 2 : 16;
                const unsigned step = stage == 0 ? 512 : stage == 1 ? 14 * 512 : 4 * 7 * 128;
                const bool on = stage != 1 || lane < 56;
#pragma unroll 1
                for (int spin = 0; spin < SPIN_CAP; spin++) {
                    u64 v[16];
#pragma unroll
                    for (int i = 0; i < 16; i++) v[i] = __hip_atomic_load((const u64*)(base + (i < n ? i : 0) * step), __ATOMIC_RELAXED, __HIP_MEMORY_SCOPE_AGENT);
                    unsigned bad = 0;
#pragma unroll
                    for (int i = 0; i < 16; i++) bad |= (v[i] != tag) ? 1u : 0u;
                    if (__builtin_amdgcn_ballot_w64(on && bad) == 0) break;
                    __builtin_amdgcn_s_sleep(16);
                }
            }
            if (lane == 0) __hip_atomic_store((u64*)(p.ws + WS_EPOCH), tag, __ATOMIC_RELAXED, __HIP_MEMORY_SCOPE_AGENT);
        }
        return;
    }
    const unsigned stg = lds0 + L_WO + wb * WO_SLICE;
    const int th = j & 1, cs = j >> 1, n0 = 64 * cs, T0 = 4 * g + 2 * th;
    const unsigned woff = ((lane & 31) >> 2) * 896 + (lane >> 5) * 64 + (lane & 3) * 16;
    const __amdgpu_buffer_rsrc_t rw2 = __builtin_amdgcn_make_buffer_rsrc((void*)p.w2, 0, 1792 * 896, 0x00020000);
    const unsigned w2v = woff + n0 * 2, w2s = 28 * wb * 16 * 896;
    constexpr int NB = 7;
    u32x4 Wr[NB][2];
#define ROT28(st_) (st_)
#define WLOAD(st_) do { const unsigned so_ = w2s + ROT28(st_) * (16 * 896); Wr[(st_) % NB][0] = __builtin_amdgcn_raw_buffer_load_b128(rw2, w2v, so_, 0); \
        Wr[(st_) % NB][1] = __builtin_amdgcn_raw_buffer_load_b128(rw2, w2v + 8 * 896, so_, 0); } while (0)
#pragma unroll
    for (int st = 0; st < NB; st++) WLOAD(st);
    const int Tm = T0 + (wb & 1);
    const int nc = n0 + 32 * (wb >> 1) + 4 * h;
    u32x2 b2v[4];
#pragma unroll
    for (int rg = 0; rg < 4; rg++) b2v[rg] = *(const u32x2*)(p.b2 + nc + 8 * rg);
    const unsigned fho = WS_FH + (g * 14 + j) * 512 + (14 * wb + lane) * 8;
    flags_wait(p.ws, fho, lane < 6, tag);
    lds_wait_ge(lds0 + L_CTR + C_AP, 4);
    lds_wait_ge(lds0 + L_CTR + C_B1, 4);
    STAMP(4);
#pragma unroll
    for (int i = 0; i < 4; i++) acc[i] = (f32x16){};
    {
        const unsigned wrd = stg + (4 * h + ((lane & 15) >> 2)) * 64 + ((lane >> 4) & 1) * 32 + (lane & 3) * 8;
        const unsigned hv = (unsigned)WS_HF + (T0 * 112 + 28 * wb) * 1024 + lo;
        bf16x8 H0[NB], H1[NB], wf0[2], wf1[2];
#define HLOAD(st_) do { const unsigned so_ = ROT28(st_) * 1024; H0[(st_) % NB] = ld_sc1s(wsr, hv, so_); H1[(st_) % NB] = ld_sc1s(wsr, hv + 112 * 1024, so_); } while (0)
#define WWRITE(st_) do { *(LDS_AS u32x4*)(size_t)(stg + ((st_) & 1) * 2048 + lo) = Wr[(st_) % NB][0]; *(LDS_AS u32x4*)(size_t)(stg + ((st_) & 1) * 2048 + 1024 + lo) = Wr[(st_) % NB][1]; } while (0)
#define WREAD(st_) do { wf0[(st_) & 1] = lds_tr2(wrd + ((st_) & 1) * 2048, wrd + ((st_) & 1) * 2048 + 1024); wf1[(st_) & 1] = lds_tr2(wrd + ((st_) & 1) * 2048 + 512, wrd + ((st_) & 1) * 2048 + 1536); } while (0)
#pragma unroll
        for (int st = 0; st < NB; st++) HLOAD(st);
        WWRITE(0); WLOAD(NB); WWRITE(1); WLOAD(NB + 1);
        WREAD(0);
#pragma unroll
        for (int st = 0; st < 28; st++) {
            SB;
            if (st + 1 < 28) WREAD(st + 1);
            if (st + 2 < 28) WWRITE(st + 2);
            if (st + 2 + NB < 28) WLOAD(st + 2 + NB);
            SB;
            acc[0] = MFMA(wf0[st & 1], H0[st % NB], acc[0]); acc[1] = MFMA(wf0[st & 1], H1[st % NB], acc[1]);
            acc[2] = MFMA(wf1[st & 1], H0[st % NB], acc[2]); acc[3] = MFMA(wf1[st & 1], H1[st % NB], acc[3]);
            SB;
            if (st + NB == 12) {
                u64 fv = tag;
                if (lane >= 6 && lane < 14) fv = __hip_atomic_load((const u64*)(p.ws + fho), __ATOMIC_RELAXED, __HIP_MEMORY_SCOPE_AGENT);
                if (__builtin_amdgcn_ballot_w64(fv != tag) != 0) flags_wait(p.ws, fho, lane >= 6 && lane < 14, tag);
                asm volatile("" ::: "memory");
            }
            if (st + NB < 28) HLOAD(st + NB);
        }
    }
    STAMP(5);
    asm volatile("s_waitcnt lgkmcnt(0)" ::: "memory");
    {
        const char* wos = (const char*)p.wo + n0 * 2;
#pragma unroll
        for (int i = 0; i < 14; i++) glds16(woff, wos + (size_t)(14 * wb + i) * (8 * 896), lds0 + L_WO + (14 * wb + i) * 1024);
    }
    lds_wait_ge(lds0 + L_CTR + C_QR, 4);
    {
        const unsigned rarea = lds0 + L_RED + wb * B_AREA;
#pragma unroll
        for (int i = 0; i < 4; i++) {
            if (i != wb) {
                const unsigned a = rarea + ((i - wb - 1) & 3) * 4096 + lo;
#pragma unroll
                for (int rg = 0; rg < 4; rg++) {
                    f32x4 v = {acc[i][4 * rg], acc[i][4 * rg + 1], acc[i][4 * rg + 2], acc[i][4 * rg + 3]};
                    *(LDS_AS f32x4*)(size_t)(a + rg * 1024) = v;
                }
            }
        }
    }
    f32x16 o = {};
#pragma unroll
    for (int i = 0; i < 4; i++) if (i == wb) o = acc[i];
    asm volatile("s_waitcnt vmcnt(0)" ::: "memory");
    lds_sync4(lds0 + L_CTR + C_RED, 4, lane);
#pragma unroll
    for (int u = 0; u < 4; u++) {
        if (u != wb) {
            const unsigned a = lds0 + L_RED + u * B_AREA + ((wb - u - 1) & 3) * 4096 + lo;
#pragma unroll
            for (int rg = 0; rg < 4; rg++) {
                f32x4 v = *(const LDS_AS f32x4*)(size_t)(a + rg * 1024);
                o[4 * rg] += v[0]; o[4 * rg + 1] += v[1]; o[4 * rg + 2] += v[2]; o[4 * rg + 3] += v[3];
            }
        }
    }
    {
        const float fg = 1.0f - *(const LDS_AS float*)(size_t)(lds0 + L_G + (64 * th + 32 * (wb & 1) + l31) * 4);
#pragma unroll
        for (int rg = 0; rg < 4; rg++) {
            const u32x2 bv = b2v[rg];
            o[4 * rg] = (o[4 * rg] + __uint_as_float(bv[0] << 16)) * fg; o[4 * rg + 1] = (o[4 * rg + 1] + __uint_as_float(bv[0] & 0xffff0000u)) * fg;
            o[4 * rg + 2] = (o[4 * rg + 2] + __uint_as_float(bv[1] << 16)) * fg; o[4 * rg + 3] = (o[4 * rg + 3] + __uint_as_float(bv[1] & 0xffff0000u)) * fg;
        }
    }
    STAMP(6);
    {
        const unsigned ob = (unsigned)WS_OF + Tm * 32 * 1024 + lo;
        const unsigned foo = WS_FO + (Tm * 7 + cs) * 128 + (lane & 15) * 8;
        bf16x8 of[32];
        unsigned ready = 0;
#pragma unroll
        for (int q = 0; q < 4; q++) {
#pragma unroll 1
            for (int spin = 0; spin < SPIN_CAP && ((ready >> (4 * q)) & 15u) != 15u; spin++) {
                if (spin) __builtin_amdgcn_s_sleep(POLL_SLEEP);
                u64 v = 0;
                if (lane < 16) v = __hip_atomic_load((const u64*)(p.ws + foo), __ATOMIC_RELAXED, __HIP_MEMORY_SCOPE_AGENT);
                ready = (unsigned)__builtin_amdgcn_ballot_w64(lane < 16 && v == tag);
            }
            asm volatile("" ::: "memory");
            if (q == 3) STAMP(7);
#pragma unroll
            for (int st = 8 * q; st < 8 * q + 8; st++) of[st] = ld_sc1(wsr, ob + st * 1024);
        }
        const unsigned lp16 = lds0 + L_WO + (wb >> 1) * 512 + ((lane & 15) >> 2) * 64 + ((lane >> 4) & 1) * 32 + (lane & 3) * 8;
        const unsigned wA = lp16 + h * 256, wB = lp16 + h * 768;
        f32x16 o2 = {};
#pragma unroll
        for (int st = 0; st < 32; st++) {
            const int hd = st >> 1, s = st & 1;
            const int r1 = 28 * hd + 16 * s, r2 = 28 * hd + (s ? 20 : 8);
            const unsigned a1 = ((r1 & 4) ? wB : wA) + (r1 >> 3) * 1024 + (r1 & 7) * 64, a2 = ((r2 & 4) ? wB : wA) + (r2 >> 3) * 1024 + (r2 & 7) * 64;
            const bf16x8 w0 = lds_tr2(a1, a2);
            if (st & 1) o2 = MFMA(w0, of[st], o2); else o = MFMA(w0, of[st], o);
        }
        o += o2;
    }
    STAMP(8);
    {
        unsigned short* orow = p.out + (size_t)(Tm * 32 + l31) * D_ + nc;
#pragma unroll
        for (int rg = 0; rg < 4; rg++) {
            u32x2 ov = {cvtpk(o[4 * rg], o[4 * rg + 1]), cvtpk(o[4 * rg + 2], o[4 * rg + 3])};
            *(u32x2*)(orow + 8 * rg) = ov;
        }
    }
    STAMP(9);
}

__global__ void __launch_bounds__(512, 2) gated_mix_fused(Params p) {
    extern __shared__ char smem[];
    const unsigned lds0 = (unsigned)(size_t)smem;
    const int b = blockIdx.x, xcd = b & 7, idx = b >> 3;
    const int g = xcd * 2 + (idx >> 4), j = idx & 15;
    const int w = __builtin_amdgcn_readfirstlane(threadIdx.x >> 6), lane = threadIdx.x & 63;
    const u64 epoch = __hip_atomic_load((const u64*)(p.ws + WS_EPOCH), __ATOMIC_RELAXED, __HIP_MEMORY_SCOPE_AGENT);
    if (w == 0 && lane < 16) *(LDS_AS unsigned*)(size_t)(lds0 + L_CTR + 4 * lane) = 0;
    wait_bar();
    if (w < 4) a_wave(p, lds0, g, j, w, lane, epoch);
    else b_wave(p, lds0, g, j, w - 4, lane, epoch);
}

extern "C" void kernel_launch(void* const* d_in, const int* in_sizes, int n_in, void* d_out, int out_size, void* d_ws, size_t ws_size, hipStream_t stream) {
    Params p;
    p.x = (const unsigned short*)d_in[0]; p.wq = (const unsigned short*)d_in[1]; p.wk = (const unsigned short*)d_in[2];
    p.wv = (const unsigned short*)d_in[3]; p.wo = (const unsigned short*)d_in[4]; p.w1 = (const unsigned short*)d_in[6];
    p.b1 = (const unsigned short*)d_in[7]; p.w2 = (const unsigned short*)d_in[8]; p.b2 = (const unsigned short*)d_in[9];
    p.gw = (const unsigned short*)d_in[10]; p.out = (unsigned short*)d_out; p.ws = (char*)d_ws;
    (void)hipFuncSetAttribute((const void*)gated_mix_fused, hipFuncAttributeMaxDynamicSharedMemorySize, LDS_BYTES);
    gated_mix_fused<<<256, 512, LDS_BYTES, stream>>>(p);
}
```

```cpp
#include <hip/hip_runtime.h>
#include <hip/hip_bf16.h>

typedef short bf16x8 __attribute__((ext_vector_type(8)));
typedef short s16x4 __attribute__((ext_vector_type(4)));
typedef __bf16 bf16x2 __attribute__((ext_vector_type(2)));
typedef float f32x16 __attribute__((ext_vector_type(16)));
typedef float f32x4 __attribute__((ext_vector_type(4)));
typedef float f32x2 __attribute__((ext_vector_type(2)));
typedef unsigned u32x4 __attribute__((ext_vector_type(4)));
typedef unsigned u32x2 __attribute__((ext_vector_type(2)));
typedef short v4i16_t __attribute__((ext_vector_type(4)));
typedef unsigned long long u64;
#define LDS_AS __attribute__((address_space(3)))
#define DI __device__ __forceinline__

constexpr int S_ = 2048, D_ = 448, F_ = 1792;
constexpr size_t WS_EPOCH = 0;
constexpr size_t WS_FKV = 65536, FKV_COPY = 1280;
constexpr size_t WS_FH = 163840;
constexpr size_t WS_FO = 294912;
constexpr size_t WS_KV = 1u << 20;
constexpr size_t WS_OF = 2u << 20;
constexpr size_t WS_HF = 4u << 20;

#define STAMP(i) do {} while (0)
#define MFMA(a, b, c) __builtin_amdgcn_mfma_f32_32x32x16_bf16(a, b, c, 0, 0, 0)
#define SB __builtin_amdgcn_sched_barrier(0)
#define EX2(x) __builtin_amdgcn_exp2f(x)
constexpr int SPIN_CAP = 1 << 21;
#define POLL_SLEEP 2
#define G_PRIO 3
#define F_PRIO 0
#define KV_PRIO 3
#define A1_PRIO 2
#define A2_PRIO 0
#define B_PRIO 2

DI unsigned cvtpk(float lo, float hi) { f32x2 v = {lo, hi}; return __builtin_bit_cast(unsigned, __builtin_convertvector(v, bf16x2)); }
DI void glds16(unsigned voff, const void* sbase, unsigned lds_dst) {
    unsigned keep;
    asm volatile("s_mov_b32 %0, m0\n\ts_mov_b32 m0, %3\n\ts_nop 0\n\tglobal_load_lds_dwordx4 %1, %2\n\ts_mov_b32 m0, %0"
                 : "=&s"(keep) : "v"(voff), "s"(sbase), "s"(lds_dst) : "memory");
}
DI void glds16_sc1(unsigned voff, const void* sbase, unsigned lds_dst) {
    unsigned keep;
    asm volatile("s_mov_b32 %0, m0\n\ts_mov_b32 m0, %3\n\ts_nop 0\n\tglobal_load_lds_dwordx4 %1, %2 sc1\n\ts_mov_b32 m0, %0"
                 : "=&s"(keep) : "v"(voff), "s"(sbase), "s"(lds_dst) : "memory");
}
DI bf16x8 lds_b128(unsigned a) { return *(const LDS_AS bf16x8*)(size_t)a; }
DI s16x4 lds_tr(unsigned a) { return __builtin_bit_cast(s16x4, __builtin_amdgcn_ds_read_tr16_b64_v4i16((LDS_AS v4i16_t*)(size_t)a)); }
DI bf16x8 lds_tr2(unsigned a0, unsigned a1) { s16x4 lo = lds_tr(a0), hi = lds_tr(a1); return __builtin_shufflevector(lo, hi, 0, 1, 2, 3, 4, 5, 6, 7); }
DI float dot2(unsigned a, unsigned b, float c) { return __builtin_amdgcn_fdot2_f32_bf16(__builtin_bit_cast(bf16x2, a), __builtin_bit_cast(bf16x2, b), c, false); }
DI u32x4 pack8(const f32x16& a, int s) {
    u32x4 r;
    r[0] = cvtpk(a[8 * s + 0], a[8 * s + 1]); r[1] = cvtpk(a[8 * s + 2], a[8 * s + 3]);
    r[2] = cvtpk(a[8 * s + 4], a[8 * s + 5]); r[3] = cvtpk(a[8 * s + 6], a[8 * s + 7]);
    return r;
}
DI float gelu_tanh(float x) {
    const float c1 = -2.0f * 1.4426950408889634f * 0.7978845608028654f, c2 = c1 * 0.044715f;
    float t = x * x;
    float e = EX2(x * __builtin_fmaf(t, c2, c1));
    return x * __builtin_amdgcn_rcpf(1.0f + e);
}
DI __amdgpu_buffer_rsrc_t ws_rsrc(const char* ws) { return __builtin_amdgcn_make_buffer_rsrc((void*)ws, 0, 16u << 20, 0x00020000); }
DI void st_sc1(__amdgpu_buffer_rsrc_t r, unsigned off, u32x4 v) { __builtin_amdgcn_raw_buffer_store_b128(v, r, off, 0, 16); }
DI void flag_set(char* ws, size_t off, u64 tag) { __hip_atomic_store((u64*)(ws + off), tag, __ATOMIC_RELAXED, __HIP_MEMORY_SCOPE_AGENT); }
DI void flags_wait(const char* ws, unsigned off, bool on, u64 tag) {
#pragma unroll 1
    for (int spin = 0; spin < SPIN_CAP; spin++) {
        u64 v = tag;
        if (on) v = __hip_atomic_load((const u64*)(ws + off), __ATOMIC_RELAXED, __HIP_MEMORY_SCOPE_AGENT);
        if (__builtin_amdgcn_ballot_w64(v != tag) == 0) break;
        __builtin_amdgcn_s_sleep(POLL_SLEEP);
    }
    asm volatile("s_waitcnt vmcnt(0)" ::: "memory");
}
DI u64 uniform64(u64 v) {
    asm volatile("" : "+v"(v) :: "memory");
    const unsigned lo = __builtin_amdgcn_readfirstlane((unsigned)v), hi = __builtin_amdgcn_readfirstlane((unsigned)(v >> 32));
    return ((u64)hi << 32) | lo;
}
DI unsigned lds_ld(unsigned a) { return __hip_atomic_load((LDS_AS unsigned*)(size_t)a, __ATOMIC_RELAXED, __HIP_MEMORY_SCOPE_WORKGROUP); }
DI void lds_st(unsigned a, unsigned v) { __hip_atomic_store((LDS_AS unsigned*)(size_t)a, v, __ATOMIC_RELAXED, __HIP_MEMORY_SCOPE_WORKGROUP); }
DI void lds_sync4(unsigned ctr, unsigned target, int lane) {
    asm volatile("s_waitcnt lgkmcnt(0)" ::: "memory");
    LDS_AS unsigned* c = (LDS_AS unsigned*)(size_t)ctr;
    if (lane == 0) __hip_atomic_fetch_add(c, 1u, __ATOMIC_RELAXED, __HIP_MEMORY_SCOPE_WORKGROUP);
#pragma unroll 1
    for (int spin = 0; spin < SPIN_CAP; spin++) {
        unsigned v = __builtin_amdgcn_readfirstlane(__hip_atomic_load(c, __ATOMIC_RELAXED, __HIP_MEMORY_SCOPE_WORKGROUP));
        if (v >= target) break;
        __builtin_amdgcn_s_sleep(1);
    }
    asm volatile("" ::: "memory");
}

struct Params {
    const unsigned short *x, *wq, *wk, *wv, *wo, *w1, *b1, *w2, *b2, *gw;
    unsigned short* out;
    char* ws;
};

constexpr unsigned X_SLOT = 16384, L_WQ = 114688, L_RING = 143360, L_QX = L_WQ;
constexpr unsigned L_WO = 0, WO_SLICE = 14336, L_MRG = 57344, L_RED = 106496, B_AREA = 12288;
constexpr unsigned P1_GW = 159744, L_G = P1_GW + 1024;
constexpr unsigned L_CTR = L_G + 512;
constexpr unsigned C_QW = 0, C_QR = 4, C_OW = 8, C_RED = 12, C_AP = 16, C_B1 = 20, C_KVS = 24, L_XF = L_CTR + 32, L_XE = L_CTR + 48;
constexpr unsigned LDS_BYTES = L_CTR + 64;
static_assert(L_RING + 4 * 4096 <= P1_GW && L_RED + 4 * B_AREA <= P1_GW && L_MRG + 12 * 4096 <= L_RED && LDS_BYTES <= 163840 && (L_XF & 15) == 0, "LDS map");

template <int N> DI void wait_vm() { asm volatile("s_waitcnt vmcnt(%0)" ::"n"(N) : "memory"); }
DI void wait_bar() { asm volatile("s_waitcnt lgkmcnt(0)\n\ts_barrier" ::: "memory"); }
DI u32x4 lds_flags(unsigned a) { u32x4 v; asm volatile("ds_read_b128 %0, %1" : "=v"(v) : "v"(a) : "memory"); return v; }
DI void lds_wait_ge(unsigned a, unsigned need) {
#pragma unroll 1
    for (int spin = 0; spin < SPIN_CAP; spin++) {
        if ((unsigned)__builtin_amdgcn_readfirstlane(lds_ld(a)) >= need) break;
        __builtin_amdgcn_s_sleep(1);
    }
    asm volatile("" ::: "memory");
}
DI void x_wait(unsigned xf, unsigned need, int only) {
#pragma unroll 1
    for (int spin = 0; spin < SPIN_CAP; spin++) {
        unsigned m;
        if (only < 0) {
            const unsigned a = lds_ld(xf), b = lds_ld(xf + 4), c = lds_ld(xf + 8), d = lds_ld(xf + 12);
            m = min(min(a, b), min(c, d));
        } else m = lds_ld(xf + 4 * only);
        if ((unsigned)__builtin_amdgcn_readfirstlane(m) >= need) break;
        __builtin_amdgcn_s_sleep(1);
    }
    asm volatile("" ::: "memory");
}
DI unsigned x_addr(unsigned lds0, int lane, int s) {
    const int h = lane >> 5, l31 = lane & 31;
    return lds0 + l31 * 128 + (((2 * s + h) ^ ((l31 >> 1) & 7)) << 4);
}
DI unsigned w_lane(int lane) { return (lane >> 5) * 512 + ((lane & 15) >> 2) * 64 + ((lane >> 4) & 1) * 32 + (lane & 3) * 8; }
#define NR1 8
template <typename Ld>
DI void proj_loop(f32x16 (&acc)[4], unsigned lds0, unsigned ring, int lane, bool light, Ld&& ld, unsigned long long* ptb) {
    constexpr int NR = NR1;
    u32x4 R[NR];
#define W1_DELAY 3
    if (light) lds_wait_ge(lds0 + L_CTR + C_KVS, 2);
    else if (W1_DELAY > 0) x_wait(lds0 + L_XF, W1_DELAY, -1);
#pragma unroll
    for (int s = 0; s < NR; s++) R[s] = ld(s);
    unsigned xa[4];
#pragma unroll
    for (int s = 0; s < 4; s++) xa[s] = x_addr(lds0, lane, s);
    const unsigned wr = ring + lane * 16, rd = ring + w_lane(lane), xf = lds0 + L_XF;
    bf16x8 xf_[3][4], wf[3];
#define PWRITE(s_) do { *(LDS_AS u32x4*)(size_t)(wr + ((s_) & 3) * 1024) = R[(s_) % NR]; if ((s_) + NR < 28) R[(s_) % NR] = ld((s_) + NR); } while (0)
#define PREADW(s_) do { wf[(s_) % 3] = lds_tr2(rd + ((s_) & 3) * 1024, rd + ((s_) & 3) * 1024 + 256); } while (0)
#define PREADX(s_, t_) do { xf_[(s_) % 3][t_] = lds_b128(xa[(s_) & 3] + ((s_) >> 2) * X_SLOT + (t_) * 4096); } while (0)
    PWRITE(0); PWRITE(1); PWRITE(2);
    x_wait(xf, 1, -1);
    PREADW(0); PREADX(0, 0); PREADX(0, 1); PREADX(0, 2); PREADX(0, 3);
    PREADW(1); PREADX(1, 0); PREADX(1, 1); PREADX(1, 2); PREADX(1, 3);
#pragma unroll
    for (int s = 0; s < 28; s++) {
        SB;
        if ((s & 3) == 2 && s + 2 < 28) x_wait(xf, (s + 2) / 4 + 1, -1);
        acc[0] = MFMA(wf[s % 3], xf_[s % 3][0], acc[0]); SB;
        if (s + 2 < 28) { PREADW(s + 2); PREADX(s + 2, 0); } SB;
        acc[1] = MFMA(wf[s % 3], xf_[s % 3][1], acc[1]); SB;
        if (s + 2 < 28) { PREADX(s + 2, 1); PREADX(s + 2, 2); } SB;
        acc[2] = MFMA(wf[s % 3], xf_[s % 3][2], acc[2]); SB;
        if (s + 2 < 28) PREADX(s + 2, 3);
        if (s + 3 < 28) PWRITE(s + 3);
        SB;
        acc[3] = MFMA(wf[s % 3], xf_[s % 3][3], acc[3]);
    }
    SB;
#undef PWRITE
#undef PREADW
#undef PREADX
}
DI void gelu_store(const Params& p, const f32x16& acc, const u32x2 (&bv)[4], int T, int fb, int lane) {
    f32x16 a;
#pragma unroll
    for (int q = 0; q < 4; q++) {
        a[4 * q + 0] = acc[4 * q + 0] + __uint_as_float(bv[q][0] << 16); a[4 * q + 1] = acc[4 * q + 1] + __uint_as_float(bv[q][0] & 0xffff0000u);
        a[4 * q + 2] = acc[4 * q + 2] + __uint_as_float(bv[q][1] << 16); a[4 * q + 3] = acc[4 * q + 3] + __uint_as_float(bv[q][1] & 0xffff0000u);
    }
#pragma unroll
    for (int r = 0; r < 16; r++) a[r] = gelu_tanh(a[r]);
    const unsigned dst = (unsigned)WS_HF + (T * 112 + 2 * fb) * 1024 + lane * 16;
    const __amdgpu_buffer_rsrc_t r_ = ws_rsrc(p.ws);
    st_sc1(r_, dst, pack8(a, 0)); st_sc1(r_, dst + 1024, pack8(a, 1));
}

DI float max16(const f32x16& s) {
    float a = __builtin_fmaxf(__builtin_fmaxf(s[0], s[1]), s[2]), b = __builtin_fmaxf(__builtin_fmaxf(s[3], s[4]), s[5]);
#pragma unroll
    for (int r = 6; r < 14; r += 4) { a = __builtin_fmaxf(__builtin_fmaxf(a, s[r]), s[r + 1]); b = __builtin_fmaxf(__builtin_fmaxf(b, s[r + 2]), s[r + 3]); }
    a = __builtin_fmaxf(__builtin_fmaxf(a, s[14]), s[15]);
    return __builtin_fmaxf(a, b);
}
DI void set_max(bf16x8& q1, float m, int h) {
    auto rr = __builtin_amdgcn_permlane32_swap(__float_as_uint(m), __float_as_uint(m), false, false);
    const float nm = -__builtin_fmaxf(__uint_as_float(rr[0]), __uint_as_float(rr[1]));
    const unsigned a = cvtpk(nm, 0.f) & 0xffffu;
    const float r1 = nm - __uint_as_float(a << 16);
    const unsigned b = cvtpk(r1, 0.f) & 0xffffu;
    const float r2 = r1 - __uint_as_float(b << 16);
    const unsigned c = cvtpk(r2, 0.f) & 0xffffu;
    u32x4 t = __builtin_bit_cast(u32x4, q1);
    if (h == 0) { t[2] = a | (b << 16); t[3] = c; }
    q1 = __builtin_bit_cast(bf16x8, t);
}
DI bf16x8 ld_sc1s(__amdgpu_buffer_rsrc_t r, unsigned off, unsigned soff) { return __builtin_bit_cast(bf16x8, __builtin_amdgcn_raw_buffer_load_b128(r, off, soff, 16)); }
DI bf16x8 ld_sc1(__amdgpu_buffer_rsrc_t r, unsigned off) { return __builtin_bit_cast(bf16x8, __builtin_amdgcn_raw_buffer_load_b128(r, off, 0, 16)); }

DI void a_wave(const Params& p, unsigned lds0, int g, int j, int w, int lane, u64 epoch_v) {
    const int h = lane >> 5, l31 = lane & 31;
    const int T = 4 * g + w;
    f32x16 accq = {};
    STAMP(0);
    __builtin_amdgcn_s_setprio(A1_PRIO);
    unsigned xoff[4];
#pragma unroll
    for (int i = 0; i < 4; i++) {
        const int row = 32 * w + 8 * i + (lane >> 3);
        const int c = (lane & 7) ^ ((row >> 1) & 7);
        xoff[i] = row * 896 + c * 16;
    }
    const char* xb = (const char*)p.x + (size_t)g * 128 * 896;
    const unsigned qoff = (16 * w + (lane >> 2)) * 896 + ((lane & 3) == 3 ? 40 : (lane & 3) * 16);
    const char* qb_ = (const char*)p.wq + j * 56;
    glds16((lane < 56 ? lane : 55) * 16, p.gw, lds0 + P1_GW);
    auto dma = [&](int c) {
#pragma unroll
        for (int i = 0; i < 4; i++) glds16(xoff[i], xb + c * 128, lds0 + c * X_SLOT + (32 * w + 8 * i) * 128);
        glds16(qoff, qb_ + c * (64 * 896), lds0 + L_WQ + c * 4096 + w * 1024);
    };
    const bool light = j >= 12;
    if (light) {
        const int row = 32 * (j - 12) + 8 * w + (lane >> 3);
        const unsigned eo = row * 896 + (((lane & 7) ^ ((row >> 1) & 7)) << 4);
#pragma unroll
        for (int c = 0; c < 7; c++) glds16(eo, xb + c * 128, lds0 + c * X_SLOT + (32 * (j - 12) + 8 * w) * 128);
    }
    if (light) {
        wait_vm<0>();
        if (lane == 0) lds_st(lds0 + L_XE + 4 * w, 1);
        lds_wait_ge(lds0 + L_CTR + C_KVS, 2);
    }
    dma(0); dma(1); dma(2);
    u64 tag;
    const __amdgpu_buffer_rsrc_t wsr = ws_rsrc(p.ws);
    const int t0 = (4 * g + 4 * j + 16 * w) & 63;
    bf16x8 K0[4], K1[4], V0[4], V1[4];
    const unsigned kvb = (unsigned)WS_KV + lane * 16;
#define KVLOAD(set, vo) do { const unsigned o_ = (vo); K0[set] = ld_sc1(wsr, o_); K1[set] = ld_sc1(wsr, o_ + 1024); V0[set] = ld_sc1(wsr, o_ + 2048); V1[set] = ld_sc1(wsr, o_ + 3072); } while (0)
    {
        unsigned xa[4];
#pragma unroll
        for (int s = 0; s < 4; s++) xa[s] = x_addr(lds0, lane, s) + w * 4096;
        const unsigned wa = lds0 + L_WQ + w_lane(lane);
        unsigned short gwv[7];
        auto publish = [&](int c) {
            if (lane < 16) *(LDS_AS unsigned short*)(size_t)(lds0 + L_WQ + c * 4096 + w * 1024 + lane * 64 + 48) = gwv[c];
            asm volatile("s_waitcnt lgkmcnt(0)" ::: "memory");
            if (lane == 0) lds_st(lds0 + L_XF + 4 * w, c + 1);
        };
        auto qchunk = [&](int c) {
            bf16x8 xf[4], wf[4];
#pragma unroll
            for (int s = 0; s < 4; s++) { xf[s] = lds_b128(xa[s] + c * X_SLOT); wf[s] = lds_tr2(wa + c * 4096 + s * 1024, wa + c * 4096 + s * 1024 + 256); }
#pragma unroll
            for (int s = 0; s < 4; s++) accq = MFMA(wf[s], xf[s], accq);
        };
#define TC(c)
        wait_vm<10>(); TC(0);
#pragma unroll
        for (int c = 0; c < 7; c++) gwv[c] = *(const LDS_AS unsigned short*)(size_t)(lds0 + P1_GW + (64 * c + 16 * w + (lane & 15)) * 2);
        publish(0); dma(3);
        wait_vm<10>(); TC(1); publish(1); dma(4);
        wait_vm<10>(); TC(2); publish(2); dma(5);
        wait_vm<10>(); TC(3); publish(3); dma(6);
        wait_vm<10>(); TC(4); publish(4);
        x_wait(lds0 + L_XF, 2, -1); qchunk(0); qchunk(1); SB;
        wait_vm<5>(); TC(5); publish(5);
        x_wait(lds0 + L_XF, 4, -1); qchunk(2); qchunk(3); SB;
        wait_vm<0>(); TC(6); publish(6);
        tag = uniform64(epoch_v) + 1;
        const unsigned fo = WS_FKV + (g * 4 + (j >> 2)) * FKV_COPY + ((t0 + (lane >> 1)) & 63) * 16 + (lane & 1) * 8;
        u64 fv = tag;
        if (lane < 32) fv = __hip_atomic_load((const u64*)(p.ws + fo), __ATOMIC_RELAXED, __HIP_MEMORY_SCOPE_AGENT);
        x_wait(lds0 + L_XF, 6, -1); TC(8); qchunk(4); qchunk(5); SB; TC(9);
        x_wait(lds0 + L_XF, 7, -1); TC(11); qchunk(6); SB; TC(12);
        asm volatile("" : "+v"(fv) :: "memory");
        if (__builtin_amdgcn_ballot_w64(fv != tag) != 0) flags_wait(p.ws, fo, lane < 32, tag);
        TC(10);
        KVLOAD(0, kvb + t0 * 4096); KVLOAD(1, kvb + (t0 + 1) * 4096); KVLOAD(2, kvb + (t0 + 2) * 4096);
        SB;
    }
    const float zz = __uint_as_float(__builtin_amdgcn_permlane32_swap(__float_as_uint(accq[12]), __float_as_uint(accq[12]), false, false)[0]);
    const float gtok = __builtin_amdgcn_rcpf(1.0f + EX2(-1.4426950408889634f * zz));
    if (h == 0) *(LDS_AS float*)(size_t)(lds0 + L_G + (32 * w + l31) * 4) = gtok;
    bf16x8 q0[4], q1[4];
    bf16x8 qa, qb;
    {
        const float qs = 1.4426950408889634f * 0.1889822365046136f;
        f32x16 q = accq * qs;
        u32x4 a0 = pack8(q, 0), a1 = pack8(q, 1);
        if (h == 0) { a1[2] = 0; a1[3] = 0; }
        qa = __builtin_bit_cast(bf16x8, a0); qb = __builtin_bit_cast(bf16x8, a1);
    }
    asm volatile("s_waitcnt lgkmcnt(0)" ::: "memory");
    if (lane == 0) __hip_atomic_fetch_add((LDS_AS unsigned*)(size_t)(lds0 + L_CTR + C_AP), 1u, __ATOMIC_RELAXED, __HIP_MEMORY_SCOPE_WORKGROUP);
    const f32x16 zero = {};
    V1[3] = (bf16x8){}; K0[3] = V1[3]; K1[3] = V1[3]; V0[3] = V1[3];
    f32x16 S = MFMA(K0[0], qa, zero); S = MFMA(K1[0], qb, S);
    set_max(qb, max16(S), h);
    const unsigned lo = lane * 16;
    *(LDS_AS bf16x8*)(size_t)(lds0 + L_QX + w * 2048 + lo) = qa;
    *(LDS_AS bf16x8*)(size_t)(lds0 + L_QX + w * 2048 + 1024 + lo) = qb;
    lds_sync4(lds0 + L_CTR + C_QW, 4, lane);
#pragma unroll
    for (int b = 0; b < 4; b++) { q0[b] = lds_b128(lds0 + L_QX + b * 2048 + lo); q1[b] = lds_b128(lds0 + L_QX + b * 2048 + 1024 + lo); }
    asm volatile("s_waitcnt lgkmcnt(0)" ::: "memory");
    if (lane == 0) __hip_atomic_fetch_add((LDS_AS unsigned*)(size_t)(lds0 + L_CTR + C_QR), 1u, __ATOMIC_RELAXED, __HIP_MEMORY_SCOPE_WORKGROUP);
    TC(13);
    __builtin_amdgcn_s_setprio(A2_PRIO);
    S = MFMA(K0[0], q0[0], zero); S = MFMA(K1[0], q1[0], S);
    f32x16 O[4];
#pragma unroll
    for (int b = 0; b < 4; b++) O[b] = zero;
    u32x4 P1 = {0, 0, 0, 0};
#define ASTEP(c, b) do { \
        constexpr int pc = (b) == 0 ? ((c) + 3) & 3 : (c), pb = ((b) + 3) & 3, nc = (b) == 3 ? ((c) + 1) & 3 : (c), nb = ((b) + 1) & 3; \
        O[pb] = MFMA(V1[pc], __builtin_bit_cast(bf16x8, P1), O[pb]); SB; \
        u32x4 p0, p1; \
        p0[0] = cvtpk(EX2(S[0]), EX2(S[1])); p0[1] = cvtpk(EX2(S[2]), EX2(S[3])); SB; \
        f32x16 Sn = MFMA(K0[nc], q0[nb], zero); SB; \
        if ((b) == 1) { KVLOAD(pc3(c), vnext); } \
        p0[2] = cvtpk(EX2(S[4]), EX2(S[5])); p0[3] = cvtpk(EX2(S[6]), EX2(S[7])); SB; \
        Sn = MFMA(K1[nc], q1[nb], Sn); SB; \
        p1[0] = cvtpk(EX2(S[8]), EX2(S[9])); p1[1] = cvtpk(EX2(S[10]), EX2(S[11])); SB; \
        O[b] = MFMA(V0[c], __builtin_bit_cast(bf16x8, p0), O[b]); SB; \
        p1[2] = cvtpk(EX2(S[12]), EX2(S[13])); p1[3] = cvtpk(EX2(S[14]), EX2(S[15])); SB; \
        P1 = p1; S = Sn; } while (0)
#define pc3(c) (((c) + 3) & 3)
#define ATILE(c) do { \
        const int tn = tt + (c) + 3;     \
        const unsigned vnext = lo + (tn < 16 ? (unsigned)WS_KV + ((t0 + tn) & 63) * 4096u : 0x7f000000u); \
        ASTEP(c, 0); ASTEP(c, 1); ASTEP(c, 2); ASTEP(c, 3); } while (0)
#pragma unroll 1
    for (int tt = 0; tt < 16; tt += 4) {
        ATILE(0); ATILE(1); ATILE(2); ATILE(3);
    }
    O[3] = MFMA(V1[3], __builtin_bit_cast(bf16x8, P1), O[3]);
    STAMP(5);
    lds_wait_ge(lds0 + L_CTR + C_B1, 4);
#pragma unroll
    for (int b = 0; b < 4; b++) {
        if (b != w) {
            const unsigned a = lds0 + L_MRG + (b * 3 + ((w - b - 1) & 3)) * 4096 + lo;
#pragma unroll
            for (int rg = 0; rg < 4; rg++) {
                f32x4 v = {O[b][4 * rg], O[b][4 * rg + 1], O[b][4 * rg + 2], O[b][4 * rg + 3]};
                *(LDS_AS f32x4*)(size_t)(a + rg * 1024) = v;
            }
        }
    }
    f32x16 o = zero;
#pragma unroll
    for (int b = 0; b < 4; b++) if (b == w) { o = O[b]; qa = q0[b]; qb = q1[b]; }
    lds_sync4(lds0 + L_CTR + C_OW, 4, lane);
#pragma unroll
    for (int u = 0; u < 3; u++) {
        const unsigned a = lds0 + L_MRG + (w * 3 + u) * 4096 + lo;
#pragma unroll
        for (int rg = 0; rg < 4; rg++) {
            f32x4 v = *(const LDS_AS f32x4*)(size_t)(a + rg * 1024);
            o[4 * rg] += v[0]; o[4 * rg + 1] += v[1]; o[4 * rg + 2] += v[2]; o[4 * rg + 3] += v[3];
        }
    }
    STAMP(6);
    auto rr = __builtin_amdgcn_permlane32_swap(__float_as_uint(o[12]), __float_as_uint(o[12]), false, false);
    float l = __uint_as_float(rr[0]);
    {
        bool ok = l < 1.152921504606846976e18f && l > 8.673617379884035e-19f;
#pragma unroll
        for (int r = 0; r < 16; r++) ok = ok && (__builtin_fabsf(o[r]) < 3.0e38f);
        if (__builtin_amdgcn_ballot_w64(!ok) != 0) {
            u32x4 tq = __builtin_bit_cast(u32x4, qb);
            if (h == 0) { tq[2] = 0; tq[3] = 0; }
            qb = __builtin_bit_cast(bf16x8, tq);
            float m = -INFINITY;
#pragma unroll 1
            for (int t = 0; t < 64; t++) {
                bf16x8 k0 = ld_sc1(wsr, kvb + t * 4096), k1 = ld_sc1(wsr, kvb + t * 4096 + 1024);
                f32x16 s = MFMA(k0, qa, zero); s = MFMA(k1, qb, s);
                m = __builtin_fmaxf(m, max16(s));
            }
            set_max(qb, m, h);
            o = zero;
#pragma unroll 1
            for (int t = 0; t < 64; t++) {
                bf16x8 k0 = ld_sc1(wsr, kvb + t * 4096), k1 = ld_sc1(wsr, kvb + t * 4096 + 1024);
                bf16x8 v0 = ld_sc1(wsr, kvb + t * 4096 + 2048), v1 = ld_sc1(wsr, kvb + t * 4096 + 3072);
                f32x16 s = MFMA(k0, qa, zero); s = MFMA(k1, qb, s);
#pragma unroll
                for (int r = 0; r < 16; r++) s[r] = EX2(s[r]);
                o = MFMA(v0, __builtin_bit_cast(bf16x8, pack8(s, 0)), o); o = MFMA(v1, __builtin_bit_cast(bf16x8, pack8(s, 1)), o);
            }
            auto r2 = __builtin_amdgcn_permlane32_swap(__float_as_uint(o[12]), __float_as_uint(o[12]), false, false);
            l = __uint_as_float(r2[0]);
        }
    }
    const float sc = gtok * __builtin_amdgcn_rcpf(l);
#pragma unroll
    for (int r = 0; r < 16; r++) o[r] *= sc;
    u32x4 o0 = pack8(o, 0), o1 = pack8(o, 1);
    if (h == 0) { o1[2] = 0; o1[3] = 0; }
    const unsigned dst = (unsigned)WS_OF + (T * 32 + 2 * j) * 1024 + lo;
    st_sc1(wsr, dst, o0); st_sc1(wsr, dst + 1024, o1);
    asm volatile("s_waitcnt vmcnt(0)" ::: "memory");
    if (lane < 7) flag_set(p.ws, WS_FO + (T * 7 + lane) * 128 + j * 8, tag);
    STAMP(7);
}

DI void b_wave(const Params& p, unsigned lds0, int g, int j, int wb, int lane, u64 epoch_v) {
    const int h = lane >> 5, l31 = lane & 31;
    const bool light = j >= 12;
    const int lp = j - 12;
    const int fb = light ? 48 + 2 * lp + wb : 4 * j + wb;
    const unsigned lo = lane * 16;
    f32x16 acc[4];
#pragma unroll
    for (int b = 0; b < 4; b++) acc[b] = (f32x16){};
    STAMP(0);
    __builtin_amdgcn_s_setprio(B_PRIO);
    const __amdgpu_buffer_rsrc_t wsr = ws_rsrc(p.ws);
    const unsigned ring = lds0 + L_RING + wb * 4096;
    u64 tag;
    if (light && wb >= 2) {
        __builtin_amdgcn_s_setprio(KV_PRIO);
        const int T = 4 * g + lp;
        const __amdgpu_buffer_rsrc_t rk = __builtin_amdgcn_make_buffer_rsrc((void*)(wb == 2 ? p.wk : p.wv), 0, 448 * 56, 0x00020000);
        const unsigned off = (lane >> 2) * 56 + ((lane & 3) == 3 ? 40 : (lane & 3) * 16);
        const unsigned dst = (unsigned)WS_KV + T * 4096 + lo;
        u32x4 R[28];
#pragma unroll
        for (int s = 0; s < 28; s++) R[s] = __builtin_amdgcn_raw_buffer_load_b128(rk, off, s * (16 * 56), 0);
        const unsigned wr = ring + lo, rd = ring + w_lane(lane);
        bf16x8 wf[28];
#pragma unroll
        for (int q = 0; q < 28; q += 4) {
#pragma unroll
            for (int u = 0; u < 4; u++) *(LDS_AS u32x4*)(size_t)(wr + u * 1024) = R[q + u];
#pragma unroll
            for (int u = 0; u < 4; u++) wf[q + u] = lds_tr2(rd + u * 1024, rd + u * 1024 + 256);
            SB;
        }
        STAMP(10);
        x_wait(lds0 + L_XE, 1, -1);
        STAMP(11);
        f32x16 a2 = {};
        {
            unsigned xa[4];
#pragma unroll
            for (int s = 0; s < 4; s++) xa[s] = x_addr(lds0, lane, s) + lp * 4096;
            bf16x8 xf[2][4];
#define KVX(q_) _Pragma("unroll") for (int u = 0; u < 4; u++) xf[(q_) & 1][u] = lds_b128(xa[u] + (q_) * X_SLOT)
            KVX(0); KVX(1);
            if (wb == 2) {
#pragma unroll
                for (int q = 0; q < 7; q++) {
                    SB;
#pragma unroll
                    for (int u = 0; u < 4; u++) { if (u & 1) a2 = MFMA(wf[4 * q + u], xf[q & 1][u], a2); else acc[0] = MFMA(wf[4 * q + u], xf[q & 1][u], acc[0]); }
                    SB;
                    if (q + 2 < 7) { KVX(q + 2); }
                }
            } else {
#pragma unroll
                for (int q = 0; q < 7; q++) {
                    SB;
#pragma unroll
                    for (int u = 0; u < 4; u++) { if (u & 1) a2 = MFMA(xf[q & 1][u], wf[4 * q + u], a2); else acc[0] = MFMA(xf[q & 1][u], wf[4 * q + u], acc[0]); }
                    SB;
                    if (q + 2 < 7) { KVX(q + 2); }
                }
            }
        }
        acc[0] += a2;
        STAMP(13);
        {
            u32x4 a0 = pack8(acc[0], 0), a1 = pack8(acc[0], 1);
            if (wb == 2) {
                if (h == 0) { a1[2] = 0x3f803f80u; a1[3] = 0x00003f80u; }
                st_sc1(wsr, dst, a0); st_sc1(wsr, dst + 1024, a1);
            } else {
                if (l31 == 24) { a0 = (u32x4){0x3f803f80u, 0x3f803f80u, 0x3f803f80u, 0x3f803f80u}; a1 = a0; }
                else if (l31 > 24 && l31 < 28) { a0 = (u32x4){0, 0, 0, 0}; a1 = a0; }
                st_sc1(wsr, dst + 2048, a0); st_sc1(wsr, dst + 3072, a1);
            }
        }
        STAMP(1);
        if (lane == 0) __hip_atomic_fetch_add((LDS_AS unsigned*)(size_t)(lds0 + L_CTR + C_B1), 1u, __ATOMIC_RELAXED, __HIP_MEMORY_SCOPE_WORKGROUP);
#define KVS_AT 1
        if (KVS_AT == 0 && lane == 0) __hip_atomic_fetch_add((LDS_AS unsigned*)(size_t)(lds0 + L_CTR + C_KVS), 1u, __ATOMIC_RELAXED, __HIP_MEMORY_SCOPE_WORKGROUP);
        tag = uniform64(epoch_v) + 1;
        asm volatile("s_waitcnt vmcnt(0)" ::: "memory");
        STAMP(14);
        flag_set(p.ws, WS_FKV + lane * FKV_COPY + T * 16 + (wb & 1) * 8, tag);
        if (KVS_AT == 1 && lane == 0) __hip_atomic_fetch_add((LDS_AS unsigned*)(size_t)(lds0 + L_CTR + C_KVS), 1u, __ATOMIC_RELAXED, __HIP_MEMORY_SCOPE_WORKGROUP);
        STAMP(15);
    } else {
        const __amdgpu_buffer_rsrc_t rw1 = __builtin_amdgcn_make_buffer_rsrc((void*)p.w1, 0, 448 * 3584, 0x00020000);
        const unsigned off = (lane >> 2) * 3584 + fb * 64 + (lane & 3) * 16;
        u32x2 bv[4];
#pragma unroll
        for (int q = 0; q < 4; q++) bv[q] = *(const u32x2*)(p.b1 + fb * 32 + 4 * h + 8 * q);
        auto ld = [&](int s) { return __builtin_amdgcn_raw_buffer_load_b128(rw1, off, s * (16 * 3584), 0); };
        unsigned long long ptb[8];
        proj_loop(acc, lds0, ring, lane, light, ld, ptb);
        STAMP(1);
        if (lane == 0) __hip_atomic_fetch_add((LDS_AS unsigned*)(size_t)(lds0 + L_CTR + C_B1), 1u, __ATOMIC_RELAXED, __HIP_MEMORY_SCOPE_WORKGROUP);
        tag = uniform64(epoch_v) + 1;
        __builtin_amdgcn_s_setprio(G_PRIO);
#pragma unroll
        for (int t = 0; t < 4; t++) gelu_store(p, acc[t], bv, 4 * g + t, fb, lane);
        __builtin_amdgcn_s_setprio(F_PRIO);
        STAMP(12);
        asm volatile("s_waitcnt vmcnt(0)" ::: "memory");
        if (lane < 14) flag_set(p.ws, WS_FH + (g * 14 + lane) * 512 + fb * 8, tag);
        STAMP(3);

    }
    const bool tile = j < 14;
    if (!tile) {
        if (g == 0 && j == 15 && wb == 0) {
#pragma unroll 1
            for (int stage = 0; stage < 3; stage++) {
                const char* base = p.ws + (stage == 0 ? WS_FKV + lane * 8 : stage == 1 ? WS_FH + lane * 8 : WS_FO + (lane >> 4) * (7 * 128) + (lane & 15) * 8);
                const int n = stage == 0 ? 2 : 16;
                const unsigned step = stage == 0 ? 512 : stage == 1 ? 14 * 512 : 4 * 7 * 128;
                const bool on = stage != 1 || lane < 56;
#pragma unroll 1
                for (int spin = 0; spin < SPIN_CAP; spin++) {
                    u64 v[16];
#pragma unroll
                    for (int i = 0; i < 16; i++) v[i] = __hip_atomic_load((const u64*)(base + (i < n ? i : 0) * step), __ATOMIC_RELAXED, __HIP_MEMORY_SCOPE_AGENT);
                    unsigned bad = 0;
#pragma unroll
                    for (int i = 0; i < 16; i++) bad |= (v[i] != tag) ? 1u : 0u;
                    if (__builtin_amdgcn_ballot_w64(on && bad) == 0) break;
                    __builtin_amdgcn_s_sleep(16);
                }
            }
            if (lane == 0) __hip_atomic_store((u64*)(p.ws + WS_EPOCH), tag, __ATOMIC_RELAXED, __HIP_MEMORY_SCOPE_AGENT);
        }
        return;
    }
    const unsigned stg = lds0 + L_WO + wb * WO_SLICE;
    const int th = j & 1, cs = j >> 1, n0 = 64 * cs, T0 = 4 * g + 2 * th;
    const unsigned woff = ((lane & 31) >> 2) * 896 + (lane >> 5) * 64 + (lane & 3) * 16;
    const __amdgpu_buffer_rsrc_t rw2 = __builtin_amdgcn_make_buffer_rsrc((void*)p.w2, 0, 1792 * 896, 0x00020000);
    const unsigned w2v = woff + n0 * 2, w2s = 28 * wb * 16 * 896;
    constexpr int NB = 7;
    u32x4 Wr[NB][2];
#define ROT28(st_) (st_)
#define WLOAD(st_) do { const unsigned so_ = w2s + ROT28(st_) * (16 * 896); Wr[(st_) % NB][0] = __builtin_amdgcn_raw_buffer_load_b128(rw2, w2v, so_, 0); \
        Wr[(st_) % NB][1] = __builtin_amdgcn_raw_buffer_load_b128(rw2, w2v + 8 * 896, so_, 0); } while (0)
#pragma unroll
    for (int st = 0; st < NB; st++) WLOAD(st);
    const int Tm = T0 + (wb & 1);
    const int nc = n0 + 32 * (wb >> 1) + 4 * h;
    u32x2 b2v[4];
#pragma unroll
    for (int rg = 0; rg < 4; rg++) b2v[rg] = *(const u32x2*)(p.b2 + nc + 8 * rg);
    const unsigned fho = WS_FH + (g * 14 + j) * 512 + (14 * wb + lane) * 8;
    flags_wait(p.ws, fho, lane < 6, tag);
    lds_wait_ge(lds0 + L_CTR + C_AP, 4);
    lds_wait_ge(lds0 + L_CTR + C_B1, 4);
    STAMP(4);
#pragma unroll
    for (int i = 0; i < 4; i++) acc[i] = (f32x16){};
    {
        const unsigned wrd = stg + (4 * h + ((lane & 15) >> 2)) * 64 + ((lane >> 4) & 1) * 32 + (lane & 3) * 8;
        const unsigned hv = (unsigned)WS_HF + (T0 * 112 + 28 * wb) * 1024 + lo;
        bf16x8 H0[NB], H1[NB], wf0[2], wf1[2];
#define HLOAD(st_) do { const unsigned so_ = ROT28(st_) * 1024; H0[(st_) % NB] = ld_sc1s(wsr, hv, so_); H1[(st_) % NB] = ld_sc1s(wsr, hv + 112 * 1024, so_); } while (0)
#define WWRITE(st_) do { *(LDS_AS u32x4*)(size_t)(stg + ((st_) & 1) * 2048 + lo) = Wr[(st_) % NB][0]; *(LDS_AS u32x4*)(size_t)(stg + ((st_) & 1) * 2048 + 1024 + lo) = Wr[(st_) % NB][1]; } while (0)
#define WREAD(st_) do { wf0[(st_) & 1] = lds_tr2(wrd + ((st_) & 1) * 2048, wrd + ((st_) & 1) * 2048 + 1024); wf1[(st_) & 1] = lds_tr2(wrd + ((st_) & 1) * 2048 + 512, wrd + ((st_) & 1) * 2048 + 1536); } while (0)
#pragma unroll
        for (int st = 0; st < NB; st++) HLOAD(st);
        WWRITE(0); WLOAD(NB); WWRITE(1); WLOAD(NB + 1);
        WREAD(0);
#pragma unroll
        for (int st = 0; st < 28; st++) {
            SB;
            if (st + 1 < 28) WREAD(st + 1);
            if (st + 2 < 28) WWRITE(st + 2);
            if (st + 2 + NB < 28) WLOAD(st + 2 + NB);
            SB;
            acc[0] = MFMA(wf0[st & 1], H0[st % NB], acc[0]); acc[1] = MFMA(wf0[st & 1], H1[st % NB], acc[1]);
            acc[2] = MFMA(wf1[st & 1], H0[st % NB], acc[2]); acc[3] = MFMA(wf1[st & 1], H1[st % NB], acc[3]);
            SB;
            if (st + NB == 12) {
                u64 fv = tag;
                if (lane >= 6 && lane < 14) fv = __hip_atomic_load((const u64*)(p.ws + fho), __ATOMIC_RELAXED, __HIP_MEMORY_SCOPE_AGENT);
                if (__builtin_amdgcn_ballot_w64(fv != tag) != 0) flags_wait(p.ws, fho, lane >= 6 && lane < 14, tag);
                asm volatile("" ::: "memory");
            }
            if (st + NB < 28) HLOAD(st + NB);
        }
    }
    STAMP(5);
    asm volatile("s_waitcnt lgkmcnt(0)" ::: "memory");
    {
        const char* wos = (const char*)p.wo + n0 * 2;
#pragma unroll
        for (int i = 0; i < 14; i++) glds16(woff, wos + (size_t)(14 * wb + i) * (8 * 896), lds0 + L_WO + (14 * wb + i) * 1024);
    }
    lds_wait_ge(lds0 + L_CTR + C_QR, 4);
    {
        const unsigned rarea = lds0 + L_RED + wb * B_AREA;
#pragma unroll
        for (int i = 0; i < 4; i++) {
            if (i != wb) {
                const unsigned a = rarea + ((i - wb - 1) & 3) * 4096 + lo;
#pragma unroll
                for (int rg = 0; rg < 4; rg++) {
                    f32x4 v = {acc[i][4 * rg], acc[i][4 * rg + 1], acc[i][4 * rg + 2], acc[i][4 * rg + 3]};
                    *(LDS_AS f32x4*)(size_t)(a + rg * 1024) = v;
                }
            }
        }
    }
    f32x16 o = {};
#pragma unroll
    for (int i = 0; i < 4; i++) if (i == wb) o = acc[i];
    asm volatile("s_waitcnt vmcnt(0)" ::: "memory");
    lds_sync4(lds0 + L_CTR + C_RED, 4, lane);
#pragma unroll
    for (int u = 0; u < 4; u++) {
        if (u != wb) {
            const unsigned a = lds0 + L_RED + u * B_AREA + ((wb - u - 1) & 3) * 4096 + lo;
#pragma unroll
            for (int rg = 0; rg < 4; rg++) {
                f32x4 v = *(const LDS_AS f32x4*)(size_t)(a + rg * 1024);
                o[4 * rg] += v[0]; o[4 * rg + 1] += v[1]; o[4 * rg + 2] += v[2]; o[4 * rg + 3] += v[3];
            }
        }
    }
    {
        const float fg = 1.0f - *(const LDS_AS float*)(size_t)(lds0 + L_G + (64 * th + 32 * (wb & 1) + l31) * 4);
#pragma unroll
        for (int rg = 0; rg < 4; rg++) {
            const u32x2 bv = b2v[rg];
            o[4 * rg] = (o[4 * rg] + __uint_as_float(bv[0] << 16)) * fg; o[4 * rg + 1] = (o[4 * rg + 1] + __uint_as_float(bv[0] & 0xffff0000u)) * fg;
            o[4 * rg + 2] = (o[4 * rg + 2] + __uint_as_float(bv[1] << 16)) * fg; o[4 * rg + 3] = (o[4 * rg + 3] + __uint_as_float(bv[1] & 0xffff0000u)) * fg;
        }
    }
    STAMP(6);
    {
        const unsigned ob = (unsigned)WS_OF + Tm * 32 * 1024 + lo;
        const unsigned foo = WS_FO + (Tm * 7 + cs) * 128 + (lane & 15) * 8;
        bf16x8 of[32];
        unsigned ready = 0;
#pragma unroll
        for (int q = 0; q < 4; q++) {
#pragma unroll 1
            for (int spin = 0; spin < SPIN_CAP && ((ready >> (4 * q)) & 15u) != 15u; spin++) {
                if (spin) __builtin_amdgcn_s_sleep(POLL_SLEEP);
                u64 v = 0;
                if (lane < 16) v = __hip_atomic_load((const u64*)(p.ws + foo), __ATOMIC_RELAXED, __HIP_MEMORY_SCOPE_AGENT);
                ready = (unsigned)__builtin_amdgcn_ballot_w64(lane < 16 && v == tag);
            }
            asm volatile("" ::: "memory");
            if (q == 3) STAMP(7);
#pragma unroll
            for (int st = 8 * q; st < 8 * q + 8; st++) of[st] = ld_sc1(wsr, ob + st * 1024);
        }
        const unsigned lp16 = lds0 + L_WO + (wb >> 1) * 512 + ((lane & 15) >> 2) * 64 + ((lane >> 4) & 1) * 32 + (lane & 3) * 8;
        const unsigned wA = lp16 + h * 256, wB = lp16 + h * 768;
        f32x16 o2 = {};
#pragma unroll
        for (int st = 0; st < 32; st++) {
            const int hd = st >> 1, s = st & 1;
            const int r1 = 28 * hd + 16 * s, r2 = 28 * hd + (s ? 20 : 8);
            const unsigned a1 = ((r1 & 4) ? wB : wA) + (r1 >> 3) * 1024 + (r1 & 7) * 64, a2 = ((r2 & 4) ? wB : wA) + (r2 >> 3) * 1024 + (r2 & 7) * 64;
            const bf16x8 w0 = lds_tr2(a1, a2);
            if (st & 1) o2 = MFMA(w0, of[st], o2); else o = MFMA(w0, of[st], o);
        }
        o += o2;
    }
    STAMP(8);
    {
        unsigned short* orow = p.out + (size_t)(Tm * 32 + l31) * D_ + nc;
#pragma unroll
        for (int rg = 0; rg < 4; rg++) {
            u32x2 ov = {cvtpk(o[4 * rg], o[4 * rg + 1]), cvtpk(o[4 * rg + 2], o[4 * rg + 3])};
            *(u32x2*)(orow + 8 * rg) = ov;
        }
    }
    STAMP(9);
}

__global__ void __launch_bounds__(512, 2) gated_mix_fused(Params p) {
    extern __shared__ char smem[];
    const unsigned lds0 = (unsigned)(size_t)smem;
    const int b = blockIdx.x, xcd = b & 7, idx = b >> 3;
    const int g = xcd * 2 + (idx >> 4), j = idx & 15;
    const int w = __builtin_amdgcn_readfirstlane(threadIdx.x >> 6), lane = threadIdx.x & 63;
    const u64 epoch = __hip_atomic_load((const u64*)(p.ws + WS_EPOCH), __ATOMIC_RELAXED, __HIP_MEMORY_SCOPE_AGENT);
    if (w == 0 && lane < 16) *(LDS_AS unsigned*)(size_t)(lds0 + L_CTR + 4 * lane) = 0;
    wait_bar();
    if (w < 4) a_wave(p, lds0, g, j, w, lane, epoch);
    else b_wave(p, lds0, g, j, w - 4, lane, epoch);
}

extern "C" void kernel_launch(void* const* d_in, const int* in_sizes, int n_in, void* d_out, int out_size, void* d_ws, size_t ws_size, hipStream_t stream) {
    Params p;
    p.x = (const unsigned short*)d_in[0]; p.wq = (const unsigned short*)d_in[1]; p.wk = (const unsigned short*)d_in[2];
    p.wv = (const unsigned short*)d_in[3]; p.wo = (const unsigned short*)d_in[4]; p.w1 = (const unsigned short*)d_in[6];
    p.b1 = (const unsigned short*)d_in[7]; p.w2 = (const unsigned short*)d_in[8]; p.b2 = (const unsigned short*)d_in[9];
    p.gw = (const unsigned short*)d_in[10]; p.out = (unsigned short*)d_out; p.ws = (char*)d_ws;
    (void)hipFuncSetAttribute((const void*)gated_mix_fused, hipFuncAttributeMaxDynamicSharedMemorySize, LDS_BYTES);
    gated_mix_fused<<<256, 512, LDS_BYTES, stream>>>(p);
}
```

```cpp
#define LFFN_GATE 1
#include <hip/hip_runtime.h>
#include <hip/hip_bf16.h>

typedef short bf16x8 __attribute__((ext_vector_type(8)));
typedef short s16x4 __attribute__((ext_vector_type(4)));
typedef __bf16 bf16x2 __attribute__((ext_vector_type(2)));
typedef float f32x16 __attribute__((ext_vector_type(16)));
typedef float f32x4 __attribute__((ext_vector_type(4)));
typedef float f32x2 __attribute__((ext_vector_type(2)));
typedef unsigned u32x4 __attribute__((ext_vector_type(4)));
typedef unsigned u32x2 __attribute__((ext_vector_type(2)));
typedef short v4i16_t __attribute__((ext_vector_type(4)));
typedef unsigned long long u64;
#define LDS_AS __attribute__((address_space(3)))
#define DI __device__ __forceinline__

constexpr int S_ = 2048, D_ = 448, F_ = 1792;
constexpr size_t WS_EPOCH = 0;
constexpr size_t WS_FKV = 65536, FKV_COPY = 1280;
constexpr size_t WS_FH = 163840;
constexpr size_t WS_FO = 294912;
constexpr size_t WS_KV = 1u << 20;
constexpr size_t WS_OF = 2u << 20;
constexpr size_t WS_HF = 4u << 20;

#define STAMP(i) do {} while (0)
#define MFMA(a, b, c) __builtin_amdgcn_mfma_f32_32x32x16_bf16(a, b, c, 0, 0, 0)
#define SB __builtin_amdgcn_sched_barrier(0)
#define EX2(x) __builtin_amdgcn_exp2f(x)
constexpr int SPIN_CAP = 1 << 21;
#define POLL_SLEEP 2
#define G_PRIO 3
#define F_PRIO 0
#define KV_PRIO 3
#define A1_PRIO 2
#define A2_PRIO 0
#define B_PRIO 2

DI unsigned cvtpk(float lo, float hi) { f32x2 v = {lo, hi}; return __builtin_bit_cast(unsigned, __builtin_convertvector(v, bf16x2)); }
DI void glds16(unsigned voff, const void* sbase, unsigned lds_dst) {
    unsigned keep;
    asm volatile("s_mov_b32 %0, m0\n\ts_mov_b32 m0, %3\n\ts_nop 0\n\tglobal_load_lds_dwordx4 %1, %2\n\ts_mov_b32 m0, %0"
                 : "=&s"(keep) : "v"(voff), "s"(sbase), "s"(lds_dst) : "memory");
}
DI void glds16_sc1(unsigned voff, const void* sbase, unsigned lds_dst) {
    unsigned keep;
    asm volatile("s_mov_b32 %0, m0\n\ts_mov_b32 m0, %3\n\ts_nop 0\n\tglobal_load_lds_dwordx4 %1, %2 sc1\n\ts_mov_b32 m0, %0"
                 : "=&s"(keep) : "v"(voff), "s"(sbase), "s"(lds_dst) : "memory");
}
DI bf16x8 lds_b128(unsigned a) { return *(const LDS_AS bf16x8*)(size_t)a; }
DI s16x4 lds_tr(unsigned a) { return __builtin_bit_cast(s16x4, __builtin_amdgcn_ds_read_tr16_b64_v4i16((LDS_AS v4i16_t*)(size_t)a)); }
DI bf16x8 lds_tr2(unsigned a0, unsigned a1) { s16x4 lo = lds_tr(a0), hi = lds_tr(a1); return __builtin_shufflevector(lo, hi, 0, 1, 2, 3, 4, 5, 6, 7); }
DI float dot2(unsigned a, unsigned b, float c) { return __builtin_amdgcn_fdot2_f32_bf16(__builtin_bit_cast(bf16x2, a), __builtin_bit_cast(bf16x2, b), c, false); }
DI u32x4 pack8(const f32x16& a, int s) {
    u32x4 r;
    r[0] = cvtpk(a[8 * s + 0], a[8 * s + 1]); r[1] = cvtpk(a[8 * s + 2], a[8 * s + 3]);
    r[2] = cvtpk(a[8 * s + 4], a[8 * s + 5]); r[3] = cvtpk(a[8 * s + 6], a[8 * s + 7]);
    return r;
}
DI float gelu_tanh(float x) {
    const float c1 = -2.0f * 1.4426950408889634f * 0.7978845608028654f, c2 = c1 * 0.044715f;
    float t = x * x;
    float e = EX2(x * __builtin_fmaf(t, c2, c1));
    return x * __builtin_amdgcn_rcpf(1.0f + e);
}
DI __amdgpu_buffer_rsrc_t ws_rsrc(const char* ws) { return __builtin_amdgcn_make_buffer_rsrc((void*)ws, 0, 16u << 20, 0x00020000); }
DI void st_sc1(__amdgpu_buffer_rsrc_t r, unsigned off, u32x4 v) { __builtin_amdgcn_raw_buffer_store_b128(v, r, off, 0, 16); }
DI void flag_set(char* ws, size_t off, u64 tag) { __hip_atomic_store((u64*)(ws + off), tag, __ATOMIC_RELAXED, __HIP_MEMORY_SCOPE_AGENT); }
DI void flags_wait(const char* ws, unsigned off, bool on, u64 tag) {
#pragma unroll 1
    for (int spin = 0; spin < SPIN_CAP; spin++) {
        u64 v = tag;
        if (on) v = __hip_atomic_load((const u64*)(ws + off), __ATOMIC_RELAXED, __HIP_MEMORY_SCOPE_AGENT);
        if (__builtin_amdgcn_ballot_w64(v != tag) == 0) break;
        __builtin_amdgcn_s_sleep(POLL_SLEEP);
    }
    asm volatile("s_waitcnt vmcnt(0)" ::: "memory");
}
DI u64 uniform64(u64 v) {
    asm volatile("" : "+v"(v) :: "memory");
    const unsigned lo = __builtin_amdgcn_readfirstlane((unsigned)v), hi = __builtin_amdgcn_readfirstlane((unsigned)(v >> 32));
    return ((u64)hi << 32) | lo;
}
DI unsigned lds_ld(unsigned a) { return __hip_atomic_load((LDS_AS unsigned*)(size_t)a, __ATOMIC_RELAXED, __HIP_MEMORY_SCOPE_WORKGROUP); }
DI void lds_st(unsigned a, unsigned v) { __hip_atomic_store((LDS_AS unsigned*)(size_t)a, v, __ATOMIC_RELAXED, __HIP_MEMORY_SCOPE_WORKGROUP); }
DI void lds_sync4(unsigned ctr, unsigned target, int lane) {
    asm volatile("s_waitcnt lgkmcnt(0)" ::: "memory");
    LDS_AS unsigned* c = (LDS_AS unsigned*)(size_t)ctr;
    if (lane == 0) __hip_atomic_fetch_add(c, 1u, __ATOMIC_RELAXED, __HIP_MEMORY_SCOPE_WORKGROUP);
#pragma unroll 1
    for (int spin = 0; spin < SPIN_CAP; spin++) {
        unsigned v = __builtin_amdgcn_readfirstlane(__hip_atomic_load(c, __ATOMIC_RELAXED, __HIP_MEMORY_SCOPE_WORKGROUP));
        if (v >= target) break;
        __builtin_amdgcn_s_sleep(1);
    }
    asm volatile("" ::: "memory");
}

struct Params {
    const unsigned short *x, *wq, *wk, *wv, *wo, *w1, *b1, *w2, *b2, *gw;
    unsigned short* out;
    char* ws;
};

constexpr unsigned X_SLOT = 16384, L_WQ = 114688, L_RING = 143360, L_QX = L_WQ;
constexpr unsigned L_WO = 0, WO_SLICE = 14336, L_MRG = 57344, L_RED = 106496, B_AREA = 12288;
constexpr unsigned P1_GW = 159744, L_G = P1_GW + 1024;
constexpr unsigned L_CTR = L_G + 512;
constexpr unsigned C_QW = 0, C_QR = 4, C_OW = 8, C_RED = 12, C_AP = 16, C_B1 = 20, C_KVS = 24, L_XF = L_CTR + 32, L_XE = L_CTR + 48;
constexpr unsigned LDS_BYTES = L_CTR + 64;
static_assert(L_RING + 4 * 4096 <= P1_GW && L_RED + 4 * B_AREA <= P1_GW && L_MRG + 12 * 4096 <= L_RED && LDS_BYTES <= 163840 && (L_XF & 15) == 0, "LDS map");

template <int N> DI void wait_vm() { asm volatile("s_waitcnt vmcnt(%0)" ::"n"(N) : "memory"); }
DI void wait_bar() { asm volatile("s_waitcnt lgkmcnt(0)\n\ts_barrier" ::: "memory"); }
DI u32x4 lds_flags(unsigned a) { u32x4 v; asm volatile("ds_read_b128 %0, %1" : "=v"(v) : "v"(a) : "memory"); return v; }
DI void lds_wait_ge(unsigned a, unsigned need) {
#pragma unroll 1
    for (int spin = 0; spin < SPIN_CAP; spin++) {
        if ((unsigned)__builtin_amdgcn_readfirstlane(lds_ld(a)) >= need) break;
        __builtin_amdgcn_s_sleep(1);
    }
    asm volatile("" ::: "memory");
}
DI void x_wait(unsigned xf, unsigned need, int only) {
#pragma unroll 1
    for (int spin = 0; spin < SPIN_CAP; spin++) {
        unsigned m;
        if (only < 0) {
            const unsigned a = lds_ld(xf), b = lds_ld(xf + 4), c = lds_ld(xf + 8), d = lds_ld(xf + 12);
            m = min(min(a, b), min(c, d));
        } else m = lds_ld(xf + 4 * only);
        if ((unsigned)__builtin_amdgcn_readfirstlane(m) >= need) break;
        __builtin_amdgcn_s_sleep(1);
    }
    asm volatile("" ::: "memory");
}
DI unsigned x_addr(unsigned lds0, int lane, int s) {
    const int h = lane >> 5, l31 = lane & 31;
    return lds0 + l31 * 128 + (((2 * s + h) ^ ((l31 >> 1) & 7)) << 4);
}
DI unsigned w_lane(int lane) { return (lane >> 5) * 512 + ((lane & 15) >> 2) * 64 + ((lane >> 4) & 1) * 32 + (lane & 3) * 8; }
#define NR1 8
template <typename Ld>
DI void proj_loop(f32x16 (&acc)[4], unsigned lds0, unsigned ring, int lane, bool light, Ld&& ld, unsigned long long* ptb) {
    constexpr int NR = NR1;
    u32x4 R[NR];
#define W1_DELAY 3
    if (light) { if (LFFN_GATE) lds_wait_ge(lds0 + L_CTR + C_KVS, 2); else x_wait(lds0 + L_XE, 1, -1); }
    else if (W1_DELAY > 0) x_wait(lds0 + L_XF, W1_DELAY, -1);
#pragma unroll
    for (int s = 0; s < NR; s++) R[s] = ld(s);
    unsigned xa[4];
#pragma unroll
    for (int s = 0; s < 4; s++) xa[s] = x_addr(lds0, lane, s);
    const unsigned wr = ring + lane * 16, rd = ring + w_lane(lane), xf = lds0 + L_XF;
    bf16x8 xf_[3][4], wf[3];
#define PWRITE(s_) do { *(LDS_AS u32x4*)(size_t)(wr + ((s_) & 3) * 1024) = R[(s_) % NR]; if ((s_) + NR < 28) R[(s_) % NR] = ld((s_) + NR); } while (0)
#define PREADW(s_) do { wf[(s_) % 3] = lds_tr2(rd + ((s_) & 3) * 1024, rd + ((s_) & 3) * 1024 + 256); } while (0)
#define PREADX(s_, t_) do { xf_[(s_) % 3][t_] = lds_b128(xa[(s_) & 3] + ((s_) >> 2) * X_SLOT + (t_) * 4096); } while (0)
    PWRITE(0); PWRITE(1); PWRITE(2);
    x_wait(xf, 1, -1);
    PREADW(0); PREADX(0, 0); PREADX(0, 1); PREADX(0, 2); PREADX(0, 3);
    PREADW(1); PREADX(1, 0); PREADX(1, 1); PREADX(1, 2); PREADX(1, 3);
#pragma unroll
    for (int s = 0; s < 28; s++) {
        SB;
        if ((s & 3) == 2 && s + 2 < 28) x_wait(xf, (s + 2) / 4 + 1, -1);
        acc[0] = MFMA(wf[s % 3], xf_[s % 3][0], acc[0]); SB;
        if (s + 2 < 28) { PREADW(s + 2); PREADX(s + 2, 0); } SB;
        acc[1] = MFMA(wf[s % 3], xf_[s % 3][1], acc[1]); SB;
        if (s + 2 < 28) { PREADX(s + 2, 1); PREADX(s + 2, 2); } SB;
        acc[2] = MFMA(wf[s % 3], xf_[s % 3][2], acc[2]); SB;
        if (s + 2 < 28) PREADX(s + 2, 3);
        if (s + 3 < 28) PWRITE(s + 3);
        SB;
        acc[3] = MFMA(wf[s % 3], xf_[s % 3][3], acc[3]);
    }
    SB;
#undef PWRITE
#undef PREADW
#undef PREADX
}
DI void gelu_store(const Params& p, const f32x16& acc, const u32x2 (&bv)[4], int T, int fb, int lane) {
    f32x16 a;
#pragma unroll
    for (int q = 0; q < 4; q++) {
        a[4 * q + 0] = acc[4 * q + 0] + __uint_as_float(bv[q][0] << 16); a[4 * q + 1] = acc[4 * q + 1] + __uint_as_float(bv[q][0] & 0xffff0000u);
        a[4 * q + 2] = acc[4 * q + 2] + __uint_as_float(bv[q][1] << 16); a[4 * q + 3] = acc[4 * q + 3] + __uint_as_float(bv[q][1] & 0xffff0000u);
    }
#pragma unroll
    for (int r = 0; r < 16; r++) a[r] = gelu_tanh(a[r]);
    const unsigned dst = (unsigned)WS_HF + (T * 112 + 2 * fb) * 1024 + lane * 16;
    const __amdgpu_buffer_rsrc_t r_ = ws_rsrc(p.ws);
    st_sc1(r_, dst, pack8(a, 0)); st_sc1(r_, dst + 1024, pack8(a, 1));
}

DI float max16(const f32x16& s) {
    float a = __builtin_fmaxf(__builtin_fmaxf(s[0], s[1]), s[2]), b = __builtin_fmaxf(__builtin_fmaxf(s[3], s[4]), s[5]);
#pragma unroll
    for (int r = 6; r < 14; r += 4) { a = __builtin_fmaxf(__builtin_fmaxf(a, s[r]), s[r + 1]); b = __builtin_fmaxf(__builtin_fmaxf(b, s[r + 2]), s[r + 3]); }
    a = __builtin_fmaxf(__builtin_fmaxf(a, s[14]), s[15]);
    return __builtin_fmaxf(a, b);
}
DI void set_max(bf16x8& q1, float m, int h) {
    auto rr = __builtin_amdgcn_permlane32_swap(__float_as_uint(m), __float_as_uint(m), false, false);
    const float nm = -__builtin_fmaxf(__uint_as_float(rr[0]), __uint_as_float(rr[1]));
    const unsigned a = cvtpk(nm, 0.f) & 0xffffu;
    const float r1 = nm - __uint_as_float(a << 16);
    const unsigned b = cvtpk(r1, 0.f) & 0xffffu;
    const float r2 = r1 - __uint_as_float(b << 16);
    const unsigned c = cvtpk(r2, 0.f) & 0xffffu;
    u32x4 t = __builtin_bit_cast(u32x4, q1);
    if (h == 0) { t[2] = a | (b << 16); t[3] = c; }
    q1 = __builtin_bit_cast(bf16x8, t);
}
DI bf16x8 ld_sc1s(__amdgpu_buffer_rsrc_t r, unsigned off, unsigned soff) { return __builtin_bit_cast(bf16x8, __builtin_amdgcn_raw_buffer_load_b128(r, off, soff, 16)); }
DI bf16x8 ld_sc1(__amdgpu_buffer_rsrc_t r, unsigned off) { return __builtin_bit_cast(bf16x8, __builtin_amdgcn_raw_buffer_load_b128(r, off, 0, 16)); }

DI void a_wave(const Params& p, unsigned lds0, int g, int j, int w, int lane, u64 epoch_v) {
    const int h = lane >> 5, l31 = lane & 31;
    const int T = 4 * g + w;
    f32x16 accq = {};
    STAMP(0);
    __builtin_amdgcn_s_setprio(A1_PRIO);
    unsigned xoff[4];
#pragma unroll
    for (int i = 0; i < 4; i++) {
        const int row = 32 * w + 8 * i + (lane >> 3);
        const int c = (lane & 7) ^ ((row >> 1) & 7);
        xoff[i] = row * 896 + c * 16;
    }
    const char* xb = (const char*)p.x + (size_t)g * 128 * 896;
    const unsigned qoff = (16 * w + (lane >> 2)) * 896 + ((lane & 3) == 3 ? 40 : (lane & 3) * 16);
    const char* qb_ = (const char*)p.wq + j * 56;
    glds16((lane < 56 ? lane : 55) * 16, p.gw, lds0 + P1_GW);
    auto dma = [&](int c) {
#pragma unroll
        for (int i = 0; i < 4; i++) glds16(xoff[i], xb + c * 128, lds0 + c * X_SLOT + (32 * w + 8 * i) * 128);
        glds16(qoff, qb_ + c * (64 * 896), lds0 + L_WQ + c * 4096 + w * 1024);
    };
    const bool light = j >= 12;
    if (light) {
        const int row = 32 * (j - 12) + 8 * w + (lane >> 3);
        const unsigned eo = row * 896 + (((lane & 7) ^ ((row >> 1) & 7)) << 4);
#pragma unroll
        for (int c = 0; c < 7; c++) glds16(eo, xb + c * 128, lds0 + c * X_SLOT + (32 * (j - 12) + 8 * w) * 128);
    }
    if (light) {
        dma(0);
        wait_vm<5>();
        if (lane == 0) lds_st(lds0 + L_XE + 4 * w, 1);
    } else dma(0);
    dma(1); dma(2);
    u64 tag;
    const __amdgpu_buffer_rsrc_t wsr = ws_rsrc(p.ws);
    const int t0 = (4 * g + 4 * j + 16 * w) & 63;
    bf16x8 K0[4], K1[4], V0[4], V1[4];
    const unsigned kvb = (unsigned)WS_KV + lane * 16;
#define KVLOAD(set, vo) do { const unsigned o_ = (vo); K0[set] = ld_sc1(wsr, o_); K1[set] = ld_sc1(wsr, o_ + 1024); V0[set] = ld_sc1(wsr, o_ + 2048); V1[set] = ld_sc1(wsr, o_ + 3072); } while (0)
    {
        unsigned xa[4];
#pragma unroll
        for (int s = 0; s < 4; s++) xa[s] = x_addr(lds0, lane, s) + w * 4096;
        const unsigned wa = lds0 + L_WQ + w_lane(lane);
        unsigned short gwv[7];
        auto publish = [&](int c) {
            if (lane < 16) *(LDS_AS unsigned short*)(size_t)(lds0 + L_WQ + c * 4096 + w * 1024 + lane * 64 + 48) = gwv[c];
            asm volatile("s_waitcnt lgkmcnt(0)" ::: "memory");
            if (lane == 0) lds_st(lds0 + L_XF + 4 * w, c + 1);
        };
        auto qchunk = [&](int c) {
            bf16x8 xf[4], wf[4];
#pragma unroll
            for (int s = 0; s < 4; s++) { xf[s] = lds_b128(xa[s] + c * X_SLOT); wf[s] = lds_tr2(wa + c * 4096 + s * 1024, wa + c * 4096 + s * 1024 + 256); }
#pragma unroll
            for (int s = 0; s < 4; s++) accq = MFMA(wf[s], xf[s], accq);
        };
#define TC(c)
        wait_vm<10>(); TC(0);
#pragma unroll
        for (int c = 0; c < 7; c++) gwv[c] = *(const LDS_AS unsigned short*)(size_t)(lds0 + P1_GW + (64 * c + 16 * w + (lane & 15)) * 2);
        publish(0);
        if (!light) { dma(3); wait_vm<10>(); } else wait_vm<5>();
        TC(1); publish(1);
        if (!light) { dma(4); wait_vm<10>(); } else wait_vm<0>();
        TC(2); publish(2);
        if (light) {
            lds_wait_ge(lds0 + L_CTR + C_KVS, 2);
            dma(3); dma(4);
        }
        dma(5);
        wait_vm<10>(); TC(3); publish(3); dma(6);
        wait_vm<10>(); TC(4); publish(4);
        x_wait(lds0 + L_XF, 2, -1); qchunk(0); qchunk(1); SB;
        wait_vm<5>(); TC(5); publish(5);
        x_wait(lds0 + L_XF, 4, -1); qchunk(2); qchunk(3); SB;
        wait_vm<0>(); TC(6); publish(6);
        tag = uniform64(epoch_v) + 1;
        const unsigned fo = WS_FKV + (g * 4 + (j >> 2)) * FKV_COPY + ((t0 + (lane >> 1)) & 63) * 16 + (lane & 1) * 8;
        u64 fv = tag;
        if (lane < 32) fv = __hip_atomic_load((const u64*)(p.ws + fo), __ATOMIC_RELAXED, __HIP_MEMORY_SCOPE_AGENT);
        x_wait(lds0 + L_XF, 6, -1); TC(8); qchunk(4); qchunk(5); SB; TC(9);
        x_wait(lds0 + L_XF, 7, -1); TC(11); qchunk(6); SB; TC(12);
        asm volatile("" : "+v"(fv) :: "memory");
        if (__builtin_amdgcn_ballot_w64(fv != tag) != 0) flags_wait(p.ws, fo, lane < 32, tag);
        TC(10);
        KVLOAD(0, kvb + t0 * 4096); KVLOAD(1, kvb + (t0 + 1) * 4096); KVLOAD(2, kvb + (t0 + 2) * 4096);
        SB;
    }
    const float zz = __uint_as_float(__builtin_amdgcn_permlane32_swap(__float_as_uint(accq[12]), __float_as_uint(accq[12]), false, false)[0]);
    const float gtok = __builtin_amdgcn_rcpf(1.0f + EX2(-1.4426950408889634f * zz));
    if (h == 0) *(LDS_AS float*)(size_t)(lds0 + L_G + (32 * w + l31) * 4) = gtok;
    bf16x8 q0[4], q1[4];
    bf16x8 qa, qb;
    {
        const float qs = 1.4426950408889634f * 0.1889822365046136f;
        f32x16 q = accq * qs;
        u32x4 a0 = pack8(q, 0), a1 = pack8(q, 1);
        if (h == 0) { a1[2] = 0; a1[3] = 0; }
        qa = __builtin_bit_cast(bf16x8, a0); qb = __builtin_bit_cast(bf16x8, a1);
    }
    asm volatile("s_waitcnt lgkmcnt(0)" ::: "memory");
    if (lane == 0) __hip_atomic_fetch_add((LDS_AS unsigned*)(size_t)(lds0 + L_CTR + C_AP), 1u, __ATOMIC_RELAXED, __HIP_MEMORY_SCOPE_WORKGROUP);
    const f32x16 zero = {};
    V1[3] = (bf16x8){}; K0[3] = V1[3]; K1[3] = V1[3]; V0[3] = V1[3];
    f32x16 S = MFMA(K0[0], qa, zero); S = MFMA(K1[0], qb, S);
    set_max(qb, max16(S), h);
    const unsigned lo = lane * 16;
    *(LDS_AS bf16x8*)(size_t)(lds0 + L_QX + w * 2048 + lo) = qa;
    *(LDS_AS bf16x8*)(size_t)(lds0 + L_QX + w * 2048 + 1024 + lo) = qb;
    lds_sync4(lds0 + L_CTR + C_QW, 4, lane);
#pragma unroll
    for (int b = 0; b < 4; b++) { q0[b] = lds_b128(lds0 + L_QX + b * 2048 + lo); q1[b] = lds_b128(lds0 + L_QX + b * 2048 + 1024 + lo); }
    asm volatile("s_waitcnt lgkmcnt(0)" ::: "memory");
    if (lane == 0) __hip_atomic_fetch_add((LDS_AS unsigned*)(size_t)(lds0 + L_CTR + C_QR), 1u, __ATOMIC_RELAXED, __HIP_MEMORY_SCOPE_WORKGROUP);
    TC(13);
    __builtin_amdgcn_s_setprio(A2_PRIO);
    S = MFMA(K0[0], q0[0], zero); S = MFMA(K1[0], q1[0], S);
    f32x16 O[4];
#pragma unroll
    for (int b = 0; b < 4; b++) O[b] = zero;
    u32x4 P1 = {0, 0, 0, 0};
#define ASTEP(c, b) do { \
        constexpr int pc = (b) == 0 ? ((c) + 3) & 3 : (c), pb = ((b) + 3) & 3, nc = (b) == 3 ? ((c) + 1) & 3 : (c), nb = ((b) + 1) & 3; \
        O[pb] = MFMA(V1[pc], __builtin_bit_cast(bf16x8, P1), O[pb]); SB; \
        u32x4 p0, p1; \
        p0[0] = cvtpk(EX2(S[0]), EX2(S[1])); p0[1] = cvtpk(EX2(S[2]), EX2(S[3])); SB; \
        f32x16 Sn = MFMA(K0[nc], q0[nb], zero); SB; \
        if ((b) == 1) { KVLOAD(pc3(c), vnext); } \
        p0[2] = cvtpk(EX2(S[4]), EX2(S[5])); p0[3] = cvtpk(EX2(S[6]), EX2(S[7])); SB; \
        Sn = MFMA(K1[nc], q1[nb], Sn); SB; \
        p1[0] = cvtpk(EX2(S[8]), EX2(S[9])); p1[1] = cvtpk(EX2(S[10]), EX2(S[11])); SB; \
        O[b] = MFMA(V0[c], __builtin_bit_cast(bf16x8, p0), O[b]); SB; \
        p1[2] = cvtpk(EX2(S[12]), EX2(S[13])); p1[3] = cvtpk(EX2(S[14]), EX2(S[15])); SB; \
        P1 = p1; S = Sn; } while (0)
#define pc3(c) (((c) + 3) & 3)
#define ATILE(c) do { \
        const int tn = tt + (c) + 3;     \
        const unsigned vnext = lo + (tn < 16 ? (unsigned)WS_KV + ((t0 + tn) & 63) * 4096u : 0x7f000000u); \
        ASTEP(c, 0); ASTEP(c, 1); ASTEP(c, 2); ASTEP(c, 3); } while (0)
#pragma unroll 1
    for (int tt = 0; tt < 16; tt += 4) {
        ATILE(0); ATILE(1); ATILE(2); ATILE(3);
    }
    O[3] = MFMA(V1[3], __builtin_bit_cast(bf16x8, P1), O[3]);
    STAMP(5);
    lds_wait_ge(lds0 + L_CTR + C_B1, 4);
#pragma unroll
    for (int b = 0; b < 4; b++) {
        if (b != w) {
            const unsigned a = lds0 + L_MRG + (b * 3 + ((w - b - 1) & 3)) * 4096 + lo;
#pragma unroll
            for (int rg = 0; rg < 4; rg++) {
                f32x4 v = {O[b][4 * rg], O[b][4 * rg + 1], O[b][4 * rg + 2], O[b][4 * rg + 3]};
                *(LDS_AS f32x4*)(size_t)(a + rg * 1024) = v;
            }
        }
    }
    f32x16 o = zero;
#pragma unroll
    for (int b = 0; b < 4; b++) if (b == w) { o = O[b]; qa = q0[b]; qb = q1[b]; }
    lds_sync4(lds0 + L_CTR + C_OW, 4, lane);
#pragma unroll
    for (int u = 0; u < 3; u++) {
        const unsigned a = lds0 + L_MRG + (w * 3 + u) * 4096 + lo;
#pragma unroll
        for (int rg = 0; rg < 4; rg++) {
            f32x4 v = *(const LDS_AS f32x4*)(size_t)(a + rg * 1024);
            o[4 * rg] += v[0]; o[4 * rg + 1] += v[1]; o[4 * rg + 2] += v[2]; o[4 * rg + 3] += v[3];
        }
    }
    STAMP(6);
    auto rr = __builtin_amdgcn_permlane32_swap(__float_as_uint(o[12]), __float_as_uint(o[12]), false, false);
    float l = __uint_as_float(rr[0]);
    {
        bool ok = l < 1.152921504606846976e18f && l > 8.673617379884035e-19f;
#pragma unroll
        for (int r = 0; r < 16; r++) ok = ok && (__builtin_fabsf(o[r]) < 3.0e38f);
        if (__builtin_amdgcn_ballot_w64(!ok) != 0) {
            u32x4 tq = __builtin_bit_cast(u32x4, qb);
            if (h == 0) { tq[2] = 0; tq[3] = 0; }
            qb = __builtin_bit_cast(bf16x8, tq);
            float m = -INFINITY;
#pragma unroll 1
            for (int t = 0; t < 64; t++) {
                bf16x8 k0 = ld_sc1(wsr, kvb + t * 4096), k1 = ld_sc1(wsr, kvb + t * 4096 + 1024);
                f32x16 s = MFMA(k0, qa, zero); s = MFMA(k1, qb, s);
                m = __builtin_fmaxf(m, max16(s));
            }
            set_max(qb, m, h);
            o = zero;
#pragma unroll 1
            for (int t = 0; t < 64; t++) {
                bf16x8 k0 = ld_sc1(wsr, kvb + t * 4096), k1 = ld_sc1(wsr, kvb + t * 4096 + 1024);
                bf16x8 v0 = ld_sc1(wsr, kvb + t * 4096 + 2048), v1 = ld_sc1(wsr, kvb + t * 4096 + 3072);
                f32x16 s = MFMA(k0, qa, zero); s = MFMA(k1, qb, s);
#pragma unroll
                for (int r = 0; r < 16; r++) s[r] = EX2(s[r]);
                o = MFMA(v0, __builtin_bit_cast(bf16x8, pack8(s, 0)), o); o = MFMA(v1, __builtin_bit_cast(bf16x8, pack8(s, 1)), o);
            }
            auto r2 = __builtin_amdgcn_permlane32_swap(__float_as_uint(o[12]), __float_as_uint(o[12]), false, false);
            l = __uint_as_float(r2[0]);
        }
    }
    const float sc = gtok * __builtin_amdgcn_rcpf(l);
#pragma unroll
    for (int r = 0; r < 16; r++) o[r] *= sc;
    u32x4 o0 = pack8(o, 0), o1 = pack8(o, 1);
    if (h == 0) { o1[2] = 0; o1[3] = 0; }
    const unsigned dst = (unsigned)WS_OF + (T * 32 + 2 * j) * 1024 + lo;
    st_sc1(wsr, dst, o0); st_sc1(wsr, dst + 1024, o1);
    asm volatile("s_waitcnt vmcnt(0)" ::: "memory");
    if (lane < 7) flag_set(p.ws, WS_FO + (T * 7 + lane) * 128 + j * 8, tag);
    STAMP(7);
}

DI void b_wave(const Params& p, unsigned lds0, int g, int j, int wb, int lane, u64 epoch_v) {
    const int h = lane >> 5, l31 = lane & 31;
    const bool light = j >= 12;
    const int lp = j - 12;
    const int fb = light ? 48 + 2 * lp + wb : 4 * j + wb;
    const unsigned lo = lane * 16;
    f32x16 acc[4];
#pragma unroll
    for (int b = 0; b < 4; b++) acc[b] = (f32x16){};
    STAMP(0);
    __builtin_amdgcn_s_setprio(B_PRIO);
    const __amdgpu_buffer_rsrc_t wsr = ws_rsrc(p.ws);
    const unsigned ring = lds0 + L_RING + wb * 4096;
    u64 tag;
    if (light && wb >= 2) {
        __builtin_amdgcn_s_setprio(KV_PRIO);
        const int T = 4 * g + lp;
        const __amdgpu_buffer_rsrc_t rk = __builtin_amdgcn_make_buffer_rsrc((void*)(wb == 2 ? p.wk : p.wv), 0, 448 * 56, 0x00020000);
        const unsigned off = (lane >> 2) * 56 + ((lane & 3) == 3 ? 40 : (lane & 3) * 16);
        const unsigned dst = (unsigned)WS_KV + T * 4096 + lo;
        u32x4 R[28];
#pragma unroll
        for (int s = 0; s < 28; s++) R[s] = __builtin_amdgcn_raw_buffer_load_b128(rk, off, s * (16 * 56), 0);
        const unsigned wr = ring + lo, rd = ring + w_lane(lane);
        bf16x8 wf[28];
#pragma unroll
        for (int q = 0; q < 28; q += 4) {
#pragma unroll
            for (int u = 0; u < 4; u++) *(LDS_AS u32x4*)(size_t)(wr + u * 1024) = R[q + u];
#pragma unroll
            for (int u = 0; u < 4; u++) wf[q + u] = lds_tr2(rd + u * 1024, rd + u * 1024 + 256);
            SB;
        }
        STAMP(10);
        x_wait(lds0 + L_XE, 1, -1);
        STAMP(11);
        f32x16 a2 = {};
        {
            unsigned xa[4];
#pragma unroll
            for (int s = 0; s < 4; s++) xa[s] = x_addr(lds0, lane, s) + lp * 4096;
            bf16x8 xf[2][4];
#define KVX(q_) _Pragma("unroll") for (int u = 0; u < 4; u++) xf[(q_) & 1][u] = lds_b128(xa[u] + (q_) * X_SLOT)
            KVX(0); KVX(1);
            if (wb == 2) {
#pragma unroll
                for (int q = 0; q < 7; q++) {
                    SB;
#pragma unroll
                    for (int u = 0; u < 4; u++) { if (u & 1) a2 = MFMA(wf[4 * q + u], xf[q & 1][u], a2); else acc[0] = MFMA(wf[4 * q + u], xf[q & 1][u], acc[0]); }
                    SB;
                    if (q + 2 < 7) { KVX(q + 2); }
                }
            } else {
#pragma unroll
                for (int q = 0; q < 7; q++) {
                    SB;
#pragma unroll
                    for (int u = 0; u < 4; u++) { if (u & 1) a2 = MFMA(xf[q & 1][u], wf[4 * q + u], a2); else acc[0] = MFMA(xf[q & 1][u], wf[4 * q + u], acc[0]); }
                    SB;
                    if (q + 2 < 7) { KVX(q + 2); }
                }
            }
        }
        acc[0] += a2;
        STAMP(13);
        {
            u32x4 a0 = pack8(acc[0], 0), a1 = pack8(acc[0], 1);
            if (wb == 2) {
                if (h == 0) { a1[2] = 0x3f803f80u; a1[3] = 0x00003f80u; }
                st_sc1(wsr, dst, a0); st_sc1(wsr, dst + 1024, a1);
            } else {
                if (l31 == 24) { a0 = (u32x4){0x3f803f80u, 0x3f803f80u, 0x3f803f80u, 0x3f803f80u}; a1 = a0; }
                else if (l31 > 24 && l31 < 28) { a0 = (u32x4){0, 0, 0, 0}; a1 = a0; }
                st_sc1(wsr, dst + 2048, a0); st_sc1(wsr, dst + 3072, a1);
            }
        }
        STAMP(1);
        if (lane == 0) __hip_atomic_fetch_add((LDS_AS unsigned*)(size_t)(lds0 + L_CTR + C_B1), 1u, __ATOMIC_RELAXED, __HIP_MEMORY_SCOPE_WORKGROUP);
#define KVS_AT 1
        if (KVS_AT == 0 && lane == 0) __hip_atomic_fetch_add((LDS_AS unsigned*)(size_t)(lds0 + L_CTR + C_KVS), 1u, __ATOMIC_RELAXED, __HIP_MEMORY_SCOPE_WORKGROUP);
        tag = uniform64(epoch_v) + 1;
        asm volatile("s_waitcnt vmcnt(0)" ::: "memory");
        STAMP(14);
        flag_set(p.ws, WS_FKV + lane * FKV_COPY + T * 16 + (wb & 1) * 8, tag);
        if (KVS_AT == 1 && lane == 0) __hip_atomic_fetch_add((LDS_AS unsigned*)(size_t)(lds0 + L_CTR + C_KVS), 1u, __ATOMIC_RELAXED, __HIP_MEMORY_SCOPE_WORKGROUP);
        STAMP(15);
    } else {
        const __amdgpu_buffer_rsrc_t rw1 = __builtin_amdgcn_make_buffer_rsrc((void*)p.w1, 0, 448 * 3584, 0x00020000);
        const unsigned off = (lane >> 2) * 3584 + fb * 64 + (lane & 3) * 16;
        u32x2 bv[4];
#pragma unroll
        for (int q = 0; q < 4; q++) bv[q] = *(const u32x2*)(p.b1 + fb * 32 + 4 * h + 8 * q);
        auto ld = [&](int s) { return __builtin_amdgcn_raw_buffer_load_b128(rw1, off, s * (16 * 3584), 0); };
        unsigned long long ptb[8];
        proj_loop(acc, lds0, ring, lane, light, ld, ptb);
        STAMP(1);
        if (lane == 0) __hip_atomic_fetch_add((LDS_AS unsigned*)(size_t)(lds0 + L_CTR + C_B1), 1u, __ATOMIC_RELAXED, __HIP_MEMORY_SCOPE_WORKGROUP);
        tag = uniform64(epoch_v) + 1;
        __builtin_amdgcn_s_setprio(G_PRIO);
#pragma unroll
        for (int t = 0; t < 4; t++) gelu_store(p, acc[t], bv, 4 * g + t, fb, lane);
        __builtin_amdgcn_s_setprio(F_PRIO);
        STAMP(12);
        asm volatile("s_waitcnt vmcnt(0)" ::: "memory");
        if (lane < 14) flag_set(p.ws, WS_FH + (g * 14 + lane) * 512 + fb * 8, tag);
        STAMP(3);

    }
    const bool tile = j < 14;
    if (!tile) {
        if (g == 0 && j == 15 && wb == 0) {
#pragma unroll 1
            for (int stage = 0; stage < 3; stage++) {
                const char* base = p.ws + (stage == 0 ? WS_FKV + lane * 8 : stage == 1 ? WS_FH + lane * 8 : WS_FO + (lane >> 4) * (7 * 128) + (lane & 15) * 8);
                const int n = stage == 0 ? 2 : 16;
                const unsigned step = stage == 0 ? 512 : stage == 1 ? 14 * 512 : 4 * 7 * 128;
                const bool on = stage != 1 || lane < 56;
#pragma unroll 1
                for (int spin = 0; spin < SPIN_CAP; spin++) {
                    u64 v[16];
#pragma unroll
                    for (int i = 0; i < 16; i++) v[i] = __hip_atomic_load((const u64*)(base + (i < n ? i : 0) * step), __ATOMIC_RELAXED, __HIP_MEMORY_SCOPE_AGENT);
                    unsigned bad = 0;
#pragma unroll
                    for (int i = 0; i < 16; i++) bad |= (v[i] != tag) ? 1u : 0u;
                    if (__builtin_amdgcn_ballot_w64(on && bad) == 0) break;
                    __builtin_amdgcn_s_sleep(16);
                }
            }
            if (lane == 0) __hip_atomic_store((u64*)(p.ws + WS_EPOCH), tag, __ATOMIC_RELAXED, __HIP_MEMORY_SCOPE_AGENT);
        }
        return;
    }
    const unsigned stg = lds0 + L_WO + wb * WO_SLICE;
    const int th = j & 1, cs = j >> 1, n0 = 64 * cs, T0 = 4 * g + 2 * th;
    const unsigned woff = ((lane & 31) >> 2) * 896 + (lane >> 5) * 64 + (lane & 3) * 16;
    const __amdgpu_buffer_rsrc_t rw2 = __builtin_amdgcn_make_buffer_rsrc((void*)p.w2, 0, 1792 * 896, 0x00020000);
    const unsigned w2v = woff + n0 * 2, w2s = 28 * wb * 16 * 896;
    constexpr int NB = 7;
    u32x4 Wr[NB][2];
#define ROT28(st_) (st_)
#define WLOAD(st_) do { const unsigned so_ = w2s + ROT28(st_) * (16 * 896); Wr[(st_) % NB][0] = __builtin_amdgcn_raw_buffer_load_b128(rw2, w2v, so_, 0); \
        Wr[(st_) % NB][1] = __builtin_amdgcn_raw_buffer_load_b128(rw2, w2v + 8 * 896, so_, 0); } while (0)
#pragma unroll
    for (int st = 0; st < NB; st++) WLOAD(st);
    const int Tm = T0 + (wb & 1);
    const int nc = n0 + 32 * (wb >> 1) + 4 * h;
    u32x2 b2v[4];
#pragma unroll
    for (int rg = 0; rg < 4; rg++) b2v[rg] = *(const u32x2*)(p.b2 + nc + 8 * rg);
    const unsigned fho = WS_FH + (g * 14 + j) * 512 + (14 * wb + lane) * 8;
    flags_wait(p.ws, fho, lane < 6, tag);
    lds_wait_ge(lds0 + L_CTR + C_AP, 4);
    lds_wait_ge(lds0 + L_CTR + C_B1, 4);
    STAMP(4);
#pragma unroll
    for (int i = 0; i < 4; i++) acc[i] = (f32x16){};
    {
        const unsigned wrd = stg + (4 * h + ((lane & 15) >> 2)) * 64 + ((lane >> 4) & 1) * 32 + (lane & 3) * 8;
        const unsigned hv = (unsigned)WS_HF + (T0 * 112 + 28 * wb) * 1024 + lo;
        bf16x8 H0[NB], H1[NB], wf0[2], wf1[2];
#define HLOAD(st_) do { const unsigned so_ = ROT28(st_) * 1024; H0[(st_) % NB] = ld_sc1s(wsr, hv, so_); H1[(st_) % NB] = ld_sc1s(wsr, hv + 112 * 1024, so_); } while (0)
#define WWRITE(st_) do { *(LDS_AS u32x4*)(size_t)(stg + ((st_) & 1) * 2048 + lo) = Wr[(st_) % NB][0]; *(LDS_AS u32x4*)(size_t)(stg + ((st_) & 1) * 2048 + 1024 + lo) = Wr[(st_) % NB][1]; } while (0)
#define WREAD(st_) do { wf0[(st_) & 1] = lds_tr2(wrd + ((st_) & 1) * 2048, wrd + ((st_) & 1) * 2048 + 1024); wf1[(st_) & 1] = lds_tr2(wrd + ((st_) & 1) * 2048 + 512, wrd + ((st_) & 1) * 2048 + 1536); } while (0)
#pragma unroll
        for (int st = 0; st < NB; st++) HLOAD(st);
        WWRITE(0); WLOAD(NB); WWRITE(1); WLOAD(NB + 1);
        WREAD(0);
#pragma unroll
        for (int st = 0; st < 28; st++) {
            SB;
            if (st + 1 < 28) WREAD(st + 1);
            if (st + 2 < 28) WWRITE(st + 2);
            if (st + 2 + NB < 28) WLOAD(st + 2 + NB);
            SB;
            acc[0] = MFMA(wf0[st & 1], H0[st % NB], acc[0]); acc[1] = MFMA(wf0[st & 1], H1[st % NB], acc[1]);
            acc[2] = MFMA(wf1[st & 1], H0[st % NB], acc[2]); acc[3] = MFMA(wf1[st & 1], H1[st % NB], acc[3]);
            SB;
            if (st + NB == 12) {
                u64 fv = tag;
                if (lane >= 6 && lane < 14) fv = __hip_atomic_load((const u64*)(p.ws + fho), __ATOMIC_RELAXED, __HIP_MEMORY_SCOPE_AGENT);
                if (__builtin_amdgcn_ballot_w64(fv != tag) != 0) flags_wait(p.ws, fho, lane >= 6 && lane < 14, tag);
                asm volatile("" ::: "memory");
            }
            if (st + NB < 28) HLOAD(st + NB);
        }
    }
    STAMP(5);
    asm volatile("s_waitcnt lgkmcnt(0)" ::: "memory");
    {
        const char* wos = (const char*)p.wo + n0 * 2;
#pragma unroll
        for (int i = 0; i < 14; i++) glds16(woff, wos + (size_t)(14 * wb + i) * (8 * 896), lds0 + L_WO + (14 * wb + i) * 1024);
    }
    lds_wait_ge(lds0 + L_CTR + C_QR, 4);
    {
        const unsigned rarea = lds0 + L_RED + wb * B_AREA;
#pragma unroll
        for (int i = 0; i < 4; i++) {
            if (i != wb) {
                const unsigned a = rarea + ((i - wb - 1) & 3) * 4096 + lo;
#pragma unroll
                for (int rg = 0; rg < 4; rg++) {
                    f32x4 v = {acc[i][4 * rg], acc[i][4 * rg + 1], acc[i][4 * rg + 2], acc[i][4 * rg + 3]};
                    *(LDS_AS f32x4*)(size_t)(a + rg * 1024) = v;
                }
            }
        }
    }
    f32x16 o = {};
#pragma unroll
    for (int i = 0; i < 4; i++) if (i == wb) o = acc[i];
    asm volatile("s_waitcnt vmcnt(0)" ::: "memory");
    lds_sync4(lds0 + L_CTR + C_RED, 4, lane);
#pragma unroll
    for (int u = 0; u < 4; u++) {
        if (u != wb) {
            const unsigned a = lds0 + L_RED + u * B_AREA + ((wb - u - 1) & 3) * 4096 + lo;
#pragma unroll
            for (int rg = 0; rg < 4; rg++) {
                f32x4 v = *(const LDS_AS f32x4*)(size_t)(a + rg * 1024);
                o[4 * rg] += v[0]; o[4 * rg + 1] += v[1]; o[4 * rg + 2] += v[2]; o[4 * rg + 3] += v[3];
            }
        }
    }
    {
        const float fg = 1.0f - *(const LDS_AS float*)(size_t)(lds0 + L_G + (64 * th + 32 * (wb & 1) + l31) * 4);
#pragma unroll
        for (int rg = 0; rg < 4; rg++) {
            const u32x2 bv = b2v[rg];
            o[4 * rg] = (o[4 * rg] + __uint_as_float(bv[0] << 16)) * fg; o[4 * rg + 1] = (o[4 * rg + 1] + __uint_as_float(bv[0] & 0xffff0000u)) * fg;
            o[4 * rg + 2] = (o[4 * rg + 2] + __uint_as_float(bv[1] << 16)) * fg; o[4 * rg + 3] = (o[4 * rg + 3] + __uint_as_float(bv[1] & 0xffff0000u)) * fg;
        }
    }
    STAMP(6);
    {
        const unsigned ob = (unsigned)WS_OF + Tm * 32 * 1024 + lo;
        const unsigned foo = WS_FO + (Tm * 7 + cs) * 128 + (lane & 15) * 8;
        bf16x8 of[32];
        unsigned ready = 0;
#pragma unroll
        for (int q = 0; q < 4; q++) {
#pragma unroll 1
            for (int spin = 0; spin < SPIN_CAP && ((ready >> (4 * q)) & 15u) != 15u; spin++) {
                if (spin) __builtin_amdgcn_s_sleep(POLL_SLEEP);
                u64 v = 0;
                if (lane < 16) v = __hip_atomic_load((const u64*)(p.ws + foo), __ATOMIC_RELAXED, __HIP_MEMORY_SCOPE_AGENT);
                ready = (unsigned)__builtin_amdgcn_ballot_w64(lane < 16 && v == tag);
            }
            asm volatile("" ::: "memory");
            if (q == 3) STAMP(7);
#pragma unroll
            for (int st = 8 * q; st < 8 * q + 8; st++) of[st] = ld_sc1(wsr, ob + st * 1024);
        }
        const unsigned lp16 = lds0 + L_WO + (wb >> 1) * 512 + ((lane & 15) >> 2) * 64 + ((lane >> 4) & 1) * 32 + (lane & 3) * 8;
        const unsigned wA = lp16 + h * 256, wB = lp16 + h * 768;
        f32x16 o2 = {};
#pragma unroll
        for (int st = 0; st < 32; st++) {
            const int hd = st >> 1, s = st & 1;
            const int r1 = 28 * hd + 16 * s, r2 = 28 * hd + (s ? 20 : 8);
            const unsigned a1 = ((r1 & 4) ? wB : wA) + (r1 >> 3) * 1024 + (r1 & 7) * 64, a2 = ((r2 & 4) ? wB : wA) + (r2 >> 3) * 1024 + (r2 & 7) * 64;
            const bf16x8 w0 = lds_tr2(a1, a2);
            if (st & 1) o2 = MFMA(w0, of[st], o2); else o = MFMA(w0, of[st], o);
        }
        o += o2;
    }
    STAMP(8);
    {
        unsigned short* orow = p.out + (size_t)(Tm * 32 + l31) * D_ + nc;
#pragma unroll
        for (int rg = 0; rg < 4; rg++) {
            u32x2 ov = {cvtpk(o[4 * rg], o[4 * rg + 1]), cvtpk(o[4 * rg + 2], o[4 * rg + 3])};
            *(u32x2*)(orow + 8 * rg) = ov;
        }
    }
    STAMP(9);
}

__global__ void __launch_bounds__(512, 2) gated_mix_fused(Params p) {
    extern __shared__ char smem[];
    const unsigned lds0 = (unsigned)(size_t)smem;
    const int b = blockIdx.x, xcd = b & 7, idx = b >> 3;
    const int g = xcd * 2 + (idx >> 4), j = idx & 15;
    const int w = __builtin_amdgcn_readfirstlane(threadIdx.x >> 6), lane = threadIdx.x & 63;
    const u64 epoch = __hip_atomic_load((const u64*)(p.ws + WS_EPOCH), __ATOMIC_RELAXED, __HIP_MEMORY_SCOPE_AGENT);
    if (w == 0 && lane < 16) *(LDS_AS unsigned*)(size_t)(lds0 + L_CTR + 4 * lane) = 0;
    wait_bar();
    if (w < 4) a_wave(p, lds0, g, j, w, lane, epoch);
    else b_wave(p, lds0, g, j, w - 4, lane, epoch);
}

extern "C" void kernel_launch(void* const* d_in, const int* in_sizes, int n_in, void* d_out, int out_size, void* d_ws, size_t ws_size, hipStream_t stream) {
    Params p;
    p.x = (const unsigned short*)d_in[0]; p.wq = (const unsigned short*)d_in[1]; p.wk = (const unsigned short*)d_in[2];
    p.wv = (const unsigned short*)d_in[3]; p.wo = (const unsigned short*)d_in[4]; p.w1 = (const unsigned short*)d_in[6];
    p.b1 = (const unsigned short*)d_in[7]; p.w2 = (const unsigned short*)d_in[8]; p.b2 = (const unsigned short*)d_in[9];
    p.gw = (const unsigned short*)d_in[10]; p.out = (unsigned short*)d_out; p.ws = (char*)d_ws;
    (void)hipFuncSetAttribute((const void*)gated_mix_fused, hipFuncAttributeMaxDynamicSharedMemorySize, LDS_BYTES);
    gated_mix_fused<<<256, 512, LDS_BYTES, stream>>>(p);
}
```

```cpp
#define BAR_EARLY 1
#define LFFN_GATE 1
#include <hip/hip_runtime.h>
#include <hip/hip_bf16.h>

typedef short bf16x8 __attribute__((ext_vector_type(8)));
typedef short s16x4 __attribute__((ext_vector_type(4)));
typedef __bf16 bf16x2 __attribute__((ext_vector_type(2)));
typedef float f32x16 __attribute__((ext_vector_type(16)));
typedef float f32x4 __attribute__((ext_vector_type(4)));
typedef float f32x2 __attribute__((ext_vector_type(2)));
typedef unsigned u32x4 __attribute__((ext_vector_type(4)));
typedef unsigned u32x2 __attribute__((ext_vector_type(2)));
typedef short v4i16_t __attribute__((ext_vector_type(4)));
typedef unsigned long long u64;
#define LDS_AS __attribute__((address_space(3)))
#define DI __device__ __forceinline__

constexpr int S_ = 2048, D_ = 448, F_ = 1792;
constexpr size_t WS_EPOCH = 0;
constexpr size_t WS_FKV = 65536, FKV_COPY = 1280;
constexpr size_t WS_FH = 163840;
constexpr size_t WS_FO = 294912;
constexpr size_t WS_KV = 1u << 20;
constexpr size_t WS_OF = 2u << 20;
constexpr size_t WS_HF = 4u << 20;

#define STAMP(i) do {} while (0)
#define MFMA(a, b, c) __builtin_amdgcn_mfma_f32_32x32x16_bf16(a, b, c, 0, 0, 0)
#define SB __builtin_amdgcn_sched_barrier(0)
#define EX2(x) __builtin_amdgcn_exp2f(x)
constexpr int SPIN_CAP = 1 << 21;
#define POLL_SLEEP 2
#define G_PRIO 3
#define F_PRIO 0
#define KV_PRIO 3
#define A1_PRIO 2
#define A2_PRIO 0
#define B_PRIO 2

DI unsigned cvtpk(float lo, float hi) { f32x2 v = {lo, hi}; return __builtin_bit_cast(unsigned, __builtin_convertvector(v, bf16x2)); }
DI void glds16(unsigned voff, const void* sbase, unsigned lds_dst) {
    unsigned keep;
    asm volatile("s_mov_b32 %0, m0\n\ts_mov_b32 m0, %3\n\ts_nop 0\n\tglobal_load_lds_dwordx4 %1, %2\n\ts_mov_b32 m0, %0"
                 : "=&s"(keep) : "v"(voff), "s"(sbase), "s"(lds_dst) : "memory");
}
DI void glds16_sc1(unsigned voff, const void* sbase, unsigned lds_dst) {
    unsigned keep;
    asm volatile("s_mov_b32 %0, m0\n\ts_mov_b32 m0, %3\n\ts_nop 0\n\tglobal_load_lds_dwordx4 %1, %2 sc1\n\ts_mov_b32 m0, %0"
                 : "=&s"(keep) : "v"(voff), "s"(sbase), "s"(lds_dst) : "memory");
}
DI bf16x8 lds_b128(unsigned a) { return *(const LDS_AS bf16x8*)(size_t)a; }
DI s16x4 lds_tr(unsigned a) { return __builtin_bit_cast(s16x4, __builtin_amdgcn_ds_read_tr16_b64_v4i16((LDS_AS v4i16_t*)(size_t)a)); }
DI bf16x8 lds_tr2(unsigned a0, unsigned a1) { s16x4 lo = lds_tr(a0), hi = lds_tr(a1); return __builtin_shufflevector(lo, hi, 0, 1, 2, 3, 4, 5, 6, 7); }
DI float dot2(unsigned a, unsigned b, float c) { return __builtin_amdgcn_fdot2_f32_bf16(__builtin_bit_cast(bf16x2, a), __builtin_bit_cast(bf16x2, b), c, false); }
DI u32x4 pack8(const f32x16& a, int s) {
    u32x4 r;
    r[0] = cvtpk(a[8 * s + 0], a[8 * s + 1]); r[1] = cvtpk(a[8 * s + 2], a[8 * s + 3]);
    r[2] = cvtpk(a[8 * s + 4], a[8 * s + 5]); r[3] = cvtpk(a[8 * s + 6], a[8 * s + 7]);
    return r;
}
DI float gelu_tanh(float x) {
    const float c1 = -2.0f * 1.4426950408889634f * 0.7978845608028654f, c2 = c1 * 0.044715f;
    float t = x * x;
    float e = EX2(x * __builtin_fmaf(t, c2, c1));
    return x * __builtin_amdgcn_rcpf(1.0f + e);
}
DI __amdgpu_buffer_rsrc_t ws_rsrc(const char* ws) { return __builtin_amdgcn_make_buffer_rsrc((void*)ws, 0, 16u << 20, 0x00020000); }
DI void st_sc1(__amdgpu_buffer_rsrc_t r, unsigned off, u32x4 v) { __builtin_amdgcn_raw_buffer_store_b128(v, r, off, 0, 16); }
DI void flag_set(char* ws, size_t off, u64 tag) { __hip_atomic_store((u64*)(ws + off), tag, __ATOMIC_RELAXED, __HIP_MEMORY_SCOPE_AGENT); }
DI void flags_wait(const char* ws, unsigned off, bool on, u64 tag) {
#pragma unroll 1
    for (int spin = 0; spin < SPIN_CAP; spin++) {
        u64 v = tag;
        if (on) v = __hip_atomic_load((const u64*)(ws + off), __ATOMIC_RELAXED, __HIP_MEMORY_SCOPE_AGENT);
        if (__builtin_amdgcn_ballot_w64(v != tag) == 0) break;
        __builtin_amdgcn_s_sleep(POLL_SLEEP);
    }
    asm volatile("s_waitcnt vmcnt(0)" ::: "memory");
}
DI u64 uniform64(u64 v) {
    asm volatile("" : "+v"(v) :: "memory");
    const unsigned lo = __builtin_amdgcn_readfirstlane((unsigned)v), hi = __builtin_amdgcn_readfirstlane((unsigned)(v >> 32));
    return ((u64)hi << 32) | lo;
}
DI unsigned lds_ld(unsigned a) { return __hip_atomic_load((LDS_AS unsigned*)(size_t)a, __ATOMIC_RELAXED, __HIP_MEMORY_SCOPE_WORKGROUP); }
DI void lds_st(unsigned a, unsigned v) { __hip_atomic_store((LDS_AS unsigned*)(size_t)a, v, __ATOMIC_RELAXED, __HIP_MEMORY_SCOPE_WORKGROUP); }
DI void lds_sync4(unsigned ctr, unsigned target, int lane) {
    asm volatile("s_waitcnt lgkmcnt(0)" ::: "memory");
    LDS_AS unsigned* c = (LDS_AS unsigned*)(size_t)ctr;
    if (lane == 0) __hip_atomic_fetch_add(c, 1u, __ATOMIC_RELAXED, __HIP_MEMORY_SCOPE_WORKGROUP);
#pragma unroll 1
    for (int spin = 0; spin < SPIN_CAP; spin++) {
        unsigned v = __builtin_amdgcn_readfirstlane(__hip_atomic_load(c, __ATOMIC_RELAXED, __HIP_MEMORY_SCOPE_WORKGROUP));
        if (v >= target) break;
        __builtin_amdgcn_s_sleep(1);
    }
    asm volatile("" ::: "memory");
}

struct Params {
    const unsigned short *x, *wq, *wk, *wv, *wo, *w1, *b1, *w2, *b2, *gw;
    unsigned short* out;
    char* ws;
};

constexpr unsigned X_SLOT = 16384, L_WQ = 114688, L_RING = 143360, L_QX = L_WQ;
constexpr unsigned L_WO = 0, WO_SLICE = 14336, L_MRG = 57344, L_RED = 106496, B_AREA = 12288;
constexpr unsigned P1_GW = 159744, L_G = P1_GW + 1024;
constexpr unsigned L_CTR = L_G + 512;
constexpr unsigned C_QW = 0, C_QR = 4, C_OW = 8, C_RED = 12, C_AP = 16, C_B1 = 20, C_KVS = 24, L_XF = L_CTR + 32, L_XE = L_CTR + 48;
constexpr unsigned LDS_BYTES = L_CTR + 64;
static_assert(L_RING + 4 * 4096 <= P1_GW && L_RED + 4 * B_AREA <= P1_GW && L_MRG + 12 * 4096 <= L_RED && LDS_BYTES <= 163840 && (L_XF & 15) == 0, "LDS map");

template <int N> DI void wait_vm() { asm volatile("s_waitcnt vmcnt(%0)" ::"n"(N) : "memory"); }
DI void wait_bar() { asm volatile("s_waitcnt lgkmcnt(0)\n\ts_barrier" ::: "memory"); }
DI u32x4 lds_flags(unsigned a) { u32x4 v; asm volatile("ds_read_b128 %0, %1" : "=v"(v) : "v"(a) : "memory"); return v; }
DI void lds_wait_ge(unsigned a, unsigned need) {
#pragma unroll 1
    for (int spin = 0; spin < SPIN_CAP; spin++) {
        if ((unsigned)__builtin_amdgcn_readfirstlane(lds_ld(a)) >= need) break;
        __builtin_amdgcn_s_sleep(1);
    }
    asm volatile("" ::: "memory");
}
DI void x_wait(unsigned xf, unsigned need, int only) {
#pragma unroll 1
    for (int spin = 0; spin < SPIN_CAP; spin++) {
        unsigned m;
        if (only < 0) {
            const unsigned a = lds_ld(xf), b = lds_ld(xf + 4), c = lds_ld(xf + 8), d = lds_ld(xf + 12);
            m = min(min(a, b), min(c, d));
        } else m = lds_ld(xf + 4 * only);
        if ((unsigned)__builtin_amdgcn_readfirstlane(m) >= need) break;
        __builtin_amdgcn_s_sleep(1);
    }
    asm volatile("" ::: "memory");
}
DI unsigned x_addr(unsigned lds0, int lane, int s) {
    const int h = lane >> 5, l31 = lane & 31;
    return lds0 + l31 * 128 + (((2 * s + h) ^ ((l31 >> 1) & 7)) << 4);
}
DI unsigned w_lane(int lane) { return (lane >> 5) * 512 + ((lane & 15) >> 2) * 64 + ((lane >> 4) & 1) * 32 + (lane & 3) * 8; }
#define NR1 8
template <typename Ld>
DI void proj_loop(f32x16 (&acc)[4], unsigned lds0, unsigned ring, int lane, bool light, Ld&& ld, unsigned long long* ptb) {
    constexpr int NR = NR1;
    u32x4 R[NR];
#define W1_DELAY 3
    if (light) { if (LFFN_GATE) lds_wait_ge(lds0 + L_CTR + C_KVS, 2); else x_wait(lds0 + L_XE, 1, -1); }
    else if (W1_DELAY > 0) x_wait(lds0 + L_XF, W1_DELAY, -1);
#pragma unroll
    for (int s = 0; s < NR; s++) R[s] = ld(s);
    unsigned xa[4];
#pragma unroll
    for (int s = 0; s < 4; s++) xa[s] = x_addr(lds0, lane, s);
    const unsigned wr = ring + lane * 16, rd = ring + w_lane(lane), xf = lds0 + L_XF;
    bf16x8 xf_[3][4], wf[3];
#define PWRITE(s_) do { *(LDS_AS u32x4*)(size_t)(wr + ((s_) & 3) * 1024) = R[(s_) % NR]; if ((s_) + NR < 28) R[(s_) % NR] = ld((s_) + NR); } while (0)
#define PREADW(s_) do { wf[(s_) % 3] = lds_tr2(rd + ((s_) & 3) * 1024, rd + ((s_) & 3) * 1024 + 256); } while (0)
#define PREADX(s_, t_) do { xf_[(s_) % 3][t_] = lds_b128(xa[(s_) & 3] + ((s_) >> 2) * X_SLOT + (t_) * 4096); } while (0)
    PWRITE(0); PWRITE(1); PWRITE(2);
    x_wait(xf, 1, -1);
    PREADW(0); PREADX(0, 0); PREADX(0, 1); PREADX(0, 2); PREADX(0, 3);
    PREADW(1); PREADX(1, 0); PREADX(1, 1); PREADX(1, 2); PREADX(1, 3);
#pragma unroll
    for (int s = 0; s < 28; s++) {
        SB;
        if ((s & 3) == 2 && s + 2 < 28) x_wait(xf, (s + 2) / 4 + 1, -1);
        acc[0] = MFMA(wf[s % 3], xf_[s % 3][0], acc[0]); SB;
        if (s + 2 < 28) { PREADW(s + 2); PREADX(s + 2, 0); } SB;
        acc[1] = MFMA(wf[s % 3], xf_[s % 3][1], acc[1]); SB;
        if (s + 2 < 28) { PREADX(s + 2, 1); PREADX(s + 2, 2); } SB;
        acc[2] = MFMA(wf[s % 3], xf_[s % 3][2], acc[2]); SB;
        if (s + 2 < 28) PREADX(s + 2, 3);
        if (s + 3 < 28) PWRITE(s + 3);
        SB;
        acc[3] = MFMA(wf[s % 3], xf_[s % 3][3], acc[3]);
    }
    SB;
#undef PWRITE
#undef PREADW
#undef PREADX
}
DI void gelu_store(const Params& p, const f32x16& acc, const u32x2 (&bv)[4], int T, int fb, int lane) {
    f32x16 a;
#pragma unroll
    for (int q = 0; q < 4; q++) {
        a[4 * q + 0] = acc[4 * q + 0] + __uint_as_float(bv[q][0] << 16); a[4 * q + 1] = acc[4 * q + 1] + __uint_as_float(bv[q][0] & 0xffff0000u);
        a[4 * q + 2] = acc[4 * q + 2] + __uint_as_float(bv[q][1] << 16); a[4 * q + 3] = acc[4 * q + 3] + __uint_as_float(bv[q][1] & 0xffff0000u);
    }
#pragma unroll
    for (int r = 0; r < 16; r++) a[r] = gelu_tanh(a[r]);
    const unsigned dst = (unsigned)WS_HF + (T * 112 + 2 * fb) * 1024 + lane * 16;
    const __amdgpu_buffer_rsrc_t r_ = ws_rsrc(p.ws);
    st_sc1(r_, dst, pack8(a, 0)); st_sc1(r_, dst + 1024, pack8(a, 1));
}

DI float max16(const f32x16& s) {
    float a = __builtin_fmaxf(__builtin_fmaxf(s[0], s[1]), s[2]), b = __builtin_fmaxf(__builtin_fmaxf(s[3], s[4]), s[5]);
#pragma unroll
    for (int r = 6; r < 14; r += 4) { a = __builtin_fmaxf(__builtin_fmaxf(a, s[r]), s[r + 1]); b = __builtin_fmaxf(__builtin_fmaxf(b, s[r + 2]), s[r + 3]); }
    a = __builtin_fmaxf(__builtin_fmaxf(a, s[14]), s[15]);
    return __builtin_fmaxf(a, b);
}
DI void set_max(bf16x8& q1, float m, int h) {
    auto rr = __builtin_amdgcn_permlane32_swap(__float_as_uint(m), __float_as_uint(m), false, false);
    const float nm = -__builtin_fmaxf(__uint_as_float(rr[0]), __uint_as_float(rr[1]));
    const unsigned a = cvtpk(nm, 0.f) & 0xffffu;
    const float r1 = nm - __uint_as_float(a << 16);
    const unsigned b = cvtpk(r1, 0.f) & 0xffffu;
    const float r2 = r1 - __uint_as_float(b << 16);
    const unsigned c = cvtpk(r2, 0.f) & 0xffffu;
    u32x4 t = __builtin_bit_cast(u32x4, q1);
    if (h == 0) { t[2] = a | (b << 16); t[3] = c; }
    q1 = __builtin_bit_cast(bf16x8, t);
}
DI bf16x8 ld_sc1s(__amdgpu_buffer_rsrc_t r, unsigned off, unsigned soff) { return __builtin_bit_cast(bf16x8, __builtin_amdgcn_raw_buffer_load_b128(r, off, soff, 16)); }
DI bf16x8 ld_sc1(__amdgpu_buffer_rsrc_t r, unsigned off) { return __builtin_bit_cast(bf16x8, __builtin_amdgcn_raw_buffer_load_b128(r, off, 0, 16)); }

DI void a_wave(const Params& p, unsigned lds0, int g, int j, int w, int lane, u64 epoch_v) {
    const int h = lane >> 5, l31 = lane & 31;
    const int T = 4 * g + w;
    f32x16 accq = {};
    STAMP(0);
    __builtin_amdgcn_s_setprio(A1_PRIO);
    unsigned xoff[4];
#pragma unroll
    for (int i = 0; i < 4; i++) {
        const int row = 32 * w + 8 * i + (lane >> 3);
        const int c = (lane & 7) ^ ((row >> 1) & 7);
        xoff[i] = row * 896 + c * 16;
    }
    const char* xb = (const char*)p.x + (size_t)g * 128 * 896;
    const unsigned qoff = (16 * w + (lane >> 2)) * 896 + ((lane & 3) == 3 ? 40 : (lane & 3) * 16);
    const char* qb_ = (const char*)p.wq + j * 56;
    glds16((lane < 56 ? lane : 55) * 16, p.gw, lds0 + P1_GW);
    auto dma = [&](int c) {
#pragma unroll
        for (int i = 0; i < 4; i++) glds16(xoff[i], xb + c * 128, lds0 + c * X_SLOT + (32 * w + 8 * i) * 128);
        glds16(qoff, qb_ + c * (64 * 896), lds0 + L_WQ + c * 4096 + w * 1024);
    };
    const bool light = j >= 12;
    if (light) {
        const int row = 32 * (j - 12) + 8 * w + (lane >> 3);
        const unsigned eo = row * 896 + (((lane & 7) ^ ((row >> 1) & 7)) << 4);
#pragma unroll
        for (int c = 0; c < 7; c++) glds16(eo, xb + c * 128, lds0 + c * X_SLOT + (32 * (j - 12) + 8 * w) * 128);
    }
    dma(0);
    if (!light) { dma(1); dma(2); }
    if (light) {
        wait_vm<5>();
        if (lane == 0) lds_st(lds0 + L_XE + 4 * w, 1);
        dma(1); dma(2);
    }
    u64 tag;
    const __amdgpu_buffer_rsrc_t wsr = ws_rsrc(p.ws);
    const int t0 = (4 * g + 4 * j + 16 * w) & 63;
    bf16x8 K0[4], K1[4], V0[4], V1[4];
    const unsigned kvb = (unsigned)WS_KV + lane * 16;
#define KVLOAD(set, vo) do { const unsigned o_ = (vo); K0[set] = ld_sc1(wsr, o_); K1[set] = ld_sc1(wsr, o_ + 1024); V0[set] = ld_sc1(wsr, o_ + 2048); V1[set] = ld_sc1(wsr, o_ + 3072); } while (0)
    {
        unsigned xa[4];
#pragma unroll
        for (int s = 0; s < 4; s++) xa[s] = x_addr(lds0, lane, s) + w * 4096;
        const unsigned wa = lds0 + L_WQ + w_lane(lane);
        unsigned short gwv[7];
        auto publish = [&](int c) {
            if (lane < 16) *(LDS_AS unsigned short*)(size_t)(lds0 + L_WQ + c * 4096 + w * 1024 + lane * 64 + 48) = gwv[c];
            asm volatile("s_waitcnt lgkmcnt(0)" ::: "memory");
            if (lane == 0) lds_st(lds0 + L_XF + 4 * w, c + 1);
        };
        auto qchunk = [&](int c) {
            bf16x8 xf[4], wf[4];
#pragma unroll
            for (int s = 0; s < 4; s++) { xf[s] = lds_b128(xa[s] + c * X_SLOT); wf[s] = lds_tr2(wa + c * 4096 + s * 1024, wa + c * 4096 + s * 1024 + 256); }
#pragma unroll
            for (int s = 0; s < 4; s++) accq = MFMA(wf[s], xf[s], accq);
        };
#define TC(c)
        wait_vm<10>(); TC(0);
#pragma unroll
        for (int c = 0; c < 7; c++) gwv[c] = *(const LDS_AS unsigned short*)(size_t)(lds0 + P1_GW + (64 * c + 16 * w + (lane & 15)) * 2);
        publish(0);
        if (!light) { dma(3); wait_vm<10>(); } else wait_vm<5>();
        TC(1); publish(1);
        if (!light) { dma(4); wait_vm<10>(); } else wait_vm<0>();
        TC(2); publish(2);
        if (light) {
            lds_wait_ge(lds0 + L_CTR + C_KVS, 2);
            dma(3); dma(4);
        }
        dma(5);
        wait_vm<10>(); TC(3); publish(3); dma(6);
        wait_vm<10>(); TC(4); publish(4);
        x_wait(lds0 + L_XF, 2, -1); qchunk(0); qchunk(1); SB;
        wait_vm<5>(); TC(5); publish(5);
        x_wait(lds0 + L_XF, 4, -1); qchunk(2); qchunk(3); SB;
        wait_vm<0>(); TC(6); publish(6);
        tag = uniform64(epoch_v) + 1;
        const unsigned fo = WS_FKV + (g * 4 + (j >> 2)) * FKV_COPY + ((t0 + (lane >> 1)) & 63) * 16 + (lane & 1) * 8;
        u64 fv = tag;
        if (lane < 32) fv = __hip_atomic_load((const u64*)(p.ws + fo), __ATOMIC_RELAXED, __HIP_MEMORY_SCOPE_AGENT);
        x_wait(lds0 + L_XF, 6, -1); TC(8); qchunk(4); qchunk(5); SB; TC(9);
        x_wait(lds0 + L_XF, 7, -1); TC(11); qchunk(6); SB; TC(12);
        asm volatile("" : "+v"(fv) :: "memory");
        if (__builtin_amdgcn_ballot_w64(fv != tag) != 0) flags_wait(p.ws, fo, lane < 32, tag);
        TC(10);
        KVLOAD(0, kvb + t0 * 4096); KVLOAD(1, kvb + (t0 + 1) * 4096); KVLOAD(2, kvb + (t0 + 2) * 4096);
        SB;
    }
    const float zz = __uint_as_float(__builtin_amdgcn_permlane32_swap(__float_as_uint(accq[12]), __float_as_uint(accq[12]), false, false)[0]);
    const float gtok = __builtin_amdgcn_rcpf(1.0f + EX2(-1.4426950408889634f * zz));
    if (h == 0) *(LDS_AS float*)(size_t)(lds0 + L_G + (32 * w + l31) * 4) = gtok;
    bf16x8 q0[4], q1[4];
    bf16x8 qa, qb;
    {
        const float qs = 1.4426950408889634f * 0.1889822365046136f;
        f32x16 q = accq * qs;
        u32x4 a0 = pack8(q, 0), a1 = pack8(q, 1);
        if (h == 0) { a1[2] = 0; a1[3] = 0; }
        qa = __builtin_bit_cast(bf16x8, a0); qb = __builtin_bit_cast(bf16x8, a1);
    }
    asm volatile("s_waitcnt lgkmcnt(0)" ::: "memory");
    if (lane == 0) __hip_atomic_fetch_add((LDS_AS unsigned*)(size_t)(lds0 + L_CTR + C_AP), 1u, __ATOMIC_RELAXED, __HIP_MEMORY_SCOPE_WORKGROUP);
    const f32x16 zero = {};
    V1[3] = (bf16x8){}; K0[3] = V1[3]; K1[3] = V1[3]; V0[3] = V1[3];
    f32x16 S = MFMA(K0[0], qa, zero); S = MFMA(K1[0], qb, S);
    set_max(qb, max16(S), h);
    const unsigned lo = lane * 16;
    *(LDS_AS bf16x8*)(size_t)(lds0 + L_QX + w * 2048 + lo) = qa;
    *(LDS_AS bf16x8*)(size_t)(lds0 + L_QX + w * 2048 + 1024 + lo) = qb;
    lds_sync4(lds0 + L_CTR + C_QW, 4, lane);
#pragma unroll
    for (int b = 0; b < 4; b++) { q0[b] = lds_b128(lds0 + L_QX + b * 2048 + lo); q1[b] = lds_b128(lds0 + L_QX + b * 2048 + 1024 + lo); }
    asm volatile("s_waitcnt lgkmcnt(0)" ::: "memory");
    if (lane == 0) __hip_atomic_fetch_add((LDS_AS unsigned*)(size_t)(lds0 + L_CTR + C_QR), 1u, __ATOMIC_RELAXED, __HIP_MEMORY_SCOPE_WORKGROUP);
    TC(13);
    __builtin_amdgcn_s_setprio(A2_PRIO);
    S = MFMA(K0[0], q0[0], zero); S = MFMA(K1[0], q1[0], S);
    f32x16 O[4];
#pragma unroll
    for (int b = 0; b < 4; b++) O[b] = zero;
    u32x4 P1 = {0, 0, 0, 0};
#define ASTEP(c, b) do { \
        constexpr int pc = (b) == 0 ? ((c) + 3) & 3 : (c), pb = ((b) + 3) & 3, nc = (b) == 3 ? ((c) + 1) & 3 : (c), nb = ((b) + 1) & 3; \
        O[pb] = MFMA(V1[pc], __builtin_bit_cast(bf16x8, P1), O[pb]); SB; \
        u32x4 p0, p1; \
        p0[0] = cvtpk(EX2(S[0]), EX2(S[1])); p0[1] = cvtpk(EX2(S[2]), EX2(S[3])); SB; \
        f32x16 Sn = MFMA(K0[nc], q0[nb], zero); SB; \
        if ((b) == 1) { KVLOAD(pc3(c), vnext); } \
        p0[2] = cvtpk(EX2(S[4]), EX2(S[5])); p0[3] = cvtpk(EX2(S[6]), EX2(S[7])); SB; \
        Sn = MFMA(K1[nc], q1[nb], Sn); SB; \
        p1[0] = cvtpk(EX2(S[8]), EX2(S[9])); p1[1] = cvtpk(EX2(S[10]), EX2(S[11])); SB; \
        O[b] = MFMA(V0[c], __builtin_bit_cast(bf16x8, p0), O[b]); SB; \
        p1[2] = cvtpk(EX2(S[12]), EX2(S[13])); p1[3] = cvtpk(EX2(S[14]), EX2(S[15])); SB; \
        P1 = p1; S = Sn; } while (0)
#define pc3(c) (((c) + 3) & 3)
#define ATILE(c) do { \
        const int tn = tt + (c) + 3;     \
        const unsigned vnext = lo + (tn < 16 ? (unsigned)WS_KV + ((t0 + tn) & 63) * 4096u : 0x7f000000u); \
        ASTEP(c, 0); ASTEP(c, 1); ASTEP(c, 2); ASTEP(c, 3); } while (0)
#pragma unroll 1
    for (int tt = 0; tt < 16; tt += 4) {
        ATILE(0); ATILE(1); ATILE(2); ATILE(3);
    }
    O[3] = MFMA(V1[3], __builtin_bit_cast(bf16x8, P1), O[3]);
    STAMP(5);
    lds_wait_ge(lds0 + L_CTR + C_B1, 4);
#pragma unroll
    for (int b = 0; b < 4; b++) {
        if (b != w) {
            const unsigned a = lds0 + L_MRG + (b * 3 + ((w - b - 1) & 3)) * 4096 + lo;
#pragma unroll
            for (int rg = 0; rg < 4; rg++) {
                f32x4 v = {O[b][4 * rg], O[b][4 * rg + 1], O[b][4 * rg + 2], O[b][4 * rg + 3]};
                *(LDS_AS f32x4*)(size_t)(a + rg * 1024) = v;
            }
        }
    }
    f32x16 o = zero;
#pragma unroll
    for (int b = 0; b < 4; b++) if (b == w) { o = O[b]; qa = q0[b]; qb = q1[b]; }
    lds_sync4(lds0 + L_CTR + C_OW, 4, lane);
#pragma unroll
    for (int u = 0; u < 3; u++) {
        const unsigned a = lds0 + L_MRG + (w * 3 + u) * 4096 + lo;
#pragma unroll
        for (int rg = 0; rg < 4; rg++) {
            f32x4 v = *(const LDS_AS f32x4*)(size_t)(a + rg * 1024);
            o[4 * rg] += v[0]; o[4 * rg + 1] += v[1]; o[4 * rg + 2] += v[2]; o[4 * rg + 3] += v[3];
        }
    }
    STAMP(6);
    auto rr = __builtin_amdgcn_permlane32_swap(__float_as_uint(o[12]), __float_as_uint(o[12]), false, false);
    float l = __uint_as_float(rr[0]);
    {
        bool ok = l < 1.152921504606846976e18f && l > 8.673617379884035e-19f;
#pragma unroll
        for (int r = 0; r < 16; r++) ok = ok && (__builtin_fabsf(o[r]) < 3.0e38f);
        if (__builtin_amdgcn_ballot_w64(!ok) != 0) {
            u32x4 tq = __builtin_bit_cast(u32x4, qb);
            if (h == 0) { tq[2] = 0; tq[3] = 0; }
            qb = __builtin_bit_cast(bf16x8, tq);
            float m = -INFINITY;
#pragma unroll 1
            for (int t = 0; t < 64; t++) {
                bf16x8 k0 = ld_sc1(wsr, kvb + t * 4096), k1 = ld_sc1(wsr, kvb + t * 4096 + 1024);
                f32x16 s = MFMA(k0, qa, zero); s = MFMA(k1, qb, s);
                m = __builtin_fmaxf(m, max16(s));
            }
            set_max(qb, m, h);
            o = zero;
#pragma unroll 1
            for (int t = 0; t < 64; t++) {
                bf16x8 k0 = ld_sc1(wsr, kvb + t * 4096), k1 = ld_sc1(wsr, kvb + t * 4096 + 1024);
                bf16x8 v0 = ld_sc1(wsr, kvb + t * 4096 + 2048), v1 = ld_sc1(wsr, kvb + t * 4096 + 3072);
                f32x16 s = MFMA(k0, qa, zero); s = MFMA(k1, qb, s);
#pragma unroll
                for (int r = 0; r < 16; r++) s[r] = EX2(s[r]);
                o = MFMA(v0, __builtin_bit_cast(bf16x8, pack8(s, 0)), o); o = MFMA(v1, __builtin_bit_cast(bf16x8, pack8(s, 1)), o);
            }
            auto r2 = __builtin_amdgcn_permlane32_swap(__float_as_uint(o[12]), __float_as_uint(o[12]), false, false);
            l = __uint_as_float(r2[0]);
        }
    }
    const float sc = gtok * __builtin_amdgcn_rcpf(l);
#pragma unroll
    for (int r = 0; r < 16; r++) o[r] *= sc;
    u32x4 o0 = pack8(o, 0), o1 = pack8(o, 1);
    if (h == 0) { o1[2] = 0; o1[3] = 0; }
    const unsigned dst = (unsigned)WS_OF + (T * 32 + 2 * j) * 1024 + lo;
    st_sc1(wsr, dst, o0); st_sc1(wsr, dst + 1024, o1);
    asm volatile("s_waitcnt vmcnt(0)" ::: "memory");
    if (lane < 7) flag_set(p.ws, WS_FO + (T * 7 + lane) * 128 + j * 8, tag);
    STAMP(7);
}

DI void b_wave(const Params& p, unsigned lds0, int g, int j, int wb, int lane, u64 epoch_v) {
    const int h = lane >> 5, l31 = lane & 31;
    const bool light = j >= 12;
    const int lp = j - 12;
    const int fb = light ? 48 + 2 * lp + wb : 4 * j + wb;
    const unsigned lo = lane * 16;
    f32x16 acc[4];
#pragma unroll
    for (int b = 0; b < 4; b++) acc[b] = (f32x16){};
    STAMP(0);
    __builtin_amdgcn_s_setprio(B_PRIO);
    const __amdgpu_buffer_rsrc_t wsr = ws_rsrc(p.ws);
    const unsigned ring = lds0 + L_RING + wb * 4096;
    u64 tag;
    if (light && wb >= 2) {
        __builtin_amdgcn_s_setprio(KV_PRIO);
        const int T = 4 * g + lp;
        const __amdgpu_buffer_rsrc_t rk = __builtin_amdgcn_make_buffer_rsrc((void*)(wb == 2 ? p.wk : p.wv), 0, 448 * 56, 0x00020000);
        const unsigned off = (lane >> 2) * 56 + ((lane & 3) == 3 ? 40 : (lane & 3) * 16);
        const unsigned dst = (unsigned)WS_KV + T * 4096 + lo;
        u32x4 R[28];
#pragma unroll
        for (int s = 0; s < 28; s++) R[s] = __builtin_amdgcn_raw_buffer_load_b128(rk, off, s * (16 * 56), 0);
        const unsigned wr = ring + lo, rd = ring + w_lane(lane);
        bf16x8 wf[28];
#pragma unroll
        for (int q = 0; q < 28; q += 4) {
#pragma unroll
            for (int u = 0; u < 4; u++) *(LDS_AS u32x4*)(size_t)(wr + u * 1024) = R[q + u];
#pragma unroll
            for (int u = 0; u < 4; u++) wf[q + u] = lds_tr2(rd + u * 1024, rd + u * 1024 + 256);
            SB;
        }
        STAMP(10);
        x_wait(lds0 + L_XE, 1, -1);
        STAMP(11);
        f32x16 a2 = {};
        {
            unsigned xa[4];
#pragma unroll
            for (int s = 0; s < 4; s++) xa[s] = x_addr(lds0, lane, s) + lp * 4096;
            bf16x8 xf[2][4];
#define KVX(q_) _Pragma("unroll") for (int u = 0; u < 4; u++) xf[(q_) & 1][u] = lds_b128(xa[u] + (q_) * X_SLOT)
            KVX(0); KVX(1);
            if (wb == 2) {
#pragma unroll
                for (int q = 0; q < 7; q++) {
                    SB;
#pragma unroll
                    for (int u = 0; u < 4; u++) { if (u & 1) a2 = MFMA(wf[4 * q + u], xf[q & 1][u], a2); else acc[0] = MFMA(wf[4 * q + u], xf[q & 1][u], acc[0]); }
                    SB;
                    if (q + 2 < 7) { KVX(q + 2); }
                }
            } else {
#pragma unroll
                for (int q = 0; q < 7; q++) {
                    SB;
#pragma unroll
                    for (int u = 0; u < 4; u++) { if (u & 1) a2 = MFMA(xf[q & 1][u], wf[4 * q + u], a2); else acc[0] = MFMA(xf[q & 1][u], wf[4 * q + u], acc[0]); }
                    SB;
                    if (q + 2 < 7) { KVX(q + 2); }
                }
            }
        }
        acc[0] += a2;
        STAMP(13);
        {
            u32x4 a0 = pack8(acc[0], 0), a1 = pack8(acc[0], 1);
            if (wb == 2) {
                if (h == 0) { a1[2] = 0x3f803f80u; a1[3] = 0x00003f80u; }
                st_sc1(wsr, dst, a0); st_sc1(wsr, dst + 1024, a1);
            } else {
                if (l31 == 24) { a0 = (u32x4){0x3f803f80u, 0x3f803f80u, 0x3f803f80u, 0x3f803f80u}; a1 = a0; }
                else if (l31 > 24 && l31 < 28) { a0 = (u32x4){0, 0, 0, 0}; a1 = a0; }
                st_sc1(wsr, dst + 2048, a0); st_sc1(wsr, dst + 3072, a1);
            }
        }
        STAMP(1);
        if (lane == 0) __hip_atomic_fetch_add((LDS_AS unsigned*)(size_t)(lds0 + L_CTR + C_B1), 1u, __ATOMIC_RELAXED, __HIP_MEMORY_SCOPE_WORKGROUP);
#define KVS_AT 1
        if (KVS_AT == 0 && lane == 0) __hip_atomic_fetch_add((LDS_AS unsigned*)(size_t)(lds0 + L_CTR + C_KVS), 1u, __ATOMIC_RELAXED, __HIP_MEMORY_SCOPE_WORKGROUP);
        tag = uniform64(epoch_v) + 1;
        asm volatile("s_waitcnt vmcnt(0)" ::: "memory");
        STAMP(14);
        flag_set(p.ws, WS_FKV + lane * FKV_COPY + T * 16 + (wb & 1) * 8, tag);
        if (KVS_AT == 1 && lane == 0) __hip_atomic_fetch_add((LDS_AS unsigned*)(size_t)(lds0 + L_CTR + C_KVS), 1u, __ATOMIC_RELAXED, __HIP_MEMORY_SCOPE_WORKGROUP);
        STAMP(15);
    } else {
        const __amdgpu_buffer_rsrc_t rw1 = __builtin_amdgcn_make_buffer_rsrc((void*)p.w1, 0, 448 * 3584, 0x00020000);
        const unsigned off = (lane >> 2) * 3584 + fb * 64 + (lane & 3) * 16;
        u32x2 bv[4];
#pragma unroll
        for (int q = 0; q < 4; q++) bv[q] = *(const u32x2*)(p.b1 + fb * 32 + 4 * h + 8 * q);
        auto ld = [&](int s) { return __builtin_amdgcn_raw_buffer_load_b128(rw1, off, s * (16 * 3584), 0); };
        unsigned long long ptb[8];
        proj_loop(acc, lds0, ring, lane, light, ld, ptb);
        STAMP(1);
        if (lane == 0) __hip_atomic_fetch_add((LDS_AS unsigned*)(size_t)(lds0 + L_CTR + C_B1), 1u, __ATOMIC_RELAXED, __HIP_MEMORY_SCOPE_WORKGROUP);
        tag = uniform64(epoch_v) + 1;
        __builtin_amdgcn_s_setprio(G_PRIO);
#pragma unroll
        for (int t = 0; t < 4; t++) gelu_store(p, acc[t], bv, 4 * g + t, fb, lane);
        __builtin_amdgcn_s_setprio(F_PRIO);
        STAMP(12);
        asm volatile("s_waitcnt vmcnt(0)" ::: "memory");
        if (lane < 14) flag_set(p.ws, WS_FH + (g * 14 + lane) * 512 + fb * 8, tag);
        STAMP(3);

    }
    const bool tile = j < 14;
    if (!tile) {
        if (g == 0 && j == 15 && wb == 0) {
#pragma unroll 1
            for (int stage = 0; stage < 3; stage++) {
                const char* base = p.ws + (stage == 0 ? WS_FKV + lane * 8 : stage == 1 ? WS_FH + lane * 8 : WS_FO + (lane >> 4) * (7 * 128) + (lane & 15) * 8);
                const int n = stage == 0 ? 2 : 16;
                const unsigned step = stage == 0 ? 512 : stage == 1 ? 14 * 512 : 4 * 7 * 128;
                const bool on = stage != 1 || lane < 56;
#pragma unroll 1
                for (int spin = 0; spin < SPIN_CAP; spin++) {
                    u64 v[16];
#pragma unroll
                    for (int i = 0; i < 16; i++) v[i] = __hip_atomic_load((const u64*)(base + (i < n ? i : 0) * step), __ATOMIC_RELAXED, __HIP_MEMORY_SCOPE_AGENT);
                    unsigned bad = 0;
#pragma unroll
                    for (int i = 0; i < 16; i++) bad |= (v[i] != tag) ? 1u : 0u;
                    if (__builtin_amdgcn_ballot_w64(on && bad) == 0) break;
                    __builtin_amdgcn_s_sleep(16);
                }
            }
            if (lane == 0) __hip_atomic_store((u64*)(p.ws + WS_EPOCH), tag, __ATOMIC_RELAXED, __HIP_MEMORY_SCOPE_AGENT);
        }
        return;
    }
    const unsigned stg = lds0 + L_WO + wb * WO_SLICE;
    const int th = j & 1, cs = j >> 1, n0 = 64 * cs, T0 = 4 * g + 2 * th;
    const unsigned woff = ((lane & 31) >> 2) * 896 + (lane >> 5) * 64 + (lane & 3) * 16;
    const __amdgpu_buffer_rsrc_t rw2 = __builtin_amdgcn_make_buffer_rsrc((void*)p.w2, 0, 1792 * 896, 0x00020000);
    const unsigned w2v = woff + n0 * 2, w2s = 28 * wb * 16 * 896;
    constexpr int NB = 7;
    u32x4 Wr[NB][2];
#define ROT28(st_) (st_)
#define WLOAD(st_) do { const unsigned so_ = w2s + ROT28(st_) * (16 * 896); Wr[(st_) % NB][0] = __builtin_amdgcn_raw_buffer_load_b128(rw2, w2v, so_, 0); \
        Wr[(st_) % NB][1] = __builtin_amdgcn_raw_buffer_load_b128(rw2, w2v + 8 * 896, so_, 0); } while (0)
#pragma unroll
    for (int st = 0; st < NB; st++) WLOAD(st);
    const int Tm = T0 + (wb & 1);
    const int nc = n0 + 32 * (wb >> 1) + 4 * h;
    u32x2 b2v[4];
#pragma unroll
    for (int rg = 0; rg < 4; rg++) b2v[rg] = *(const u32x2*)(p.b2 + nc + 8 * rg);
    const unsigned fho = WS_FH + (g * 14 + j) * 512 + (14 * wb + lane) * 8;
    flags_wait(p.ws, fho, lane < 6, tag);
    lds_wait_ge(lds0 + L_CTR + C_AP, 4);
    lds_wait_ge(lds0 + L_CTR + C_B1, 4);
    STAMP(4);
#pragma unroll
    for (int i = 0; i < 4; i++) acc[i] = (f32x16){};
    {
        const unsigned wrd = stg + (4 * h + ((lane & 15) >> 2)) * 64 + ((lane >> 4) & 1) * 32 + (lane & 3) * 8;
        const unsigned hv = (unsigned)WS_HF + (T0 * 112 + 28 * wb) * 1024 + lo;
        bf16x8 H0[NB], H1[NB], wf0[2], wf1[2];
#define HLOAD(st_) do { const unsigned so_ = ROT28(st_) * 1024; H0[(st_) % NB] = ld_sc1s(wsr, hv, so_); H1[(st_) % NB] = ld_sc1s(wsr, hv + 112 * 1024, so_); } while (0)
#define WWRITE(st_) do { *(LDS_AS u32x4*)(size_t)(stg + ((st_) & 1) * 2048 + lo) = Wr[(st_) % NB][0]; *(LDS_AS u32x4*)(size_t)(stg + ((st_) & 1) * 2048 + 1024 + lo) = Wr[(st_) % NB][1]; } while (0)
#define WREAD(st_) do { wf0[(st_) & 1] = lds_tr2(wrd + ((st_) & 1) * 2048, wrd + ((st_) & 1) * 2048 + 1024); wf1[(st_) & 1] = lds_tr2(wrd + ((st_) & 1) * 2048 + 512, wrd + ((st_) & 1) * 2048 + 1536); } while (0)
#pragma unroll
        for (int st = 0; st < NB; st++) HLOAD(st);
        WWRITE(0); WLOAD(NB); WWRITE(1); WLOAD(NB + 1);
        WREAD(0);
#pragma unroll
        for (int st = 0; st < 28; st++) {
            SB;
            if (st + 1 < 28) WREAD(st + 1);
            if (st + 2 < 28) WWRITE(st + 2);
            if (st + 2 + NB < 28) WLOAD(st + 2 + NB);
            SB;
            acc[0] = MFMA(wf0[st & 1], H0[st % NB], acc[0]); acc[1] = MFMA(wf0[st & 1], H1[st % NB], acc[1]);
            acc[2] = MFMA(wf1[st & 1], H0[st % NB], acc[2]); acc[3] = MFMA(wf1[st & 1], H1[st % NB], acc[3]);
            SB;
            if (st + NB == 12) {
                u64 fv = tag;
                if (lane >= 6 && lane < 14) fv = __hip_atomic_load((const u64*)(p.ws + fho), __ATOMIC_RELAXED, __HIP_MEMORY_SCOPE_AGENT);
                if (__builtin_amdgcn_ballot_w64(fv != tag) != 0) flags_wait(p.ws, fho, lane >= 6 && lane < 14, tag);
                asm volatile("" ::: "memory");
            }
            if (st + NB < 28) HLOAD(st + NB);
        }
    }
    STAMP(5);
    asm volatile("s_waitcnt lgkmcnt(0)" ::: "memory");
    {
        const char* wos = (const char*)p.wo + n0 * 2;
#pragma unroll
        for (int i = 0; i < 14; i++) glds16(woff, wos + (size_t)(14 * wb + i) * (8 * 896), lds0 + L_WO + (14 * wb + i) * 1024);
    }
    lds_wait_ge(lds0 + L_CTR + C_QR, 4);
    {
        const unsigned rarea = lds0 + L_RED + wb * B_AREA;
#pragma unroll
        for (int i = 0; i < 4; i++) {
            if (i != wb) {
                const unsigned a = rarea + ((i - wb - 1) & 3) * 4096 + lo;
#pragma unroll
                for (int rg = 0; rg < 4; rg++) {
                    f32x4 v = {acc[i][4 * rg], acc[i][4 * rg + 1], acc[i][4 * rg + 2], acc[i][4 * rg + 3]};
                    *(LDS_AS f32x4*)(size_t)(a + rg * 1024) = v;
                }
            }
        }
    }
    f32x16 o = {};
#pragma unroll
    for (int i = 0; i < 4; i++) if (i == wb) o = acc[i];
    asm volatile("s_waitcnt vmcnt(0)" ::: "memory");
    lds_sync4(lds0 + L_CTR + C_RED, 4, lane);
#pragma unroll
    for (int u = 0; u < 4; u++) {
        if (u != wb) {
            const unsigned a = lds0 + L_RED + u * B_AREA + ((wb - u - 1) & 3) * 4096 + lo;
#pragma unroll
            for (int rg = 0; rg < 4; rg++) {
                f32x4 v = *(const LDS_AS f32x4*)(size_t)(a + rg * 1024);
                o[4 * rg] += v[0]; o[4 * rg + 1] += v[1]; o[4 * rg + 2] += v[2]; o[4 * rg + 3] += v[3];
            }
        }
    }
    {
        const float fg = 1.0f - *(const LDS_AS float*)(size_t)(lds0 + L_G + (64 * th + 32 * (wb & 1) + l31) * 4);
#pragma unroll
        for (int rg = 0; rg < 4; rg++) {
            const u32x2 bv = b2v[rg];
            o[4 * rg] = (o[4 * rg] + __uint_as_float(bv[0] << 16)) * fg; o[4 * rg + 1] = (o[4 * rg + 1] + __uint_as_float(bv[0] & 0xffff0000u)) * fg;
            o[4 * rg + 2] = (o[4 * rg + 2] + __uint_as_float(bv[1] << 16)) * fg; o[4 * rg + 3] = (o[4 * rg + 3] + __uint_as_float(bv[1] & 0xffff0000u)) * fg;
        }
    }
    STAMP(6);
    {
        const unsigned ob = (unsigned)WS_OF + Tm * 32 * 1024 + lo;
        const unsigned foo = WS_FO + (Tm * 7 + cs) * 128 + (lane & 15) * 8;
        bf16x8 of[32];
        unsigned ready = 0;
#pragma unroll
        for (int q = 0; q < 4; q++) {
#pragma unroll 1
            for (int spin = 0; spin < SPIN_CAP && ((ready >> (4 * q)) & 15u) != 15u; spin++) {
                if (spin) __builtin_amdgcn_s_sleep(POLL_SLEEP);
                u64 v = 0;
                if (lane < 16) v = __hip_atomic_load((const u64*)(p.ws + foo), __ATOMIC_RELAXED, __HIP_MEMORY_SCOPE_AGENT);
                ready = (unsigned)__builtin_amdgcn_ballot_w64(lane < 16 && v == tag);
            }
            asm volatile("" ::: "memory");
            if (q == 3) STAMP(7);
#pragma unroll
            for (int st = 8 * q; st < 8 * q + 8; st++) of[st] = ld_sc1(wsr, ob + st * 1024);
        }
        const unsigned lp16 = lds0 + L_WO + (wb >> 1) * 512 + ((lane & 15) >> 2) * 64 + ((lane >> 4) & 1) * 32 + (lane & 3) * 8;
        const unsigned wA = lp16 + h * 256, wB = lp16 + h * 768;
        f32x16 o2 = {};
#pragma unroll
        for (int st = 0; st < 32; st++) {
            const int hd = st >> 1, s = st & 1;
            const int r1 = 28 * hd + 16 * s, r2 = 28 * hd + (s ? 20 : 8);
            const unsigned a1 = ((r1 & 4) ? wB : wA) + (r1 >> 3) * 1024 + (r1 & 7) * 64, a2 = ((r2 & 4) ? wB : wA) + (r2 >> 3) * 1024 + (r2 & 7) * 64;
            const bf16x8 w0 = lds_tr2(a1, a2);
            if (st & 1) o2 = MFMA(w0, of[st], o2); else o = MFMA(w0, of[st], o);
        }
        o += o2;
    }
    STAMP(8);
    {
        unsigned short* orow = p.out + (size_t)(Tm * 32 + l31) * D_ + nc;
#pragma unroll
        for (int rg = 0; rg < 4; rg++) {
            u32x2 ov = {cvtpk(o[4 * rg], o[4 * rg + 1]), cvtpk(o[4 * rg + 2], o[4 * rg + 3])};
            *(u32x2*)(orow + 8 * rg) = ov;
        }
    }
    STAMP(9);
}

__global__ void __launch_bounds__(512, 2) gated_mix_fused(Params p) {
    extern __shared__ char smem[];
    const unsigned lds0 = (unsigned)(size_t)smem;
    const int b = blockIdx.x, xcd = b & 7, idx = b >> 3;
    const int g = xcd * 2 + (idx & 1), j = idx < 8 ? 12 + (idx >> 1) : (idx - 8) >> 1;
    const int w = __builtin_amdgcn_readfirstlane(threadIdx.x >> 6), lane = threadIdx.x & 63;
    const u64 epoch = __hip_atomic_load((const u64*)(p.ws + WS_EPOCH), __ATOMIC_RELAXED, __HIP_MEMORY_SCOPE_AGENT);
    if (w == 0 && lane < 16) *(LDS_AS unsigned*)(size_t)(lds0 + L_CTR + 4 * lane) = 0;
    wait_bar();
    if (w < 4) a_wave(p, lds0, g, j, w, lane, epoch);
    else b_wave(p, lds0, g, j, w - 4, lane, epoch);
}

extern "C" void kernel_launch(void* const* d_in, const int* in_sizes, int n_in, void* d_out, int out_size, void* d_ws, size_t ws_size, hipStream_t stream) {
    Params p;
    p.x = (const unsigned short*)d_in[0]; p.wq = (const unsigned short*)d_in[1]; p.wk = (const unsigned short*)d_in[2];
    p.wv = (const unsigned short*)d_in[3]; p.wo = (const unsigned short*)d_in[4]; p.w1 = (const unsigned short*)d_in[6];
    p.b1 = (const unsigned short*)d_in[7]; p.w2 = (const unsigned short*)d_in[8]; p.b2 = (const unsigned short*)d_in[9];
    p.gw = (const unsigned short*)d_in[10]; p.out = (unsigned short*)d_out; p.ws = (char*)d_ws;
    (void)hipFuncSetAttribute((const void*)gated_mix_fused, hipFuncAttributeMaxDynamicSharedMemorySize, LDS_BYTES);
    gated_mix_fused<<<256, 512, LDS_BYTES, stream>>>(p);
}
```

```cpp
#define BAR_EARLY 1
#define LFFN_GATE 1
#include <hip/hip_runtime.h>
#include <hip/hip_bf16.h>

typedef short bf16x8 __attribute__((ext_vector_type(8)));
typedef short s16x4 __attribute__((ext_vector_type(4)));
typedef __bf16 bf16x2 __attribute__((ext_vector_type(2)));
typedef float f32x16 __attribute__((ext_vector_type(16)));
typedef float f32x4 __attribute__((ext_vector_type(4)));
typedef float f32x2 __attribute__((ext_vector_type(2)));
typedef unsigned u32x4 __attribute__((ext_vector_type(4)));
typedef unsigned u32x2 __attribute__((ext_vector_type(2)));
typedef short v4i16_t __attribute__((ext_vector_type(4)));
typedef unsigned long long u64;
#define LDS_AS __attribute__((address_space(3)))
#define DI __device__ __forceinline__

constexpr int S_ = 2048, D_ = 448, F_ = 1792;
constexpr size_t WS_EPOCH = 0;
constexpr size_t WS_FKV = 65536, FKV_COPY = 1280;
constexpr size_t WS_FH = 163840;
constexpr size_t WS_FO = 294912;
constexpr size_t WS_KV = 1u << 20;
constexpr size_t WS_OF = 2u << 20;
constexpr size_t WS_HF = 4u << 20;

#define STAMP(i) do {} while (0)
#define MFMA(a, b, c) __builtin_amdgcn_mfma_f32_32x32x16_bf16(a, b, c, 0, 0, 0)
#define SB __builtin_amdgcn_sched_barrier(0)
#define EX2(x) __builtin_amdgcn_exp2f(x)
constexpr int SPIN_CAP = 1 << 21;
#define POLL_SLEEP 2
#define G_PRIO 3
#define F_PRIO 0
#define KV_PRIO 3
#define A1_PRIO 2
#define A2_PRIO 0
#define B_PRIO 2

DI unsigned cvtpk(float lo, float hi) { f32x2 v = {lo, hi}; return __builtin_bit_cast(unsigned, __builtin_convertvector(v, bf16x2)); }
DI void glds16(unsigned voff, const void* sbase, unsigned lds_dst) {
    unsigned keep;
    asm volatile("s_mov_b32 %0, m0\n\ts_mov_b32 m0, %3\n\ts_nop 0\n\tglobal_load_lds_dwordx4 %1, %2\n\ts_mov_b32 m0, %0"
                 : "=&s"(keep) : "v"(voff), "s"(sbase), "s"(lds_dst) : "memory");
}
DI void glds16_sc1(unsigned voff, const void* sbase, unsigned lds_dst) {
    unsigned keep;
    asm volatile("s_mov_b32 %0, m0\n\ts_mov_b32 m0, %3\n\ts_nop 0\n\tglobal_load_lds_dwordx4 %1, %2 sc1\n\ts_mov_b32 m0, %0"
                 : "=&s"(keep) : "v"(voff), "s"(sbase), "s"(lds_dst) : "memory");
}
DI bf16x8 lds_b128(unsigned a) { return *(const LDS_AS bf16x8*)(size_t)a; }
DI s16x4 lds_tr(unsigned a) { return __builtin_bit_cast(s16x4, __builtin_amdgcn_ds_read_tr16_b64_v4i16((LDS_AS v4i16_t*)(size_t)a)); }
DI bf16x8 lds_tr2(unsigned a0, unsigned a1) { s16x4 lo = lds_tr(a0), hi = lds_tr(a1); return __builtin_shufflevector(lo, hi, 0, 1, 2, 3, 4, 5, 6, 7); }
DI float dot2(unsigned a, unsigned b, float c) { return __builtin_amdgcn_fdot2_f32_bf16(__builtin_bit_cast(bf16x2, a), __builtin_bit_cast(bf16x2, b), c, false); }
DI u32x4 pack8(const f32x16& a, int s) {
    u32x4 r;
    r[0] = cvtpk(a[8 * s + 0], a[8 * s + 1]); r[1] = cvtpk(a[8 * s + 2], a[8 * s + 3]);
    r[2] = cvtpk(a[8 * s + 4], a[8 * s + 5]); r[3] = cvtpk(a[8 * s + 6], a[8 * s + 7]);
    return r;
}
DI float gelu_tanh(float x) {
    const float c1 = -2.0f * 1.4426950408889634f * 0.7978845608028654f, c2 = c1 * 0.044715f;
    float t = x * x;
    float e = EX2(x * __builtin_fmaf(t, c2, c1));
    return x * __builtin_amdgcn_rcpf(1.0f + e);
}
DI __amdgpu_buffer_rsrc_t ws_rsrc(const char* ws) { return __builtin_amdgcn_make_buffer_rsrc((void*)ws, 0, 16u << 20, 0x00020000); }
DI void st_sc1(__amdgpu_buffer_rsrc_t r, unsigned off, u32x4 v) { __builtin_amdgcn_raw_buffer_store_b128(v, r, off, 0, 16); }
DI void flag_set(char* ws, size_t off, u64 tag) { __hip_atomic_store((u64*)(ws + off), tag, __ATOMIC_RELAXED, __HIP_MEMORY_SCOPE_AGENT); }
DI void flags_wait(const char* ws, unsigned off, bool on, u64 tag) {
#pragma unroll 1
    for (int spin = 0; spin < SPIN_CAP; spin++) {
        u64 v = tag;
        if (on) v = __hip_atomic_load((const u64*)(ws + off), __ATOMIC_RELAXED, __HIP_MEMORY_SCOPE_AGENT);
        if (__builtin_amdgcn_ballot_w64(v != tag) == 0) break;
        __builtin_amdgcn_s_sleep(POLL_SLEEP);
    }
    asm volatile("s_waitcnt vmcnt(0)" ::: "memory");
}
DI u64 uniform64(u64 v) {
    asm volatile("" : "+v"(v) :: "memory");
    const unsigned lo = __builtin_amdgcn_readfirstlane((unsigned)v), hi = __builtin_amdgcn_readfirstlane((unsigned)(v >> 32));
    return ((u64)hi << 32) | lo;
}
DI unsigned lds_ld(unsigned a) { return __hip_atomic_load((LDS_AS unsigned*)(size_t)a, __ATOMIC_RELAXED, __HIP_MEMORY_SCOPE_WORKGROUP); }
DI void lds_st(unsigned a, unsigned v) { __hip_atomic_store((LDS_AS unsigned*)(size_t)a, v, __ATOMIC_RELAXED, __HIP_MEMORY_SCOPE_WORKGROUP); }
DI void lds_sync4(unsigned ctr, unsigned target, int lane) {
    asm volatile("s_waitcnt lgkmcnt(0)" ::: "memory");
    LDS_AS unsigned* c = (LDS_AS unsigned*)(size_t)ctr;
    if (lane == 0) __hip_atomic_fetch_add(c, 1u, __ATOMIC_RELAXED, __HIP_MEMORY_SCOPE_WORKGROUP);
#pragma unroll 1
    for (int spin = 0; spin < SPIN_CAP; spin++) {
        unsigned v = __builtin_amdgcn_readfirstlane(__hip_atomic_load(c, __ATOMIC_RELAXED, __HIP_MEMORY_SCOPE_WORKGROUP));
        if (v >= target) break;
        __builtin_amdgcn_s_sleep(1);
    }
    asm volatile("" ::: "memory");
}

struct Params {
    const unsigned short *x, *wq, *wk, *wv, *wo, *w1, *b1, *w2, *b2, *gw;
    unsigned short* out;
    char* ws;
};

constexpr unsigned X_SLOT = 16384, L_WQ = 114688, L_RING = 143360, L_QX = L_WQ;
constexpr unsigned L_WO = 0, WO_SLICE = 14336, L_MRG = 57344, L_RED = 106496, B_AREA = 12288;
constexpr unsigned P1_GW = 159744, L_G = P1_GW + 1024;
constexpr unsigned L_CTR = L_G + 512;
constexpr unsigned C_QW = 0, C_QR = 4, C_OW = 8, C_RED = 12, C_AP = 16, C_B1 = 20, C_KVS = 24, L_XF = L_CTR + 32, L_XE = L_CTR + 48;
constexpr unsigned LDS_BYTES = L_CTR + 64;
static_assert(L_RING + 4 * 4096 <= P1_GW && L_RED + 4 * B_AREA <= P1_GW && L_MRG + 12 * 4096 <= L_RED && LDS_BYTES <= 163840 && (L_XF & 15) == 0, "LDS map");

template <int N> DI void wait_vm() { asm volatile("s_waitcnt vmcnt(%0)" ::"n"(N) : "memory"); }
DI void wait_bar() { asm volatile("s_waitcnt lgkmcnt(0)\n\ts_barrier" ::: "memory"); }
DI u32x4 lds_flags(unsigned a) { u32x4 v; asm volatile("ds_read_b128 %0, %1" : "=v"(v) : "v"(a) : "memory"); return v; }
DI void lds_wait_ge(unsigned a, unsigned need) {
#pragma unroll 1
    for (int spin = 0; spin < SPIN_CAP; spin++) {
        if ((unsigned)__builtin_amdgcn_readfirstlane(lds_ld(a)) >= need) break;
        __builtin_amdgcn_s_sleep(1);
    }
    asm volatile("" ::: "memory");
}
DI void x_wait(unsigned xf, unsigned need, int only) {
#pragma unroll 1
    for (int spin = 0; spin < SPIN_CAP; spin++) {
        unsigned m;
        if (only < 0) {
            const unsigned a = lds_ld(xf), b = lds_ld(xf + 4), c = lds_ld(xf + 8), d = lds_ld(xf + 12);
            m = min(min(a, b), min(c, d));
        } else m = lds_ld(xf + 4 * only);
        if ((unsigned)__builtin_amdgcn_readfirstlane(m) >= need) break;
        __builtin_amdgcn_s_sleep(1);
    }
    asm volatile("" ::: "memory");
}
DI unsigned x_addr(unsigned lds0, int lane, int s) {
    const int h = lane >> 5, l31 = lane & 31;
    return lds0 + l31 * 128 + (((2 * s + h) ^ ((l31 >> 1) & 7)) << 4);
}
DI unsigned w_lane(int lane) { return (lane >> 5) * 512 + ((lane & 15) >> 2) * 64 + ((lane >> 4) & 1) * 32 + (lane & 3) * 8; }
#define NR1 8
template <typename Ld>
DI void proj_loop(f32x16 (&acc)[4], unsigned lds0, unsigned ring, int lane, bool light, Ld&& ld, unsigned long long* ptb) {
    constexpr int NR = NR1;
    u32x4 R[NR];
#define W1_DELAY 3
    if (light) { if (LFFN_GATE) lds_wait_ge(lds0 + L_CTR + C_KVS, 2); else x_wait(lds0 + L_XE, 1, -1); }
    else if (W1_DELAY > 0) x_wait(lds0 + L_XF, W1_DELAY, -1);
#pragma unroll
    for (int s = 0; s < NR; s++) R[s] = ld(s);
    unsigned xa[4];
#pragma unroll
    for (int s = 0; s < 4; s++) xa[s] = x_addr(lds0, lane, s);
    const unsigned wr = ring + lane * 16, rd = ring + w_lane(lane), xf = lds0 + L_XF;
    bf16x8 xf_[3][4], wf[3];
#define PWRITE(s_) do { *(LDS_AS u32x4*)(size_t)(wr + ((s_) & 3) * 1024) = R[(s_) % NR]; if ((s_) + NR < 28) R[(s_) % NR] = ld((s_) + NR); } while (0)
#define PREADW(s_) do { wf[(s_) % 3] = lds_tr2(rd + ((s_) & 3) * 1024, rd + ((s_) & 3) * 1024 + 256); } while (0)
#define PREADX(s_, t_) do { xf_[(s_) % 3][t_] = lds_b128(xa[(s_) & 3] + ((s_) >> 2) * X_SLOT + (t_) * 4096); } while (0)
    PWRITE(0); PWRITE(1); PWRITE(2);
    x_wait(xf, 1, -1);
    PREADW(0); PREADX(0, 0); PREADX(0, 1); PREADX(0, 2); PREADX(0, 3);
    PREADW(1); PREADX(1, 0); PREADX(1, 1); PREADX(1, 2); PREADX(1, 3);
#pragma unroll
    for (int s = 0; s < 28; s++) {
        SB;
        if ((s & 3) == 2 && s + 2 < 28) x_wait(xf, (s + 2) / 4 + 1, -1);
        acc[0] = MFMA(wf[s % 3], xf_[s % 3][0], acc[0]); SB;
        if (s + 2 < 28) { PREADW(s + 2); PREADX(s + 2, 0); } SB;
        acc[1] = MFMA(wf[s % 3], xf_[s % 3][1], acc[1]); SB;
        if (s + 2 < 28) { PREADX(s + 2, 1); PREADX(s + 2, 2); } SB;
        acc[2] = MFMA(wf[s % 3], xf_[s % 3][2], acc[2]); SB;
        if (s + 2 < 28) PREADX(s + 2, 3);
        if (s + 3 < 28) PWRITE(s + 3);
        SB;
        acc[3] = MFMA(wf[s % 3], xf_[s % 3][3], acc[3]);
    }
    SB;
#undef PWRITE
#undef PREADW
#undef PREADX
}
DI void gelu_store(const Params& p, const f32x16& acc, const u32x2 (&bv)[4], int T, int fb, int lane) {
    f32x16 a;
#pragma unroll
    for (int q = 0; q < 4; q++) {
        a[4 * q + 0] = acc[4 * q + 0] + __uint_as_float(bv[q][0] << 16); a[4 * q + 1] = acc[4 * q + 1] + __uint_as_float(bv[q][0] & 0xffff0000u);
        a[4 * q + 2] = acc[4 * q + 2] + __uint_as_float(bv[q][1] << 16); a[4 * q + 3] = acc[4 * q + 3] + __uint_as_float(bv[q][1] & 0xffff0000u);
    }
#pragma unroll
    for (int r = 0; r < 16; r++) a[r] = gelu_tanh(a[r]);
    const unsigned dst = (unsigned)WS_HF + (T * 112 + 2 * fb) * 1024 + lane * 16;
    const __amdgpu_buffer_rsrc_t r_ = ws_rsrc(p.ws);
    st_sc1(r_, dst, pack8(a, 0)); st_sc1(r_, dst + 1024, pack8(a, 1));
}

DI float max16(const f32x16& s) {
    float a = __builtin_fmaxf(__builtin_fmaxf(s[0], s[1]), s[2]), b = __builtin_fmaxf(__builtin_fmaxf(s[3], s[4]), s[5]);
#pragma unroll
    for (int r = 6; r < 14; r += 4) { a = __builtin_fmaxf(__builtin_fmaxf(a, s[r]), s[r + 1]); b = __builtin_fmaxf(__builtin_fmaxf(b, s[r + 2]), s[r + 3]); }
    a = __builtin_fmaxf(__builtin_fmaxf(a, s[14]), s[15]);
    return __builtin_fmaxf(a, b);
}
DI void set_max(bf16x8& q1, float m, int h) {
    auto rr = __builtin_amdgcn_permlane32_swap(__float_as_uint(m), __float_as_uint(m), false, false);
    const float nm = -__builtin_fmaxf(__uint_as_float(rr[0]), __uint_as_float(rr[1]));
    const unsigned a = cvtpk(nm, 0.f) & 0xffffu;
    const float r1 = nm - __uint_as_float(a << 16);
    const unsigned b = cvtpk(r1, 0.f) & 0xffffu;
    const float r2 = r1 - __uint_as_float(b << 16);
    const unsigned c = cvtpk(r2, 0.f) & 0xffffu;
    u32x4 t = __builtin_bit_cast(u32x4, q1);
    if (h == 0) { t[2] = a | (b << 16); t[3] = c; }
    q1 = __builtin_bit_cast(bf16x8, t);
}
DI bf16x8 ld_sc1s(__amdgpu_buffer_rsrc_t r, unsigned off, unsigned soff) { return __builtin_bit_cast(bf16x8, __builtin_amdgcn_raw_buffer_load_b128(r, off, soff, 16)); }
DI bf16x8 ld_sc1(__amdgpu_buffer_rsrc_t r, unsigned off) { return __builtin_bit_cast(bf16x8, __builtin_amdgcn_raw_buffer_load_b128(r, off, 0, 16)); }

DI void a_wave(const Params& p, unsigned lds0, int g, int j, int w, int lane, u64 epoch_v) {
    const int h = lane >> 5, l31 = lane & 31;
    const int T = 4 * g + w;
    f32x16 accq = {};
    STAMP(0);
    __builtin_amdgcn_s_setprio(A1_PRIO);
    unsigned xoff[4];
#pragma unroll
    for (int i = 0; i < 4; i++) {
        const int row = 32 * w + 8 * i + (lane >> 3);
        const int c = (lane & 7) ^ ((row >> 1) & 7);
        xoff[i] = row * 896 + c * 16;
    }
    const char* xb = (const char*)p.x + (size_t)g * 128 * 896;
    const unsigned qoff = (16 * w + (lane >> 2)) * 896 + ((lane & 3) == 3 ? 40 : (lane & 3) * 16);
    const char* qb_ = (const char*)p.wq + j * 56;
    glds16((lane < 56 ? lane : 55) * 16, p.gw, lds0 + P1_GW);
    auto dma = [&](int c) {
#pragma unroll
        for (int i = 0; i < 4; i++) glds16(xoff[i], xb + c * 128, lds0 + c * X_SLOT + (32 * w + 8 * i) * 128);
        glds16(qoff, qb_ + c * (64 * 896), lds0 + L_WQ + c * 4096 + w * 1024);
    };
    const bool light = j >= 12;
    if (light) {
        const int row = 32 * (j - 12) + 8 * w + (lane >> 3);
        const unsigned eo = row * 896 + (((lane & 7) ^ ((row >> 1) & 7)) << 4);
#pragma unroll
        for (int c = 0; c < 7; c++) glds16(eo, xb + c * 128, lds0 + c * X_SLOT + (32 * (j - 12) + 8 * w) * 128);
    }
    dma(0);
    if (!light) { dma(1); dma(2); }
    if (light) {
        wait_vm<5>();
        if (lane == 0) lds_st(lds0 + L_XE + 4 * w, 1);
        dma(1); dma(2);
    }
    u64 tag;
    const __amdgpu_buffer_rsrc_t wsr = ws_rsrc(p.ws);
    const int t0 = (4 * g + 4 * j + 16 * w) & 63;
    bf16x8 K0[4], K1[4], V0[4], V1[4];
    const unsigned kvb = (unsigned)WS_KV + lane * 16;
#define KVLOAD(set, vo) do { const unsigned o_ = (vo); K0[set] = ld_sc1(wsr, o_); K1[set] = ld_sc1(wsr, o_ + 1024); V0[set] = ld_sc1(wsr, o_ + 2048); V1[set] = ld_sc1(wsr, o_ + 3072); } while (0)
    {
        unsigned xa[4];
#pragma unroll
        for (int s = 0; s < 4; s++) xa[s] = x_addr(lds0, lane, s) + w * 4096;
        const unsigned wa = lds0 + L_WQ + w_lane(lane);
        unsigned short gwv[7];
        auto publish = [&](int c) {
            if (lane < 16) *(LDS_AS unsigned short*)(size_t)(lds0 + L_WQ + c * 4096 + w * 1024 + lane * 64 + 48) = gwv[c];
            asm volatile("s_waitcnt lgkmcnt(0)" ::: "memory");
            if (lane == 0) lds_st(lds0 + L_XF + 4 * w, c + 1);
        };
        auto qchunk = [&](int c) {
            bf16x8 xf[4], wf[4];
#pragma unroll
            for (int s = 0; s < 4; s++) { xf[s] = lds_b128(xa[s] + c * X_SLOT); wf[s] = lds_tr2(wa + c * 4096 + s * 1024, wa + c * 4096 + s * 1024 + 256); }
#pragma unroll
            for (int s = 0; s < 4; s++) accq = MFMA(wf[s], xf[s], accq);
        };
#define TC(c)
        wait_vm<10>(); TC(0);
#pragma unroll
        for (int c = 0; c < 7; c++) gwv[c] = *(const LDS_AS unsigned short*)(size_t)(lds0 + P1_GW + (64 * c + 16 * w + (lane & 15)) * 2);
        publish(0);
        if (!light) { dma(3); wait_vm<10>(); } else wait_vm<5>();
        TC(1); publish(1);
        if (!light) { dma(4); wait_vm<10>(); } else wait_vm<0>();
        TC(2); publish(2);
        if (light) {
            lds_wait_ge(lds0 + L_CTR + C_KVS, 2);
            dma(3); dma(4);
        }
        dma(5);
        wait_vm<10>(); TC(3); publish(3); dma(6);
        wait_vm<10>(); TC(4); publish(4);
        x_wait(lds0 + L_XF, 2, -1); qchunk(0); qchunk(1); SB;
        wait_vm<5>(); TC(5); publish(5);
        x_wait(lds0 + L_XF, 4, -1); qchunk(2); qchunk(3); SB;
        wait_vm<0>(); TC(6); publish(6);
        tag = uniform64(epoch_v) + 1;
        const unsigned fo = WS_FKV + (g * 4 + (j >> 2)) * FKV_COPY + ((t0 + (lane >> 1)) & 63) * 16 + (lane & 1) * 8;
        u64 fv = tag;
        if (lane < 32) fv = __hip_atomic_load((const u64*)(p.ws + fo), __ATOMIC_RELAXED, __HIP_MEMORY_SCOPE_AGENT);
        x_wait(lds0 + L_XF, 6, -1); TC(8); qchunk(4); qchunk(5); SB; TC(9);
        x_wait(lds0 + L_XF, 7, -1); TC(11); qchunk(6); SB; TC(12);
        asm volatile("" : "+v"(fv) :: "memory");
        if (__builtin_amdgcn_ballot_w64(fv != tag) != 0) flags_wait(p.ws, fo, lane < 32, tag);
        TC(10);
        KVLOAD(0, kvb + t0 * 4096); KVLOAD(1, kvb + (t0 + 1) * 4096); KVLOAD(2, kvb + (t0 + 2) * 4096);
        SB;
    }
    const float zz = __uint_as_float(__builtin_amdgcn_permlane32_swap(__float_as_uint(accq[12]), __float_as_uint(accq[12]), false, false)[0]);
    const float gtok = __builtin_amdgcn_rcpf(1.0f + EX2(-1.4426950408889634f * zz));
    if (h == 0) *(LDS_AS float*)(size_t)(lds0 + L_G + (32 * w + l31) * 4) = gtok;
    bf16x8 q0[4], q1[4];
    bf16x8 qa, qb;
    {
        const float qs = 1.4426950408889634f * 0.1889822365046136f;
        f32x16 q = accq * qs;
        u32x4 a0 = pack8(q, 0), a1 = pack8(q, 1);
        if (h == 0) { a1[2] = 0; a1[3] = 0; }
        qa = __builtin_bit_cast(bf16x8, a0); qb = __builtin_bit_cast(bf16x8, a1);
    }
    asm volatile("s_waitcnt lgkmcnt(0)" ::: "memory");
    if (lane == 0) __hip_atomic_fetch_add((LDS_AS unsigned*)(size_t)(lds0 + L_CTR + C_AP), 1u, __ATOMIC_RELAXED, __HIP_MEMORY_SCOPE_WORKGROUP);
    const f32x16 zero = {};
    V1[3] = (bf16x8){}; K0[3] = V1[3]; K1[3] = V1[3]; V0[3] = V1[3];
    f32x16 S = MFMA(K0[0], qa, zero); S = MFMA(K1[0], qb, S);
    set_max(qb, max16(S), h);
    const unsigned lo = lane * 16;
    *(LDS_AS bf16x8*)(size_t)(lds0 + L_QX + w * 2048 + lo) = qa;
    *(LDS_AS bf16x8*)(size_t)(lds0 + L_QX + w * 2048 + 1024 + lo) = qb;
    lds_sync4(lds0 + L_CTR + C_QW, 4, lane);
#pragma unroll
    for (int b = 0; b < 4; b++) { q0[b] = lds_b128(lds0 + L_QX + b * 2048 + lo); q1[b] = lds_b128(lds0 + L_QX + b * 2048 + 1024 + lo); }
    asm volatile("s_waitcnt lgkmcnt(0)" ::: "memory");
    if (lane == 0) __hip_atomic_fetch_add((LDS_AS unsigned*)(size_t)(lds0 + L_CTR + C_QR), 1u, __ATOMIC_RELAXED, __HIP_MEMORY_SCOPE_WORKGROUP);
    TC(13);
    __builtin_amdgcn_s_setprio(A2_PRIO);
    S = MFMA(K0[0], q0[0], zero); S = MFMA(K1[0], q1[0], S);
    f32x16 O[4];
#pragma unroll
    for (int b = 0; b < 4; b++) O[b] = zero;
    u32x4 P1 = {0, 0, 0, 0};
#define ASTEP(c, b) do { \
        constexpr int pc = (b) == 0 ? ((c) + 3) & 3 : (c), pb = ((b) + 3) & 3, nc = (b) == 3 ? ((c) + 1) & 3 : (c), nb = ((b) + 1) & 3; \
        O[pb] = MFMA(V1[pc], __builtin_bit_cast(bf16x8, P1), O[pb]); SB; \
        u32x4 p0, p1; \
        p0[0] = cvtpk(EX2(S[0]), EX2(S[1])); p0[1] = cvtpk(EX2(S[2]), EX2(S[3])); SB; \
        f32x16 Sn = MFMA(K0[nc], q0[nb], zero); SB; \
        if ((b) == 1) { KVLOAD(pc3(c), vnext); } \
        p0[2] = cvtpk(EX2(S[4]), EX2(S[5])); p0[3] = cvtpk(EX2(S[6]), EX2(S[7])); SB; \
        Sn = MFMA(K1[nc], q1[nb], Sn); SB; \
        p1[0] = cvtpk(EX2(S[8]), EX2(S[9])); p1[1] = cvtpk(EX2(S[10]), EX2(S[11])); SB; \
        O[b] = MFMA(V0[c], __builtin_bit_cast(bf16x8, p0), O[b]); SB; \
        p1[2] = cvtpk(EX2(S[12]), EX2(S[13])); p1[3] = cvtpk(EX2(S[14]), EX2(S[15])); SB; \
        P1 = p1; S = Sn; } while (0)
#define pc3(c) (((c) + 3) & 3)
#define ATILE(c) do { \
        const int tn = tt + (c) + 3;     \
        const unsigned vnext = lo + (tn < 16 ? (unsigned)WS_KV + ((t0 + tn) & 63) * 4096u : 0x7f000000u); \
        ASTEP(c, 0); ASTEP(c, 1); ASTEP(c, 2); ASTEP(c, 3); } while (0)
#pragma unroll 1
    for (int tt = 0; tt < 16; tt += 4) {
        ATILE(0); ATILE(1); ATILE(2); ATILE(3);
    }
    O[3] = MFMA(V1[3], __builtin_bit_cast(bf16x8, P1), O[3]);
    STAMP(5);
    lds_wait_ge(lds0 + L_CTR + C_B1, 4);
#pragma unroll
    for (int b = 0; b < 4; b++) {
        if (b != w) {
            const unsigned a = lds0 + L_MRG + (b * 3 + ((w - b - 1) & 3)) * 4096 + lo;
#pragma unroll
            for (int rg = 0; rg < 4; rg++) {
                f32x4 v = {O[b][4 * rg], O[b][4 * rg + 1], O[b][4 * rg + 2], O[b][4 * rg + 3]};
                *(LDS_AS f32x4*)(size_t)(a + rg * 1024) = v;
            }
        }
    }
    f32x16 o = zero;
#pragma unroll
    for (int b = 0; b < 4; b++) if (b == w) { o = O[b]; qa = q0[b]; qb = q1[b]; }
    lds_sync4(lds0 + L_CTR + C_OW, 4, lane);
#pragma unroll
    for (int u = 0; u < 3; u++) {
        const unsigned a = lds0 + L_MRG + (w * 3 + u) * 4096 + lo;
#pragma unroll
        for (int rg = 0; rg < 4; rg++) {
            f32x4 v = *(const LDS_AS f32x4*)(size_t)(a + rg * 1024);
            o[4 * rg] += v[0]; o[4 * rg + 1] += v[1]; o[4 * rg + 2] += v[2]; o[4 * rg + 3] += v[3];
        }
    }
    STAMP(6);
    auto rr = __builtin_amdgcn_permlane32_swap(__float_as_uint(o[12]), __float_as_uint(o[12]), false, false);
    float l = __uint_as_float(rr[0]);
    {
        bool ok = l < 1.152921504606846976e18f && l > 8.673617379884035e-19f;
#pragma unroll
        for (int r = 0; r < 16; r++) ok = ok && (__builtin_fabsf(o[r]) < 3.0e38f);
        if (__builtin_amdgcn_ballot_w64(!ok) != 0) {
            u32x4 tq = __builtin_bit_cast(u32x4, qb);
            if (h == 0) { tq[2] = 0; tq[3] = 0; }
            qb = __builtin_bit_cast(bf16x8, tq);
            float m = -INFINITY;
#pragma unroll 1
            for (int t = 0; t < 64; t++) {
                bf16x8 k0 = ld_sc1(wsr, kvb + t * 4096), k1 = ld_sc1(wsr, kvb + t * 4096 + 1024);
                f32x16 s = MFMA(k0, qa, zero); s = MFMA(k1, qb, s);
                m = __builtin_fmaxf(m, max16(s));
            }
            set_max(qb, m, h);
            o = zero;
#pragma unroll 1
            for (int t = 0; t < 64; t++) {
                bf16x8 k0 = ld_sc1(wsr, kvb + t * 4096), k1 = ld_sc1(wsr, kvb + t * 4096 + 1024);
                bf16x8 v0 = ld_sc1(wsr, kvb + t * 4096 + 2048), v1 = ld_sc1(wsr, kvb + t * 4096 + 3072);
                f32x16 s = MFMA(k0, qa, zero); s = MFMA(k1, qb, s);
#pragma unroll
                for (int r = 0; r < 16; r++) s[r] = EX2(s[r]);
                o = MFMA(v0, __builtin_bit_cast(bf16x8, pack8(s, 0)), o); o = MFMA(v1, __builtin_bit_cast(bf16x8, pack8(s, 1)), o);
            }
            auto r2 = __builtin_amdgcn_permlane32_swap(__float_as_uint(o[12]), __float_as_uint(o[12]), false, false);
            l = __uint_as_float(r2[0]);
        }
    }
    const float sc = gtok * __builtin_amdgcn_rcpf(l);
#pragma unroll
    for (int r = 0; r < 16; r++) o[r] *= sc;
    u32x4 o0 = pack8(o, 0), o1 = pack8(o, 1);
    if (h == 0) { o1[2] = 0; o1[3] = 0; }
    const unsigned dst = (unsigned)WS_OF + (T * 32 + 2 * j) * 1024 + lo;
    st_sc1(wsr, dst, o0); st_sc1(wsr, dst + 1024, o1);
    asm volatile("s_waitcnt vmcnt(0)" ::: "memory");
    if (lane < 7) flag_set(p.ws, WS_FO + (T * 7 + lane) * 128 + j * 8, tag);
    STAMP(7);
}

DI void b_wave(const Params& p, unsigned lds0, int g, int j, int wb, int lane, u64 epoch_v) {
    const int h = lane >> 5, l31 = lane & 31;
    const bool light = j >= 12;
    const int lp = j - 12;
    const int fb = light ? 48 + 2 * lp + wb : 4 * j + wb;
    const unsigned lo = lane * 16;
    f32x16 acc[4];
#pragma unroll
    for (int b = 0; b < 4; b++) acc[b] = (f32x16){};
    STAMP(0);
    __builtin_amdgcn_s_setprio(B_PRIO);
    const __amdgpu_buffer_rsrc_t wsr = ws_rsrc(p.ws);
    const unsigned ring = lds0 + L_RING + wb * 4096;
    u64 tag;
    if (light && wb >= 2) {
        __builtin_amdgcn_s_setprio(KV_PRIO);
        const int T = 4 * g + lp;
        const __amdgpu_buffer_rsrc_t rk = __builtin_amdgcn_make_buffer_rsrc((void*)(wb == 2 ? p.wk : p.wv), 0, 448 * 56, 0x00020000);
        const unsigned off = (lane >> 2) * 56 + ((lane & 3) == 3 ? 40 : (lane & 3) * 16);
        const unsigned dst = (unsigned)WS_KV + T * 4096 + lo;
        u32x4 R[28];
#pragma unroll
        for (int s = 0; s < 28; s++) R[s] = __builtin_amdgcn_raw_buffer_load_b128(rk, off, s * (16 * 56), 0);
        const unsigned wr = ring + lo, rd = ring + w_lane(lane);
        bf16x8 wf[28];
#pragma unroll
        for (int q = 0; q < 28; q += 4) {
#pragma unroll
            for (int u = 0; u < 4; u++) *(LDS_AS u32x4*)(size_t)(wr + u * 1024) = R[q + u];
#pragma unroll
            for (int u = 0; u < 4; u++) wf[q + u] = lds_tr2(rd + u * 1024, rd + u * 1024 + 256);
            SB;
        }
        STAMP(10);
        x_wait(lds0 + L_XE, 1, -1);
        STAMP(11);
        f32x16 a2 = {};
        {
            unsigned xa[4];
#pragma unroll
            for (int s = 0; s < 4; s++) xa[s] = x_addr(lds0, lane, s) + lp * 4096;
            bf16x8 xf[2][4];
#define KVX(q_) _Pragma("unroll") for (int u = 0; u < 4; u++) xf[(q_) & 1][u] = lds_b128(xa[u] + (q_) * X_SLOT)
            KVX(0); KVX(1);
            if (wb == 2) {
#pragma unroll
                for (int q = 0; q < 7; q++) {
                    SB;
#pragma unroll
                    for (int u = 0; u < 4; u++) { if (u & 1) a2 = MFMA(wf[4 * q + u], xf[q & 1][u], a2); else acc[0] = MFMA(wf[4 * q + u], xf[q & 1][u], acc[0]); }
                    SB;
                    if (q + 2 < 7) { KVX(q + 2); }
                }
            } else {
#pragma unroll
                for (int q = 0; q < 7; q++) {
                    SB;
#pragma unroll
                    for (int u = 0; u < 4; u++) { if (u & 1) a2 = MFMA(xf[q & 1][u], wf[4 * q + u], a2); else acc[0] = MFMA(xf[q & 1][u], wf[4 * q + u], acc[0]); }
                    SB;
                    if (q + 2 < 7) { KVX(q + 2); }
                }
            }
        }
        acc[0] += a2;
        STAMP(13);
        {
            u32x4 a0 = pack8(acc[0], 0), a1 = pack8(acc[0], 1);
            if (wb == 2) {
                if (h == 0) { a1[2] = 0x3f803f80u; a1[3] = 0x00003f80u; }
                st_sc1(wsr, dst, a0); st_sc1(wsr, dst + 1024, a1);
            } else {
                if (l31 == 24) { a0 = (u32x4){0x3f803f80u, 0x3f803f80u, 0x3f803f80u, 0x3f803f80u}; a1 = a0; }
                else if (l31 > 24 && l31 < 28) { a0 = (u32x4){0, 0, 0, 0}; a1 = a0; }
                st_sc1(wsr, dst + 2048, a0); st_sc1(wsr, dst + 3072, a1);
            }
        }
        STAMP(1);
        if (lane == 0) __hip_atomic_fetch_add((LDS_AS unsigned*)(size_t)(lds0 + L_CTR + C_B1), 1u, __ATOMIC_RELAXED, __HIP_MEMORY_SCOPE_WORKGROUP);
#define KVS_AT 1
        if (KVS_AT == 0 && lane == 0) __hip_atomic_fetch_add((LDS_AS unsigned*)(size_t)(lds0 + L_CTR + C_KVS), 1u, __ATOMIC_RELAXED, __HIP_MEMORY_SCOPE_WORKGROUP);
        tag = uniform64(epoch_v) + 1;
        asm volatile("s_waitcnt vmcnt(0)" ::: "memory");
        STAMP(14);
        flag_set(p.ws, WS_FKV + lane * FKV_COPY + T * 16 + (wb & 1) * 8, tag);
        if (KVS_AT == 1 && lane == 0) __hip_atomic_fetch_add((LDS_AS unsigned*)(size_t)(lds0 + L_CTR + C_KVS), 1u, __ATOMIC_RELAXED, __HIP_MEMORY_SCOPE_WORKGROUP);
        STAMP(15);
    } else {
        const __amdgpu_buffer_rsrc_t rw1 = __builtin_amdgcn_make_buffer_rsrc((void*)p.w1, 0, 448 * 3584, 0x00020000);
        const unsigned off = (lane >> 2) * 3584 + fb * 64 + (lane & 3) * 16;
        u32x2 bv[4];
#pragma unroll
        for (int q = 0; q < 4; q++) bv[q] = *(const u32x2*)(p.b1 + fb * 32 + 4 * h + 8 * q);
        auto ld = [&](int s) { return __builtin_amdgcn_raw_buffer_load_b128(rw1, off, s * (16 * 3584), 0); };
        unsigned long long ptb[8];
        proj_loop(acc, lds0, ring, lane, light, ld, ptb);
        STAMP(1);
        if (lane == 0) __hip_atomic_fetch_add((LDS_AS unsigned*)(size_t)(lds0 + L_CTR + C_B1), 1u, __ATOMIC_RELAXED, __HIP_MEMORY_SCOPE_WORKGROUP);
        tag = uniform64(epoch_v) + 1;
        __builtin_amdgcn_s_setprio(G_PRIO);
#pragma unroll
        for (int t = 0; t < 4; t++) gelu_store(p, acc[t], bv, 4 * g + t, fb, lane);
        __builtin_amdgcn_s_setprio(F_PRIO);
        STAMP(12);
        asm volatile("s_waitcnt vmcnt(0)" ::: "memory");
        if (lane < 14) flag_set(p.ws, WS_FH + (g * 14 + lane) * 512 + fb * 8, tag);
        STAMP(3);

    }
    const bool tile = j < 14;
    if (!tile) {
        if (g == 0 && j == 15 && wb == 0) {
#pragma unroll 1
            for (int stage = 0; stage < 3; stage++) {
                const char* base = p.ws + (stage == 0 ? WS_FKV + lane * 8 : stage == 1 ? WS_FH + lane * 8 : WS_FO + (lane >> 4) * (7 * 128) + (lane & 15) * 8);
                const int n = stage == 0 ? 2 : 16;
                const unsigned step = stage == 0 ? 512 : stage == 1 ? 14 * 512 : 4 * 7 * 128;
                const bool on = stage != 1 || lane < 56;
#pragma unroll 1
                for (int spin = 0; spin < SPIN_CAP; spin++) {
                    u64 v[16];
#pragma unroll
                    for (int i = 0; i < 16; i++) v[i] = __hip_atomic_load((const u64*)(base + (i < n ? i : 0) * step), __ATOMIC_RELAXED, __HIP_MEMORY_SCOPE_AGENT);
                    unsigned bad = 0;
#pragma unroll
                    for (int i = 0; i < 16; i++) bad |= (v[i] != tag) ? 1u : 0u;
                    if (__builtin_amdgcn_ballot_w64(on && bad) == 0) break;
                    __builtin_amdgcn_s_sleep(16);
                }
            }
            if (lane == 0) __hip_atomic_store((u64*)(p.ws + WS_EPOCH), tag, __ATOMIC_RELAXED, __HIP_MEMORY_SCOPE_AGENT);
        }
        return;
    }
    const unsigned stg = lds0 + L_WO + wb * WO_SLICE;
    const int th = j & 1, cs = j >> 1, n0 = 64 * cs, T0 = 4 * g + 2 * th;
    const unsigned woff = ((lane & 31) >> 2) * 896 + (lane >> 5) * 64 + (lane & 3) * 16;
    const __amdgpu_buffer_rsrc_t rw2 = __builtin_amdgcn_make_buffer_rsrc((void*)p.w2, 0, 1792 * 896, 0x00020000);
    const unsigned w2v = woff + n0 * 2, w2s = 28 * wb * 16 * 896;
    constexpr int NB = 7;
    u32x4 Wr[NB][2];
#define ROT28(st_) (st_)
#define WLOAD(st_) do { const unsigned so_ = w2s + ROT28(st_) * (16 * 896); Wr[(st_) % NB][0] = __builtin_amdgcn_raw_buffer_load_b128(rw2, w2v, so_, 0); \
        Wr[(st_) % NB][1] = __builtin_amdgcn_raw_buffer_load_b128(rw2, w2v + 8 * 896, so_, 0); } while (0)
#pragma unroll
    for (int st = 0; st < NB; st++) WLOAD(st);
    const int Tm = T0 + (wb & 1);
    const int nc = n0 + 32 * (wb >> 1) + 4 * h;
    u32x2 b2v[4];
#pragma unroll
    for (int rg = 0; rg < 4; rg++) b2v[rg] = *(const u32x2*)(p.b2 + nc + 8 * rg);
    const unsigned fho = WS_FH + (g * 14 + j) * 512 + (14 * wb + lane) * 8;
    flags_wait(p.ws, fho, lane < 6, tag);
    lds_wait_ge(lds0 + L_CTR + C_AP, 4);
    lds_wait_ge(lds0 + L_CTR + C_B1, 4);
    STAMP(4);
#pragma unroll
    for (int i = 0; i < 4; i++) acc[i] = (f32x16){};
    {
        const unsigned wrd = stg + (4 * h + ((lane & 15) >> 2)) * 64 + ((lane >> 4) & 1) * 32 + (lane & 3) * 8;
        const unsigned hv = (unsigned)WS_HF + (T0 * 112 + 28 * wb) * 1024 + lo;
        bf16x8 H0[NB], H1[NB], wf0[2], wf1[2];
#define HLOAD(st_) do { const unsigned so_ = ROT28(st_) * 1024; H0[(st_) % NB] = ld_sc1s(wsr, hv, so_); H1[(st_) % NB] = ld_sc1s(wsr, hv + 112 * 1024, so_); } while (0)
#define WWRITE(st_) do { *(LDS_AS u32x4*)(size_t)(stg + ((st_) & 1) * 2048 + lo) = Wr[(st_) % NB][0]; *(LDS_AS u32x4*)(size_t)(stg + ((st_) & 1) * 2048 + 1024 + lo) = Wr[(st_) % NB][1]; } while (0)
#define WREAD(st_) do { wf0[(st_) & 1] = lds_tr2(wrd + ((st_) & 1) * 2048, wrd + ((st_) & 1) * 2048 + 1024); wf1[(st_) & 1] = lds_tr2(wrd + ((st_) & 1) * 2048 + 512, wrd + ((st_) & 1) * 2048 + 1536); } while (0)
#pragma unroll
        for (int st = 0; st < NB; st++) HLOAD(st);
        WWRITE(0); WLOAD(NB); WWRITE(1); WLOAD(NB + 1);
        WREAD(0);
#pragma unroll
        for (int st = 0; st < 28; st++) {
            SB;
            if (st + 1 < 28) WREAD(st + 1);
            if (st + 2 < 28) WWRITE(st + 2);
            if (st + 2 + NB < 28) WLOAD(st + 2 + NB);
            SB;
            acc[0] = MFMA(wf0[st & 1], H0[st % NB], acc[0]); acc[1] = MFMA(wf0[st & 1], H1[st % NB], acc[1]);
            acc[2] = MFMA(wf1[st & 1], H0[st % NB], acc[2]); acc[3] = MFMA(wf1[st & 1], H1[st % NB], acc[3]);
            SB;
            if (st + NB == 12) {
                u64 fv = tag;
                if (lane >= 6 && lane < 14) fv = __hip_atomic_load((const u64*)(p.ws + fho), __ATOMIC_RELAXED, __HIP_MEMORY_SCOPE_AGENT);
                if (__builtin_amdgcn_ballot_w64(fv != tag) != 0) flags_wait(p.ws, fho, lane >= 6 && lane < 14, tag);
                asm volatile("" ::: "memory");
            }
            if (st + NB < 28) HLOAD(st + NB);
        }
    }
    STAMP(5);
    asm volatile("s_waitcnt lgkmcnt(0)" ::: "memory");
    {
        const char* wos = (const char*)p.wo + n0 * 2;
#pragma unroll
        for (int i = 0; i < 14; i++) glds16(woff, wos + (size_t)(14 * wb + i) * (8 * 896), lds0 + L_WO + (14 * wb + i) * 1024);
    }
    lds_wait_ge(lds0 + L_CTR + C_QR, 4);
    {
        const unsigned rarea = lds0 + L_RED + wb * B_AREA;
#pragma unroll
        for (int i = 0; i < 4; i++) {
            if (i != wb) {
                const unsigned a = rarea + ((i - wb - 1) & 3) * 4096 + lo;
#pragma unroll
                for (int rg = 0; rg < 4; rg++) {
                    f32x4 v = {acc[i][4 * rg], acc[i][4 * rg + 1], acc[i][4 * rg + 2], acc[i][4 * rg + 3]};
                    *(LDS_AS f32x4*)(size_t)(a + rg * 1024) = v;
                }
            }
        }
    }
    f32x16 o = {};
#pragma unroll
    for (int i = 0; i < 4; i++) if (i == wb) o = acc[i];
    asm volatile("s_waitcnt vmcnt(0)" ::: "memory");
    lds_sync4(lds0 + L_CTR + C_RED, 4, lane);
#pragma unroll
    for (int u = 0; u < 4; u++) {
        if (u != wb) {
            const unsigned a = lds0 + L_RED + u * B_AREA + ((wb - u - 1) & 3) * 4096 + lo;
#pragma unroll
            for (int rg = 0; rg < 4; rg++) {
                f32x4 v = *(const LDS_AS f32x4*)(size_t)(a + rg * 1024);
                o[4 * rg] += v[0]; o[4 * rg + 1] += v[1]; o[4 * rg + 2] += v[2]; o[4 * rg + 3] += v[3];
            }
        }
    }
    {
        const float fg = 1.0f - *(const LDS_AS float*)(size_t)(lds0 + L_G + (64 * th + 32 * (wb & 1) + l31) * 4);
#pragma unroll
        for (int rg = 0; rg < 4; rg++) {
            const u32x2 bv = b2v[rg];
            o[4 * rg] = (o[4 * rg] + __uint_as_float(bv[0] << 16)) * fg; o[4 * rg + 1] = (o[4 * rg + 1] + __uint_as_float(bv[0] & 0xffff0000u)) * fg;
            o[4 * rg + 2] = (o[4 * rg + 2] + __uint_as_float(bv[1] << 16)) * fg; o[4 * rg + 3] = (o[4 * rg + 3] + __uint_as_float(bv[1] & 0xffff0000u)) * fg;
        }
    }
    STAMP(6);
    {
        const unsigned ob = (unsigned)WS_OF + Tm * 32 * 1024 + lo;
        const unsigned foo = WS_FO + (Tm * 7 + cs) * 128 + (lane & 15) * 8;
        bf16x8 of[32];
        unsigned ready = 0;
#pragma unroll
        for (int q = 0; q < 4; q++) {
#pragma unroll 1
            for (int spin = 0; spin < SPIN_CAP && ((ready >> (4 * q)) & 15u) != 15u; spin++) {
                if (spin) __builtin_amdgcn_s_sleep(POLL_SLEEP);
                u64 v = 0;
                if (lane < 16) v = __hip_atomic_load((const u64*)(p.ws + foo), __ATOMIC_RELAXED, __HIP_MEMORY_SCOPE_AGENT);
                ready = (unsigned)__builtin_amdgcn_ballot_w64(lane < 16 && v == tag);
            }
            asm volatile("" ::: "memory");
            if (q == 3) STAMP(7);
#pragma unroll
            for (int st = 8 * q; st < 8 * q + 8; st++) of[st] = ld_sc1(wsr, ob + st * 1024);
        }
        const unsigned lp16 = lds0 + L_WO + (wb >> 1) * 512 + ((lane & 15) >> 2) * 64 + ((lane >> 4) & 1) * 32 + (lane & 3) * 8;
        const unsigned wA = lp16 + h * 256, wB = lp16 + h * 768;
        f32x16 o2 = {};
#pragma unroll
        for (int st = 0; st < 32; st++) {
            const int hd = st >> 1, s = st & 1;
            const int r1 = 28 * hd + 16 * s, r2 = 28 * hd + (s ? 20 : 8);
            const unsigned a1 = ((r1 & 4) ? wB : wA) + (r1 >> 3) * 1024 + (r1 & 7) * 64, a2 = ((r2 & 4) ? wB : wA) + (r2 >> 3) * 1024 + (r2 & 7) * 64;
            const bf16x8 w0 = lds_tr2(a1, a2);
            if (st & 1) o2 = MFMA(w0, of[st], o2); else o = MFMA(w0, of[st], o);
        }
        o += o2;
    }
    STAMP(8);
    {
#define OUT_AUX 0
        const __amdgpu_buffer_rsrc_t ro = __builtin_amdgcn_make_buffer_rsrc((void*)p.out, 0, 2048 * D_ * 2, 0x00020000);
        const unsigned oo = ((Tm * 32 + l31) * D_ + nc) * 2;
#pragma unroll
        for (int rg = 0; rg < 4; rg++) {
            u32x2 ov = {cvtpk(o[4 * rg], o[4 * rg + 1]), cvtpk(o[4 * rg + 2], o[4 * rg + 3])};
            __builtin_amdgcn_raw_buffer_store_b64(ov, ro, oo + 16 * rg, 0, OUT_AUX);
        }
    }
    STAMP(9);
}

__global__ void __launch_bounds__(512, 2) gated_mix_fused(Params p) {
    extern __shared__ char smem[];
    const unsigned lds0 = (unsigned)(size_t)smem;
    const int b = blockIdx.x, xcd = b & 7, idx = b >> 3;
    const int g = xcd * 2 + (idx & 1), j = idx < 8 ? 12 + (idx >> 1) : (idx - 8) >> 1;
    const int w = __builtin_amdgcn_readfirstlane(threadIdx.x >> 6), lane = threadIdx.x & 63;
    const u64 epoch = __hip_atomic_load((const u64*)(p.ws + WS_EPOCH), __ATOMIC_RELAXED, __HIP_MEMORY_SCOPE_AGENT);
    if (w == 0 && lane < 16) *(LDS_AS unsigned*)(size_t)(lds0 + L_CTR + 4 * lane) = 0;
    wait_bar();
    if (w < 4) a_wave(p, lds0, g, j, w, lane, epoch);
    else b_wave(p, lds0, g, j, w - 4, lane, epoch);
}

extern "C" void kernel_launch(void* const* d_in, const int* in_sizes, int n_in, void* d_out, int out_size, void* d_ws, size_t ws_size, hipStream_t stream) {
    Params p;
    p.x = (const unsigned short*)d_in[0]; p.wq = (const unsigned short*)d_in[1]; p.wk = (const unsigned short*)d_in[2];
    p.wv = (const unsigned short*)d_in[3]; p.wo = (const unsigned short*)d_in[4]; p.w1 = (const unsigned short*)d_in[6];
    p.b1 = (const unsigned short*)d_in[7]; p.w2 = (const unsigned short*)d_in[8]; p.b2 = (const unsigned short*)d_in[9];
    p.gw = (const unsigned short*)d_in[10]; p.out = (unsigned short*)d_out; p.ws = (char*)d_ws;
    (void)hipFuncSetAttribute((const void*)gated_mix_fused, hipFuncAttributeMaxDynamicSharedMemorySize, LDS_BYTES);
    gated_mix_fused<<<256, 512, LDS_BYTES, stream>>>(p);
}
```

```cpp
#define FH1 4
#define BAR_EARLY 1
#define LFFN_GATE 1
#include <hip/hip_runtime.h>
#include <hip/hip_bf16.h>

typedef short bf16x8 __attribute__((ext_vector_type(8)));
typedef short s16x4 __attribute__((ext_vector_type(4)));
typedef __bf16 bf16x2 __attribute__((ext_vector_type(2)));
typedef float f32x16 __attribute__((ext_vector_type(16)));
typedef float f32x4 __attribute__((ext_vector_type(4)));
typedef float f32x2 __attribute__((ext_vector_type(2)));
typedef unsigned u32x4 __attribute__((ext_vector_type(4)));
typedef unsigned u32x2 __attribute__((ext_vector_type(2)));
typedef short v4i16_t __attribute__((ext_vector_type(4)));
typedef unsigned long long u64;
#define LDS_AS __attribute__((address_space(3)))
#define DI __device__ __forceinline__

constexpr int S_ = 2048, D_ = 448, F_ = 1792;
constexpr size_t WS_EPOCH = 0;
constexpr size_t WS_FKV = 65536, FKV_COPY = 1280;
constexpr size_t WS_FH = 163840;
constexpr size_t WS_FO = 294912;
constexpr size_t WS_KV = 1u << 20;
constexpr size_t WS_OF = 2u << 20;
constexpr size_t WS_HF = 4u << 20;

#define STAMP(i) do {} while (0)
#define MFMA(a, b, c) __builtin_amdgcn_mfma_f32_32x32x16_bf16(a, b, c, 0, 0, 0)
#define SB __builtin_amdgcn_sched_barrier(0)
#define EX2(x) __builtin_amdgcn_exp2f(x)
constexpr int SPIN_CAP = 1 << 21;
#define POLL_SLEEP 2
#define G_PRIO 3
#define F_PRIO 0
#define KV_PRIO 3
#define A1_PRIO 2
#define A2_PRIO 0
#define B_PRIO 2

DI unsigned cvtpk(float lo, float hi) { f32x2 v = {lo, hi}; return __builtin_bit_cast(unsigned, __builtin_convertvector(v, bf16x2)); }
DI void glds16(unsigned voff, const void* sbase, unsigned lds_dst) {
    unsigned keep;
    asm volatile("s_mov_b32 %0, m0\n\ts_mov_b32 m0, %3\n\ts_nop 0\n\tglobal_load_lds_dwordx4 %1, %2\n\ts_mov_b32 m0, %0"
                 : "=&s"(keep) : "v"(voff), "s"(sbase), "s"(lds_dst) : "memory");
}
DI void glds16_sc1(unsigned voff, const void* sbase, unsigned lds_dst) {
    unsigned keep;
    asm volatile("s_mov_b32 %0, m0\n\ts_mov_b32 m0, %3\n\ts_nop 0\n\tglobal_load_lds_dwordx4 %1, %2 sc1\n\ts_mov_b32 m0, %0"
                 : "=&s"(keep) : "v"(voff), "s"(sbase), "s"(lds_dst) : "memory");
}
DI bf16x8 lds_b128(unsigned a) { return *(const LDS_AS bf16x8*)(size_t)a; }
DI s16x4 lds_tr(unsigned a) { return __builtin_bit_cast(s16x4, __builtin_amdgcn_ds_read_tr16_b64_v4i16((LDS_AS v4i16_t*)(size_t)a)); }
DI bf16x8 lds_tr2(unsigned a0, unsigned a1) { s16x4 lo = lds_tr(a0), hi = lds_tr(a1); return __builtin_shufflevector(lo, hi, 0, 1, 2, 3, 4, 5, 6, 7); }
DI float dot2(unsigned a, unsigned b, float c) { return __builtin_amdgcn_fdot2_f32_bf16(__builtin_bit_cast(bf16x2, a), __builtin_bit_cast(bf16x2, b), c, false); }
DI u32x4 pack8(const f32x16& a, int s) {
    u32x4 r;
    r[0] = cvtpk(a[8 * s + 0], a[8 * s + 1]); r[1] = cvtpk(a[8 * s + 2], a[8 * s + 3]);
    r[2] = cvtpk(a[8 * s + 4], a[8 * s + 5]); r[3] = cvtpk(a[8 * s + 6], a[8 * s + 7]);
    return r;
}
DI float gelu_tanh(float x) {
    const float c1 = -2.0f * 1.4426950408889634f * 0.7978845608028654f, c2 = c1 * 0.044715f;
    float t = x * x;
    float e = EX2(x * __builtin_fmaf(t, c2, c1));
    return x * __builtin_amdgcn_rcpf(1.0f + e);
}
typedef float f32x2_ __attribute__((ext_vector_type(2)));
DI f32x2_ gelu_tanh2(f32x2_ x) {
    const float c1 = -2.0f * 1.4426950408889634f * 0.7978845608028654f, c2 = c1 * 0.044715f;
    const f32x2_ z = x * __builtin_elementwise_fma(x * x, (f32x2_){c2, c2}, (f32x2_){c1, c1});
    const f32x2_ d = (f32x2_){EX2(z[0]), EX2(z[1])} + 1.0f;
    return x * (f32x2_){__builtin_amdgcn_rcpf(d[0]), __builtin_amdgcn_rcpf(d[1])};
}
DI __amdgpu_buffer_rsrc_t ws_rsrc(const char* ws) { return __builtin_amdgcn_make_buffer_rsrc((void*)ws, 0, 16u << 20, 0x00020000); }
DI void st_sc1(__amdgpu_buffer_rsrc_t r, unsigned off, u32x4 v) { __builtin_amdgcn_raw_buffer_store_b128(v, r, off, 0, 16); }
DI void flag_set(char* ws, size_t off, u64 tag) { __hip_atomic_store((u64*)(ws + off), tag, __ATOMIC_RELAXED, __HIP_MEMORY_SCOPE_AGENT); }
DI void flags_wait(const char* ws, unsigned off, bool on, u64 tag) {
#pragma unroll 1
    for (int spin = 0; spin < SPIN_CAP; spin++) {
        u64 v = tag;
        if (on) v = __hip_atomic_load((const u64*)(ws + off), __ATOMIC_RELAXED, __HIP_MEMORY_SCOPE_AGENT);
        if (__builtin_amdgcn_ballot_w64(v != tag) == 0) break;
        __builtin_amdgcn_s_sleep(POLL_SLEEP);
    }
    asm volatile("s_waitcnt vmcnt(0)" ::: "memory");
}
DI u64 uniform64(u64 v) {
    asm volatile("" : "+v"(v) :: "memory");
    const unsigned lo = __builtin_amdgcn_readfirstlane((unsigned)v), hi = __builtin_amdgcn_readfirstlane((unsigned)(v >> 32));
    return ((u64)hi << 32) | lo;
}
DI unsigned lds_ld(unsigned a) { return __hip_atomic_load((LDS_AS unsigned*)(size_t)a, __ATOMIC_RELAXED, __HIP_MEMORY_SCOPE_WORKGROUP); }
DI void lds_st(unsigned a, unsigned v) { __hip_atomic_store((LDS_AS unsigned*)(size_t)a, v, __ATOMIC_RELAXED, __HIP_MEMORY_SCOPE_WORKGROUP); }
DI void lds_sync4(unsigned ctr, unsigned target, int lane) {
    asm volatile("s_waitcnt lgkmcnt(0)" ::: "memory");
    LDS_AS unsigned* c = (LDS_AS unsigned*)(size_t)ctr;
    if (lane == 0) __hip_atomic_fetch_add(c, 1u, __ATOMIC_RELAXED, __HIP_MEMORY_SCOPE_WORKGROUP);
#pragma unroll 1
    for (int spin = 0; spin < SPIN_CAP; spin++) {
        unsigned v = __builtin_amdgcn_readfirstlane(__hip_atomic_load(c, __ATOMIC_RELAXED, __HIP_MEMORY_SCOPE_WORKGROUP));
        if (v >= target) break;
        __builtin_amdgcn_s_sleep(1);
    }
    asm volatile("" ::: "memory");
}

struct Params {
    const unsigned short *x, *wq, *wk, *wv, *wo, *w1, *b1, *w2, *b2, *gw;
    unsigned short* out;
    char* ws;
};

constexpr unsigned X_SLOT = 16384, L_WQ = 114688, L_RING = 143360, L_QX = L_WQ;
constexpr unsigned L_WO = 0, WO_SLICE = 14336, L_MRG = 57344, L_RED = 106496, B_AREA = 12288;
constexpr unsigned P1_GW = 159744, L_G = P1_GW + 1024;
constexpr unsigned L_CTR = L_G + 512;
constexpr unsigned C_QW = 0, C_QR = 4, C_OW = 8, C_RED = 12, C_AP = 16, C_B1 = 20, C_KVS = 24, L_XF = L_CTR + 32, L_XE = L_CTR + 48;
constexpr unsigned LDS_BYTES = L_CTR + 64;
static_assert(L_RING + 4 * 4096 <= P1_GW && L_RED + 4 * B_AREA <= P1_GW && L_MRG + 12 * 4096 <= L_RED && LDS_BYTES <= 163840 && (L_XF & 15) == 0, "LDS map");

template <int N> DI void wait_vm() { asm volatile("s_waitcnt vmcnt(%0)" ::"n"(N) : "memory"); }
DI void wait_bar() { asm volatile("s_waitcnt lgkmcnt(0)\n\ts_barrier" ::: "memory"); }
DI u32x4 lds_flags(unsigned a) { u32x4 v; asm volatile("ds_read_b128 %0, %1" : "=v"(v) : "v"(a) : "memory"); return v; }
DI void lds_wait_ge(unsigned a, unsigned need) {
#pragma unroll 1
    for (int spin = 0; spin < SPIN_CAP; spin++) {
        if ((unsigned)__builtin_amdgcn_readfirstlane(lds_ld(a)) >= need) break;
        __builtin_amdgcn_s_sleep(1);
    }
    asm volatile("" ::: "memory");
}
DI void x_wait(unsigned xf, unsigned need, int only) {
#pragma unroll 1
    for (int spin = 0; spin < SPIN_CAP; spin++) {
        unsigned m;
        if (only < 0) {
            const unsigned a = lds_ld(xf), b = lds_ld(xf + 4), c = lds_ld(xf + 8), d = lds_ld(xf + 12);
            m = min(min(a, b), min(c, d));
        } else m = lds_ld(xf + 4 * only);
        if ((unsigned)__builtin_amdgcn_readfirstlane(m) >= need) break;
        __builtin_amdgcn_s_sleep(1);
    }
    asm volatile("" ::: "memory");
}
DI unsigned x_addr(unsigned lds0, int lane, int s) {
    const int h = lane >> 5, l31 = lane & 31;
    return lds0 + l31 * 128 + (((2 * s + h) ^ ((l31 >> 1) & 7)) << 4);
}
DI unsigned w_lane(int lane) { return (lane >> 5) * 512 + ((lane & 15) >> 2) * 64 + ((lane >> 4) & 1) * 32 + (lane & 3) * 8; }
#define NR1 8
template <typename Ld>
DI void proj_loop(f32x16 (&acc)[4], unsigned lds0, unsigned ring, int lane, bool light, Ld&& ld, unsigned long long* ptb) {
    constexpr int NR = NR1;
    u32x4 R[NR];
#define W1_DELAY 3
    if (light) { if (LFFN_GATE) lds_wait_ge(lds0 + L_CTR + C_KVS, 2); else x_wait(lds0 + L_XE, 1, -1); }
    else if (W1_DELAY > 0) x_wait(lds0 + L_XF, W1_DELAY, -1);
#pragma unroll
    for (int s = 0; s < NR; s++) R[s] = ld(s);
    unsigned xa[4];
#pragma unroll
    for (int s = 0; s < 4; s++) xa[s] = x_addr(lds0, lane, s);
    const unsigned wr = ring + lane * 16, rd = ring + w_lane(lane), xf = lds0 + L_XF;
    bf16x8 xf_[3][4], wf[3];
#define PWRITE(s_) do { *(LDS_AS u32x4*)(size_t)(wr + ((s_) & 3) * 1024) = R[(s_) % NR]; if ((s_) + NR < 28) R[(s_) % NR] = ld((s_) + NR); } while (0)
#define PREADW(s_) do { wf[(s_) % 3] = lds_tr2(rd + ((s_) & 3) * 1024, rd + ((s_) & 3) * 1024 + 256); } while (0)
#define PREADX(s_, t_) do { xf_[(s_) % 3][t_] = lds_b128(xa[(s_) & 3] + ((s_) >> 2) * X_SLOT + (t_) * 4096); } while (0)
    PWRITE(0); PWRITE(1); PWRITE(2);
    x_wait(xf, 1, -1);
    PREADW(0); PREADX(0, 0); PREADX(0, 1); PREADX(0, 2); PREADX(0, 3);
    PREADW(1); PREADX(1, 0); PREADX(1, 1); PREADX(1, 2); PREADX(1, 3);
#pragma unroll
    for (int s = 0; s < 28; s++) {
        SB;
        if ((s & 3) == 2 && s + 2 < 28) x_wait(xf, (s + 2) / 4 + 1, -1);
        acc[0] = MFMA(wf[s % 3], xf_[s % 3][0], acc[0]); SB;
        if (s + 2 < 28) { PREADW(s + 2); PREADX(s + 2, 0); } SB;
        acc[1] = MFMA(wf[s % 3], xf_[s % 3][1], acc[1]); SB;
        if (s + 2 < 28) { PREADX(s + 2, 1); PREADX(s + 2, 2); } SB;
        acc[2] = MFMA(wf[s % 3], xf_[s % 3][2], acc[2]); SB;
        if (s + 2 < 28) PREADX(s + 2, 3);
        if (s + 3 < 28) PWRITE(s + 3);
        SB;
        acc[3] = MFMA(wf[s % 3], xf_[s % 3][3], acc[3]);
    }
    SB;
#undef PWRITE
#undef PREADW
#undef PREADX
}
DI void gelu_store(const Params& p, const f32x16& acc, const u32x2 (&bv)[4], int T, int fb, int lane) {
    f32x16 a;
#pragma unroll
    for (int q = 0; q < 4; q++) {
        a[4 * q + 0] = acc[4 * q + 0] + __uint_as_float(bv[q][0] << 16); a[4 * q + 1] = acc[4 * q + 1] + __uint_as_float(bv[q][0] & 0xffff0000u);
        a[4 * q + 2] = acc[4 * q + 2] + __uint_as_float(bv[q][1] << 16); a[4 * q + 3] = acc[4 * q + 3] + __uint_as_float(bv[q][1] & 0xffff0000u);
    }
#pragma unroll
    for (int r = 0; r < 16; r += 2) { const f32x2_ y = gelu_tanh2((f32x2_){a[r], a[r + 1]}); a[r] = y[0]; a[r + 1] = y[1]; }
    const unsigned dst = (unsigned)WS_HF + (T * 112 + 2 * fb) * 1024 + lane * 16;
    const __amdgpu_buffer_rsrc_t r_ = ws_rsrc(p.ws);
    st_sc1(r_, dst, pack8(a, 0)); st_sc1(r_, dst + 1024, pack8(a, 1));
}

DI float max16(const f32x16& s) {
    float a = __builtin_fmaxf(__builtin_fmaxf(s[0], s[1]), s[2]), b = __builtin_fmaxf(__builtin_fmaxf(s[3], s[4]), s[5]);
#pragma unroll
    for (int r = 6; r < 14; r += 4) { a = __builtin_fmaxf(__builtin_fmaxf(a, s[r]), s[r + 1]); b = __builtin_fmaxf(__builtin_fmaxf(b, s[r + 2]), s[r + 3]); }
    a = __builtin_fmaxf(__builtin_fmaxf(a, s[14]), s[15]);
    return __builtin_fmaxf(a, b);
}
DI void set_max(bf16x8& q1, float m, int h) {
    auto rr = __builtin_amdgcn_permlane32_swap(__float_as_uint(m), __float_as_uint(m), false, false);
    const float nm = -__builtin_fmaxf(__uint_as_float(rr[0]), __uint_as_float(rr[1]));
    const unsigned a = cvtpk(nm, 0.f) & 0xffffu;
    const float r1 = nm - __uint_as_float(a << 16);
    const unsigned b = cvtpk(r1, 0.f) & 0xffffu;
    const float r2 = r1 - __uint_as_float(b << 16);
    const unsigned c = cvtpk(r2, 0.f) & 0xffffu;
    u32x4 t = __builtin_bit_cast(u32x4, q1);
    if (h == 0) { t[2] = a | (b << 16); t[3] = c; }
    q1 = __builtin_bit_cast(bf16x8, t);
}
DI bf16x8 ld_sc1s(__amdgpu_buffer_rsrc_t r, unsigned off, unsigned soff) { return __builtin_bit_cast(bf16x8, __builtin_amdgcn_raw_buffer_load_b128(r, off, soff, 16)); }
DI bf16x8 ld_sc1(__amdgpu_buffer_rsrc_t r, unsigned off) { return __builtin_bit_cast(bf16x8, __builtin_amdgcn_raw_buffer_load_b128(r, off, 0, 16)); }

DI void a_wave(const Params& p, unsigned lds0, int g, int j, int w, int lane, u64 epoch_v) {
    const int h = lane >> 5, l31 = lane & 31;
    const int T = 4 * g + w;
    f32x16 accq = {};
    STAMP(0);
    __builtin_amdgcn_s_setprio(A1_PRIO);
    unsigned xoff[4];
#pragma unroll
    for (int i = 0; i < 4; i++) {
        const int row = 32 * w + 8 * i + (lane >> 3);
        const int c = (lane & 7) ^ ((row >> 1) & 7);
        xoff[i] = row * 896 + c * 16;
    }
    const char* xb = (const char*)p.x + (size_t)g * 128 * 896;
    const unsigned qoff = (16 * w + (lane >> 2)) * 896 + ((lane & 3) == 3 ? 40 : (lane & 3) * 16);
    const char* qb_ = (const char*)p.wq + j * 56;
    glds16((lane < 56 ? lane : 55) * 16, p.gw, lds0 + P1_GW);
    auto dma = [&](int c) {
#pragma unroll
        for (int i = 0; i < 4; i++) glds16(xoff[i], xb + c * 128, lds0 + c * X_SLOT + (32 * w + 8 * i) * 128);
        glds16(qoff, qb_ + c * (64 * 896), lds0 + L_WQ + c * 4096 + w * 1024);
    };
    const bool light = j >= 12;
    if (light) {
        const int row = 32 * (j - 12) + 8 * w + (lane >> 3);
        const unsigned eo = row * 896 + (((lane & 7) ^ ((row >> 1) & 7)) << 4);
#pragma unroll
        for (int c = 0; c < 7; c++) glds16(eo, xb + c * 128, lds0 + c * X_SLOT + (32 * (j - 12) + 8 * w) * 128);
    }
    dma(0);
    if (!light) { dma(1); dma(2); }
    if (light) {
        wait_vm<5>();
        if (lane == 0) lds_st(lds0 + L_XE + 4 * w, 1);
        dma(1); dma(2); dma(3); dma(4); dma(5); dma(6);
    }
    u64 tag;
    const __amdgpu_buffer_rsrc_t wsr = ws_rsrc(p.ws);
    const int t0 = (4 * g + 4 * j + 16 * w) & 63;
    bf16x8 K0[4], K1[4], V0[4], V1[4];
    const unsigned kvb = (unsigned)WS_KV + lane * 16;
#define KVLOAD(set, vo) do { const unsigned o_ = (vo); K0[set] = ld_sc1(wsr, o_); K1[set] = ld_sc1(wsr, o_ + 1024); V0[set] = ld_sc1(wsr, o_ + 2048); V1[set] = ld_sc1(wsr, o_ + 3072); } while (0)
    {
        unsigned xa[4];
#pragma unroll
        for (int s = 0; s < 4; s++) xa[s] = x_addr(lds0, lane, s) + w * 4096;
        const unsigned wa = lds0 + L_WQ + w_lane(lane);
        unsigned short gwv[7];
        auto publish = [&](int c) {
            if (lane < 16) *(LDS_AS unsigned short*)(size_t)(lds0 + L_WQ + c * 4096 + w * 1024 + lane * 64 + 48) = gwv[c];
            asm volatile("s_waitcnt lgkmcnt(0)" ::: "memory");
            if (lane == 0) lds_st(lds0 + L_XF + 4 * w, c + 1);
        };
        auto qchunk = [&](int c) {
            bf16x8 xf[4], wf[4];
#pragma unroll
            for (int s = 0; s < 4; s++) { xf[s] = lds_b128(xa[s] + c * X_SLOT); wf[s] = lds_tr2(wa + c * 4096 + s * 1024, wa + c * 4096 + s * 1024 + 256); }
#pragma unroll
            for (int s = 0; s < 4; s++) accq = MFMA(wf[s], xf[s], accq);
        };
#define TC(c)
        if (light) wait_vm<30>(); else wait_vm<10>();
        TC(0);
#pragma unroll
        for (int c = 0; c < 7; c++) gwv[c] = *(const LDS_AS unsigned short*)(size_t)(lds0 + P1_GW + (64 * c + 16 * w + (lane & 15)) * 2);
        publish(0);
        if (light) wait_vm<25>(); else { dma(3); wait_vm<10>(); }
        TC(1); publish(1);
        if (light) wait_vm<20>(); else { dma(4); wait_vm<10>(); }
        TC(2); publish(2);
        if (light) wait_vm<15>(); else { dma(5); wait_vm<10>(); }
        TC(3); publish(3);
        if (!light) dma(6);
        wait_vm<10>(); TC(4); publish(4);
        x_wait(lds0 + L_XF, 2, -1); qchunk(0); qchunk(1); SB;
        wait_vm<5>(); TC(5); publish(5);
        x_wait(lds0 + L_XF, 4, -1); qchunk(2); qchunk(3); SB;
        wait_vm<0>(); TC(6); publish(6);
        tag = uniform64(epoch_v) + 1;
        const unsigned fo = WS_FKV + (g * 4 + (j >> 2)) * FKV_COPY + ((t0 + (lane >> 1)) & 63) * 16 + (lane & 1) * 8;
        u64 fv = tag;
        if (lane < 32) fv = __hip_atomic_load((const u64*)(p.ws + fo), __ATOMIC_RELAXED, __HIP_MEMORY_SCOPE_AGENT);
        x_wait(lds0 + L_XF, 6, -1); TC(8); qchunk(4); qchunk(5); SB; TC(9);
        x_wait(lds0 + L_XF, 7, -1); TC(11); qchunk(6); SB; TC(12);
        asm volatile("" : "+v"(fv) :: "memory");
        if (__builtin_amdgcn_ballot_w64(fv != tag) != 0) flags_wait(p.ws, fo, lane < 32, tag);
        TC(10);
        KVLOAD(0, kvb + t0 * 4096); KVLOAD(1, kvb + (t0 + 1) * 4096); KVLOAD(2, kvb + (t0 + 2) * 4096);
        SB;
    }
    const float zz = __uint_as_float(__builtin_amdgcn_permlane32_swap(__float_as_uint(accq[12]), __float_as_uint(accq[12]), false, false)[0]);
    const float gtok = __builtin_amdgcn_rcpf(1.0f + EX2(-1.4426950408889634f * zz));
    if (h == 0) *(LDS_AS float*)(size_t)(lds0 + L_G + (32 * w + l31) * 4) = gtok;
    bf16x8 q0[4], q1[4];
    bf16x8 qa, qb;
    {
        const float qs = 1.4426950408889634f * 0.1889822365046136f;
        f32x16 q = accq * qs;
        u32x4 a0 = pack8(q, 0), a1 = pack8(q, 1);
        if (h == 0) { a1[2] = 0; a1[3] = 0; }
        qa = __builtin_bit_cast(bf16x8, a0); qb = __builtin_bit_cast(bf16x8, a1);
    }
    asm volatile("s_waitcnt lgkmcnt(0)" ::: "memory");
    if (lane == 0) __hip_atomic_fetch_add((LDS_AS unsigned*)(size_t)(lds0 + L_CTR + C_AP), 1u, __ATOMIC_RELAXED, __HIP_MEMORY_SCOPE_WORKGROUP);
    const f32x16 zero = {};
    V1[3] = (bf16x8){}; K0[3] = V1[3]; K1[3] = V1[3]; V0[3] = V1[3];
    f32x16 S = MFMA(K0[0], qa, zero); S = MFMA(K1[0], qb, S);
    set_max(qb, max16(S), h);
    const unsigned lo = lane * 16;
    *(LDS_AS bf16x8*)(size_t)(lds0 + L_QX + w * 2048 + lo) = qa;
    *(LDS_AS bf16x8*)(size_t)(lds0 + L_QX + w * 2048 + 1024 + lo) = qb;
    lds_sync4(lds0 + L_CTR + C_QW, 4, lane);
#pragma unroll
    for (int b = 0; b < 4; b++) { q0[b] = lds_b128(lds0 + L_QX + b * 2048 + lo); q1[b] = lds_b128(lds0 + L_QX + b * 2048 + 1024 + lo); }
    asm volatile("s_waitcnt lgkmcnt(0)" ::: "memory");
    if (lane == 0) __hip_atomic_fetch_add((LDS_AS unsigned*)(size_t)(lds0 + L_CTR + C_QR), 1u, __ATOMIC_RELAXED, __HIP_MEMORY_SCOPE_WORKGROUP);
    TC(13);
    __builtin_amdgcn_s_setprio(A2_PRIO);
    S = MFMA(K0[0], q0[0], zero); S = MFMA(K1[0], q1[0], S);
    f32x16 O[4];
#pragma unroll
    for (int b = 0; b < 4; b++) O[b] = zero;
    u32x4 P1 = {0, 0, 0, 0};
#define ASTEP(c, b) do { \
        constexpr int pc = (b) == 0 ? ((c) + 3) & 3 : (c), pb = ((b) + 3) & 3, nc = (b) == 3 ? ((c) + 1) & 3 : (c), nb = ((b) + 1) & 3; \
        O[pb] = MFMA(V1[pc], __builtin_bit_cast(bf16x8, P1), O[pb]); SB; \
        u32x4 p0, p1; \
        p0[0] = cvtpk(EX2(S[0]), EX2(S[1])); p0[1] = cvtpk(EX2(S[2]), EX2(S[3])); SB; \
        f32x16 Sn = MFMA(K0[nc], q0[nb], zero); SB; \
        if ((b) == 1) { KVLOAD(pc3(c), vnext); } \
        p0[2] = cvtpk(EX2(S[4]), EX2(S[5])); p0[3] = cvtpk(EX2(S[6]), EX2(S[7])); SB; \
        Sn = MFMA(K1[nc], q1[nb], Sn); SB; \
        p1[0] = cvtpk(EX2(S[8]), EX2(S[9])); p1[1] = cvtpk(EX2(S[10]), EX2(S[11])); SB; \
        O[b] = MFMA(V0[c], __builtin_bit_cast(bf16x8, p0), O[b]); SB; \
        p1[2] = cvtpk(EX2(S[12]), EX2(S[13])); p1[3] = cvtpk(EX2(S[14]), EX2(S[15])); SB; \
        P1 = p1; S = Sn; } while (0)
#define pc3(c) (((c) + 3) & 3)
#define ATILE(c) do { \
        const int tn = tt + (c) + 3;     \
        const unsigned vnext = lo + (tn < 16 ? (unsigned)WS_KV + ((t0 + tn) & 63) * 4096u : 0x7f000000u); \
        ASTEP(c, 0); ASTEP(c, 1); ASTEP(c, 2); ASTEP(c, 3); } while (0)
#pragma unroll 1
    for (int tt = 0; tt < 16; tt += 4) {
        ATILE(0); ATILE(1); ATILE(2); ATILE(3);
    }
    O[3] = MFMA(V1[3], __builtin_bit_cast(bf16x8, P1), O[3]);
    STAMP(5);
    lds_wait_ge(lds0 + L_CTR + C_B1, 4);
#pragma unroll
    for (int b = 0; b < 4; b++) {
        if (b != w) {
            const unsigned a = lds0 + L_MRG + (b * 3 + ((w - b - 1) & 3)) * 4096 + lo;
#pragma unroll
            for (int rg = 0; rg < 4; rg++) {
                f32x4 v = {O[b][4 * rg], O[b][4 * rg + 1], O[b][4 * rg + 2], O[b][4 * rg + 3]};
                *(LDS_AS f32x4*)(size_t)(a + rg * 1024) = v;
            }
        }
    }
    f32x16 o = zero;
#pragma unroll
    for (int b = 0; b < 4; b++) if (b == w) { o = O[b]; qa = q0[b]; qb = q1[b]; }
    lds_sync4(lds0 + L_CTR + C_OW, 4, lane);
#pragma unroll
    for (int u = 0; u < 3; u++) {
        const unsigned a = lds0 + L_MRG + (w * 3 + u) * 4096 + lo;
#pragma unroll
        for (int rg = 0; rg < 4; rg++) {
            f32x4 v = *(const LDS_AS f32x4*)(size_t)(a + rg * 1024);
            o[4 * rg] += v[0]; o[4 * rg + 1] += v[1]; o[4 * rg + 2] += v[2]; o[4 * rg + 3] += v[3];
        }
    }
    STAMP(6);
    auto rr = __builtin_amdgcn_permlane32_swap(__float_as_uint(o[12]), __float_as_uint(o[12]), false, false);
    float l = __uint_as_float(rr[0]);
    {
        bool ok = l < 1.152921504606846976e18f && l > 8.673617379884035e-19f;
#pragma unroll
        for (int r = 0; r < 16; r++) ok = ok && (__builtin_fabsf(o[r]) < 3.0e38f);
        if (__builtin_amdgcn_ballot_w64(!ok) != 0) {
            u32x4 tq = __builtin_bit_cast(u32x4, qb);
            if (h == 0) { tq[2] = 0; tq[3] = 0; }
            qb = __builtin_bit_cast(bf16x8, tq);
            float m = -INFINITY;
#pragma unroll 1
            for (int t = 0; t < 64; t++) {
                bf16x8 k0 = ld_sc1(wsr, kvb + t * 4096), k1 = ld_sc1(wsr, kvb + t * 4096 + 1024);
                f32x16 s = MFMA(k0, qa, zero); s = MFMA(k1, qb, s);
                m = __builtin_fmaxf(m, max16(s));
            }
            set_max(qb, m, h);
            o = zero;
#pragma unroll 1
            for (int t = 0; t < 64; t++) {
                bf16x8 k0 = ld_sc1(wsr, kvb + t * 4096), k1 = ld_sc1(wsr, kvb + t * 4096 + 1024);
                bf16x8 v0 = ld_sc1(wsr, kvb + t * 4096 + 2048), v1 = ld_sc1(wsr, kvb + t * 4096 + 3072);
                f32x16 s = MFMA(k0, qa, zero); s = MFMA(k1, qb, s);
#pragma unroll
                for (int r = 0; r < 16; r++) s[r] = EX2(s[r]);
                o = MFMA(v0, __builtin_bit_cast(bf16x8, pack8(s, 0)), o); o = MFMA(v1, __builtin_bit_cast(bf16x8, pack8(s, 1)), o);
            }
            auto r2 = __builtin_amdgcn_permlane32_swap(__float_as_uint(o[12]), __float_as_uint(o[12]), false, false);
            l = __uint_as_float(r2[0]);
        }
    }
    const float sc = gtok * __builtin_amdgcn_rcpf(l);
#pragma unroll
    for (int r = 0; r < 16; r++) o[r] *= sc;
    u32x4 o0 = pack8(o, 0), o1 = pack8(o, 1);
    if (h == 0) { o1[2] = 0; o1[3] = 0; }
    const unsigned dst = (unsigned)WS_OF + (T * 32 + 2 * j) * 1024 + lo;
    st_sc1(wsr, dst, o0); st_sc1(wsr, dst + 1024, o1);
    asm volatile("s_waitcnt vmcnt(0)" ::: "memory");
    if (lane < 7) flag_set(p.ws, WS_FO + (T * 7 + lane) * 128 + j * 8, tag);
    STAMP(7);
}

DI void b_wave(const Params& p, unsigned lds0, int g, int j, int wb, int lane, u64 epoch_v) {
    const int h = lane >> 5, l31 = lane & 31;
    const bool light = j >= 12;
    const int lp = j - 12;
    const int fb = light ? 48 + 2 * lp + wb : 4 * j + wb;
    const unsigned lo = lane * 16;
    f32x16 acc[4];
#pragma unroll
    for (int b = 0; b < 4; b++) acc[b] = (f32x16){};
    STAMP(0);
    __builtin_amdgcn_s_setprio(B_PRIO);
    const __amdgpu_buffer_rsrc_t wsr = ws_rsrc(p.ws);
    const unsigned ring = lds0 + L_RING + wb * 4096;
    u64 tag;
    if (light && wb >= 2) {
        __builtin_amdgcn_s_setprio(KV_PRIO);
        const int T = 4 * g + lp;
        const __amdgpu_buffer_rsrc_t rk = __builtin_amdgcn_make_buffer_rsrc((void*)(wb == 2 ? p.wk : p.wv), 0, 448 * 56, 0x00020000);
        const unsigned off = (lane >> 2) * 56 + ((lane & 3) == 3 ? 40 : (lane & 3) * 16);
        const unsigned dst = (unsigned)WS_KV + T * 4096 + lo;
        u32x4 R[28];
#pragma unroll
        for (int s = 0; s < 28; s++) R[s] = __builtin_amdgcn_raw_buffer_load_b128(rk, off, s * (16 * 56), 0);
        const unsigned wr = ring + lo, rd = ring + w_lane(lane);
        bf16x8 wf[28];
#pragma unroll
        for (int q = 0; q < 28; q += 4) {
#pragma unroll
            for (int u = 0; u < 4; u++) *(LDS_AS u32x4*)(size_t)(wr + u * 1024) = R[q + u];
#pragma unroll
            for (int u = 0; u < 4; u++) wf[q + u] = lds_tr2(rd + u * 1024, rd + u * 1024 + 256);
            SB;
        }
        STAMP(10);
        x_wait(lds0 + L_XE, 1, -1);
        STAMP(11);
        f32x16 a2 = {};
        {
            unsigned xa[4];
#pragma unroll
            for (int s = 0; s < 4; s++) xa[s] = x_addr(lds0, lane, s) + lp * 4096;
            bf16x8 xf[2][4];
#define KVX(q_) _Pragma("unroll") for (int u = 0; u < 4; u++) xf[(q_) & 1][u] = lds_b128(xa[u] + (q_) * X_SLOT)
            KVX(0); KVX(1);
            if (wb == 2) {
#pragma unroll
                for (int q = 0; q < 7; q++) {
                    SB;
#pragma unroll
                    for (int u = 0; u < 4; u++) { if (u & 1) a2 = MFMA(wf[4 * q + u], xf[q & 1][u], a2); else acc[0] = MFMA(wf[4 * q + u], xf[q & 1][u], acc[0]); }
                    SB;
                    if (q + 2 < 7) { KVX(q + 2); }
                }
            } else {
#pragma unroll
                for (int q = 0; q < 7; q++) {
                    SB;
#pragma unroll
                    for (int u = 0; u < 4; u++) { if (u & 1) a2 = MFMA(xf[q & 1][u], wf[4 * q + u], a2); else acc[0] = MFMA(xf[q & 1][u], wf[4 * q + u], acc[0]); }
                    SB;
                    if (q + 2 < 7) { KVX(q + 2); }
                }
            }
        }
        acc[0] += a2;
        STAMP(13);
        {
            u32x4 a0 = pack8(acc[0], 0), a1 = pack8(acc[0], 1);
            if (wb == 2) {
                if (h == 0) { a1[2] = 0x3f803f80u; a1[3] = 0x00003f80u; }
                st_sc1(wsr, dst, a0); st_sc1(wsr, dst + 1024, a1);
            } else {
                if (l31 == 24) { a0 = (u32x4){0x3f803f80u, 0x3f803f80u, 0x3f803f80u, 0x3f803f80u}; a1 = a0; }
                else if (l31 > 24 && l31 < 28) { a0 = (u32x4){0, 0, 0, 0}; a1 = a0; }
                st_sc1(wsr, dst + 2048, a0); st_sc1(wsr, dst + 3072, a1);
            }
        }
        STAMP(1);
        if (lane == 0) __hip_atomic_fetch_add((LDS_AS unsigned*)(size_t)(lds0 + L_CTR + C_B1), 1u, __ATOMIC_RELAXED, __HIP_MEMORY_SCOPE_WORKGROUP);
#define KVS_AT 1
        if (KVS_AT == 0 && lane == 0) __hip_atomic_fetch_add((LDS_AS unsigned*)(size_t)(lds0 + L_CTR + C_KVS), 1u, __ATOMIC_RELAXED, __HIP_MEMORY_SCOPE_WORKGROUP);
        tag = uniform64(epoch_v) + 1;
        asm volatile("s_waitcnt vmcnt(0)" ::: "memory");
        STAMP(14);
        flag_set(p.ws, WS_FKV + lane * FKV_COPY + T * 16 + (wb & 1) * 8, tag);
        if (KVS_AT == 1 && lane == 0) __hip_atomic_fetch_add((LDS_AS unsigned*)(size_t)(lds0 + L_CTR + C_KVS), 1u, __ATOMIC_RELAXED, __HIP_MEMORY_SCOPE_WORKGROUP);
        STAMP(15);
    } else {
        const __amdgpu_buffer_rsrc_t rw1 = __builtin_amdgcn_make_buffer_rsrc((void*)p.w1, 0, 448 * 3584, 0x00020000);
        const unsigned off = (lane >> 2) * 3584 + fb * 64 + (lane & 3) * 16;
        u32x2 bv[4];
#pragma unroll
        for (int q = 0; q < 4; q++) bv[q] = *(const u32x2*)(p.b1 + fb * 32 + 4 * h + 8 * q);
        auto ld = [&](int s) { return __builtin_amdgcn_raw_buffer_load_b128(rw1, off, s * (16 * 3584), 0); };
        unsigned long long ptb[8];
        proj_loop(acc, lds0, ring, lane, light, ld, ptb);
        STAMP(1);
        if (lane == 0) __hip_atomic_fetch_add((LDS_AS unsigned*)(size_t)(lds0 + L_CTR + C_B1), 1u, __ATOMIC_RELAXED, __HIP_MEMORY_SCOPE_WORKGROUP);
        tag = uniform64(epoch_v) + 1;
        __builtin_amdgcn_s_setprio(G_PRIO);
#pragma unroll
        for (int t = 0; t < 4; t++) gelu_store(p, acc[t], bv, 4 * g + t, fb, lane);
        __builtin_amdgcn_s_setprio(F_PRIO);
        STAMP(12);
        asm volatile("s_waitcnt vmcnt(0)" ::: "memory");
        if (lane < 14) flag_set(p.ws, WS_FH + (g * 14 + lane) * 512 + fb * 8, tag);
        STAMP(3);

    }
    const bool tile = j < 14;
    if (!tile) {
        if (g == 0 && j == 15 && wb == 0) {
#pragma unroll 1
            for (int stage = 0; stage < 3; stage++) {
                const char* base = p.ws + (stage == 0 ? WS_FKV + lane * 8 : stage == 1 ? WS_FH + lane * 8 : WS_FO + (lane >> 4) * (7 * 128) + (lane & 15) * 8);
                const int n = stage == 0 ? 2 : 16;
                const unsigned step = stage == 0 ? 512 : stage == 1 ? 14 * 512 : 4 * 7 * 128;
                const bool on = stage != 1 || lane < 56;
#pragma unroll 1
                for (int spin = 0; spin < SPIN_CAP; spin++) {
                    u64 v[16];
#pragma unroll
                    for (int i = 0; i < 16; i++) v[i] = __hip_atomic_load((const u64*)(base + (i < n ? i : 0) * step), __ATOMIC_RELAXED, __HIP_MEMORY_SCOPE_AGENT);
                    unsigned bad = 0;
#pragma unroll
                    for (int i = 0; i < 16; i++) bad |= (v[i] != tag) ? 1u : 0u;
                    if (__builtin_amdgcn_ballot_w64(on && bad) == 0) break;
                    __builtin_amdgcn_s_sleep(16);
                }
            }
            if (lane == 0) __hip_atomic_store((u64*)(p.ws + WS_EPOCH), tag, __ATOMIC_RELAXED, __HIP_MEMORY_SCOPE_AGENT);
        }
        return;
    }
    const unsigned stg = lds0 + L_WO + wb * WO_SLICE;
    const int th = j & 1, cs = j >> 1, n0 = 64 * cs, T0 = 4 * g + 2 * th;
    const unsigned woff = ((lane & 31) >> 2) * 896 + (lane >> 5) * 64 + (lane & 3) * 16;
    const __amdgpu_buffer_rsrc_t rw2 = __builtin_amdgcn_make_buffer_rsrc((void*)p.w2, 0, 1792 * 896, 0x00020000);
    const unsigned w2v = woff + n0 * 2, w2s = 28 * wb * 16 * 896;
#define NBW 7
#define NBH 7
#define WO_LATE 4
    constexpr int NB = NBW, NH = NBH;
    u32x4 Wr[NB][2];
#define ROT28(st_) (st_)
#define WLOAD(st_) do { const unsigned so_ = w2s + ROT28(st_) * (16 * 896); Wr[(st_) % NB][0] = __builtin_amdgcn_raw_buffer_load_b128(rw2, w2v, so_, 0); \
        Wr[(st_) % NB][1] = __builtin_amdgcn_raw_buffer_load_b128(rw2, w2v + 8 * 896, so_, 0); } while (0)
#pragma unroll
    for (int st = 0; st < NB; st++) WLOAD(st);
    const int Tm = T0 + (wb & 1);
    const int nc = n0 + 32 * (wb >> 1) + 4 * h;
    u32x2 b2v[4];
#pragma unroll
    for (int rg = 0; rg < 4; rg++) b2v[rg] = *(const u32x2*)(p.b2 + nc + 8 * rg);
    const unsigned fho = WS_FH + (g * 14 + j) * 512 + (14 * wb + lane) * 8;
    lds_wait_ge(lds0 + L_CTR + C_AP, 4);
    lds_wait_ge(lds0 + L_CTR + C_B1, 4);
    const char* wos = (const char*)p.wo + n0 * 2;
#pragma unroll
    for (int i = WO_LATE; i < 14; i++) glds16(woff, wos + (size_t)(14 * wb + i) * (8 * 896), lds0 + L_WO + (14 * wb + i) * 1024);
    flags_wait(p.ws, fho, lane < FH1, tag);
    STAMP(4);
#pragma unroll
    for (int i = 0; i < 4; i++) acc[i] = (f32x16){};
    {
        const unsigned wrd = stg + (4 * h + ((lane & 15) >> 2)) * 64 + ((lane >> 4) & 1) * 32 + (lane & 3) * 8;
        const unsigned hv = (unsigned)WS_HF + (T0 * 112 + 28 * wb) * 1024 + lo;
        bf16x8 H0[NH], H1[NH], wf0[2], wf1[2];
#define HLOAD(st_) do { const unsigned so_ = ROT28(st_) * 1024; H0[(st_) % NH] = ld_sc1s(wsr, hv, so_); H1[(st_) % NH] = ld_sc1s(wsr, hv + 112 * 1024, so_); } while (0)
#define WWRITE(st_) do { *(LDS_AS u32x4*)(size_t)(stg + ((st_) & 1) * 2048 + lo) = Wr[(st_) % NB][0]; *(LDS_AS u32x4*)(size_t)(stg + ((st_) & 1) * 2048 + 1024 + lo) = Wr[(st_) % NB][1]; } while (0)
#define WREAD(st_) do { wf0[(st_) & 1] = lds_tr2(wrd + ((st_) & 1) * 2048, wrd + ((st_) & 1) * 2048 + 1024); wf1[(st_) & 1] = lds_tr2(wrd + ((st_) & 1) * 2048 + 512, wrd + ((st_) & 1) * 2048 + 1536); } while (0)
#pragma unroll
        for (int st = 0; st < NH; st++) HLOAD(st);
        WWRITE(0); WLOAD(NB); WWRITE(1); WLOAD(NB + 1);
        WREAD(0);
#pragma unroll
        for (int st = 0; st < 28; st++) {
            SB;
            if (st + 1 < 28) WREAD(st + 1);
            if (st + 2 < 28) WWRITE(st + 2);
            if (st + 2 + NB < 28) WLOAD(st + 2 + NB);
            SB;
            acc[0] = MFMA(wf0[st & 1], H0[st % NH], acc[0]); acc[1] = MFMA(wf0[st & 1], H1[st % NH], acc[1]);
            acc[2] = MFMA(wf1[st & 1], H0[st % NH], acc[2]); acc[3] = MFMA(wf1[st & 1], H1[st % NH], acc[3]);
            SB;
            if (st + NH == 2 * FH1) {
                u64 fv = tag;
                if (lane >= FH1 && lane < 14) fv = __hip_atomic_load((const u64*)(p.ws + fho), __ATOMIC_RELAXED, __HIP_MEMORY_SCOPE_AGENT);
                if (__builtin_amdgcn_ballot_w64(fv != tag) != 0) flags_wait(p.ws, fho, lane >= FH1 && lane < 14, tag);
                asm volatile("" ::: "memory");
            }
            if (st + NH < 28) HLOAD(st + NH);
        }
    }
    STAMP(5);
    asm volatile("s_waitcnt lgkmcnt(0)" ::: "memory");
#pragma unroll
    for (int i = 0; i < WO_LATE; i++) glds16(woff, wos + (size_t)(14 * wb + i) * (8 * 896), lds0 + L_WO + (14 * wb + i) * 1024);
    lds_wait_ge(lds0 + L_CTR + C_QR, 4);
    {
        const unsigned rarea = lds0 + L_RED + wb * B_AREA;
#pragma unroll
        for (int i = 0; i < 4; i++) {
            if (i != wb) {
                const unsigned a = rarea + ((i - wb - 1) & 3) * 4096 + lo;
#pragma unroll
                for (int rg = 0; rg < 4; rg++) {
                    f32x4 v = {acc[i][4 * rg], acc[i][4 * rg + 1], acc[i][4 * rg + 2], acc[i][4 * rg + 3]};
                    *(LDS_AS f32x4*)(size_t)(a + rg * 1024) = v;
                }
            }
        }
    }
    f32x16 o = {};
#pragma unroll
    for (int i = 0; i < 4; i++) if (i == wb) o = acc[i];
    asm volatile("s_waitcnt vmcnt(0)" ::: "memory");
    lds_sync4(lds0 + L_CTR + C_RED, 4, lane);
#pragma unroll
    for (int u = 0; u < 4; u++) {
        if (u != wb) {
            const unsigned a = lds0 + L_RED + u * B_AREA + ((wb - u - 1) & 3) * 4096 + lo;
#pragma unroll
            for (int rg = 0; rg < 4; rg++) {
                f32x4 v = *(const LDS_AS f32x4*)(size_t)(a + rg * 1024);
                o[4 * rg] += v[0]; o[4 * rg + 1] += v[1]; o[4 * rg + 2] += v[2]; o[4 * rg + 3] += v[3];
            }
        }
    }
    {
        const float fg = 1.0f - *(const LDS_AS float*)(size_t)(lds0 + L_G + (64 * th + 32 * (wb & 1) + l31) * 4);
#pragma unroll
        for (int rg = 0; rg < 4; rg++) {
            const u32x2 bv = b2v[rg];
            o[4 * rg] = (o[4 * rg] + __uint_as_float(bv[0] << 16)) * fg; o[4 * rg + 1] = (o[4 * rg + 1] + __uint_as_float(bv[0] & 0xffff0000u)) * fg;
            o[4 * rg + 2] = (o[4 * rg + 2] + __uint_as_float(bv[1] << 16)) * fg; o[4 * rg + 3] = (o[4 * rg + 3] + __uint_as_float(bv[1] & 0xffff0000u)) * fg;
        }
    }
    STAMP(6);
    {
        const unsigned ob = (unsigned)WS_OF + Tm * 32 * 1024 + lo;
        const unsigned foo = WS_FO + (Tm * 7 + cs) * 128 + (lane & 15) * 8;
        bf16x8 of[32];
        unsigned ready = 0;
#pragma unroll
        for (int q = 0; q < 4; q++) {
#pragma unroll 1
            for (int spin = 0; spin < SPIN_CAP && ((ready >> (4 * q)) & 15u) != 15u; spin++) {
                if (spin) __builtin_amdgcn_s_sleep(POLL_SLEEP);
                u64 v = 0;
                if (lane < 16) v = __hip_atomic_load((const u64*)(p.ws + foo), __ATOMIC_RELAXED, __HIP_MEMORY_SCOPE_AGENT);
                ready = (unsigned)__builtin_amdgcn_ballot_w64(lane < 16 && v == tag);
            }
            asm volatile("" ::: "memory");
            if (q == 3) STAMP(7);
#pragma unroll
            for (int st = 8 * q; st < 8 * q + 8; st++) of[st] = ld_sc1(wsr, ob + st * 1024);
        }
        const unsigned lp16 = lds0 + L_WO + (wb >> 1) * 512 + ((lane & 15) >> 2) * 64 + ((lane >> 4) & 1) * 32 + (lane & 3) * 8;
        const unsigned wA = lp16 + h * 256, wB = lp16 + h * 768;
        f32x16 o2 = {};
#pragma unroll
        for (int st = 0; st < 32; st++) {
            const int hd = st >> 1, s = st & 1;
            const int r1 = 28 * hd + 16 * s, r2 = 28 * hd + (s ? 20 : 8);
            const unsigned a1 = ((r1 & 4) ? wB : wA) + (r1 >> 3) * 1024 + (r1 & 7) * 64, a2 = ((r2 & 4) ? wB : wA) + (r2 >> 3) * 1024 + (r2 & 7) * 64;
            const bf16x8 w0 = lds_tr2(a1, a2);
            if (st & 1) o2 = MFMA(w0, of[st], o2); else o = MFMA(w0, of[st], o);
        }
        o += o2;
    }
    STAMP(8);
    {
#define OUT_AUX 0
        const __amdgpu_buffer_rsrc_t ro = __builtin_amdgcn_make_buffer_rsrc((void*)p.out, 0, 2048 * D_ * 2, 0x00020000);
        const unsigned oo = ((Tm * 32 + l31) * D_ + nc) * 2;
#pragma unroll
        for (int rg = 0; rg < 4; rg++) {
            u32x2 ov = {cvtpk(o[4 * rg], o[4 * rg + 1]), cvtpk(o[4 * rg + 2], o[4 * rg + 3])};
            __builtin_amdgcn_raw_buffer_store_b64(ov, ro, oo + 16 * rg, 0, OUT_AUX);
        }
    }
    STAMP(9);
}

__global__ void __launch_bounds__(512, 2) gated_mix_fused(Params p) {
    extern __shared__ char smem[];
    const unsigned lds0 = (unsigned)(size_t)smem;
    const int b = blockIdx.x, xcd = b & 7, idx = b >> 3;
    const int g = xcd * 2 + (idx & 1), j = idx < 8 ? 12 + (idx >> 1) : (idx - 8) >> 1;
    const int w = __builtin_amdgcn_readfirstlane(threadIdx.x >> 6), lane = threadIdx.x & 63;
    const u64 epoch = __hip_atomic_load((const u64*)(p.ws + WS_EPOCH), __ATOMIC_RELAXED, __HIP_MEMORY_SCOPE_AGENT);
    if (w == 0 && lane < 16) *(LDS_AS unsigned*)(size_t)(lds0 + L_CTR + 4 * lane) = 0;
    wait_bar();
    if (w < 4) a_wave(p, lds0, g, j, w, lane, epoch);
    else b_wave(p, lds0, g, j, w - 4, lane, epoch);
}

extern "C" void kernel_launch(void* const* d_in, const int* in_sizes, int n_in, void* d_out, int out_size, void* d_ws, size_t ws_size, hipStream_t stream) {
    Params p;
    p.x = (const unsigned short*)d_in[0]; p.wq = (const unsigned short*)d_in[1]; p.wk = (const unsigned short*)d_in[2];
    p.wv = (const unsigned short*)d_in[3]; p.wo = (const unsigned short*)d_in[4]; p.w1 = (const unsigned short*)d_in[6];
    p.b1 = (const unsigned short*)d_in[7]; p.w2 = (const unsigned short*)d_in[8]; p.b2 = (const unsigned short*)d_in[9];
    p.gw = (const unsigned short*)d_in[10]; p.out = (unsigned short*)d_out; p.ws = (char*)d_ws;
    (void)hipFuncSetAttribute((const void*)gated_mix_fused, hipFuncAttributeMaxDynamicSharedMemorySize, LDS_BYTES);
    gated_mix_fused<<<256, 512, LDS_BYTES, stream>>>(p);
}
```

```cpp
#include <hip/hip_runtime.h>
#include <hip/hip_bf16.h>

typedef short bf16x8 __attribute__((ext_vector_type(8)));
typedef short s16x4 __attribute__((ext_vector_type(4)));
typedef __bf16 bf16x2 __attribute__((ext_vector_type(2)));
typedef float f32x16 __attribute__((ext_vector_type(16)));
typedef float f32x4 __attribute__((ext_vector_type(4)));
typedef float f32x2 __attribute__((ext_vector_type(2)));
typedef unsigned u32x4 __attribute__((ext_vector_type(4)));
typedef unsigned u32x2 __attribute__((ext_vector_type(2)));
typedef short v4i16_t __attribute__((ext_vector_type(4)));
typedef unsigned long long u64;
#define LDS_AS __attribute__((address_space(3)))
#define DI __device__ __forceinline__

constexpr int S_ = 2048, D_ = 448, F_ = 1792;
constexpr size_t WS_EPOCH = 0;
constexpr size_t WS_FKV = 65536, FKV_COPY = 1280;
constexpr size_t WS_FH = 163840;
constexpr size_t WS_FO = 294912;
constexpr size_t WS_KV = 1u << 20;
constexpr size_t WS_OF = 2u << 20;
constexpr size_t WS_HF = 4u << 20;

#define MFMA(a, b, c) __builtin_amdgcn_mfma_f32_32x32x16_bf16(a, b, c, 0, 0, 0)
#define SB __builtin_amdgcn_sched_barrier(0)
#define EX2(x) __builtin_amdgcn_exp2f(x)
constexpr int SPIN_CAP = 1 << 21;
#define POLL_SLEEP 2
#define G_PRIO 3
#define F_PRIO 0
#define KV_PRIO 3
#define A1_PRIO 2
#define A2_PRIO 0
#define B_PRIO 2

DI unsigned cvtpk(float lo, float hi) { f32x2 v = {lo, hi}; return __builtin_bit_cast(unsigned, __builtin_convertvector(v, bf16x2)); }
DI void glds16(unsigned voff, const void* sbase, unsigned lds_dst) {
    unsigned keep;
    asm volatile("s_mov_b32 %0, m0\n\ts_mov_b32 m0, %3\n\ts_nop 0\n\tglobal_load_lds_dwordx4 %1, %2\n\ts_mov_b32 m0, %0"
                 : "=&s"(keep) : "v"(voff), "s"(sbase), "s"(lds_dst) : "memory");
}
DI void glds16_sc1(unsigned voff, const void* sbase, unsigned lds_dst) {
    unsigned keep;
    asm volatile("s_mov_b32 %0, m0\n\ts_mov_b32 m0, %3\n\ts_nop 0\n\tglobal_load_lds_dwordx4 %1, %2 sc1\n\ts_mov_b32 m0, %0"
                 : "=&s"(keep) : "v"(voff), "s"(sbase), "s"(lds_dst) : "memory");
}
DI bf16x8 lds_b128(unsigned a) { return *(const LDS_AS bf16x8*)(size_t)a; }
DI s16x4 lds_tr(unsigned a) { return __builtin_bit_cast(s16x4, __builtin_amdgcn_ds_read_tr16_b64_v4i16((LDS_AS v4i16_t*)(size_t)a)); }
DI bf16x8 lds_tr2(unsigned a0, unsigned a1) { s16x4 lo = lds_tr(a0), hi = lds_tr(a1); return __builtin_shufflevector(lo, hi, 0, 1, 2, 3, 4, 5, 6, 7); }
DI float dot2(unsigned a, unsigned b, float c) { return __builtin_amdgcn_fdot2_f32_bf16(__builtin_bit_cast(bf16x2, a), __builtin_bit_cast(bf16x2, b), c, false); }
DI u32x4 pack8(const f32x16& a, int s) {
    u32x4 r;
    r[0] = cvtpk(a[8 * s + 0], a[8 * s + 1]); r[1] = cvtpk(a[8 * s + 2], a[8 * s + 3]);
    r[2] = cvtpk(a[8 * s + 4], a[8 * s + 5]); r[3] = cvtpk(a[8 * s + 6], a[8 * s + 7]);
    return r;
}
DI float gelu_tanh(float x) {
    const float c1 = -2.0f * 1.4426950408889634f * 0.7978845608028654f, c2 = c1 * 0.044715f;
    float t = x * x;
    float e = EX2(x * __builtin_fmaf(t, c2, c1));
    return x * __builtin_amdgcn_rcpf(1.0f + e);
}
typedef float f32x2_ __attribute__((ext_vector_type(2)));
DI f32x2_ gelu_tanh2(f32x2_ x) {
    const float c1 = -2.0f * 1.4426950408889634f * 0.7978845608028654f, c2 = c1 * 0.044715f;
    const f32x2_ z = x * __builtin_elementwise_fma(x * x, (f32x2_){c2, c2}, (f32x2_){c1, c1});
    const f32x2_ d = (f32x2_){EX2(z[0]), EX2(z[1])} + 1.0f;
    return x * (f32x2_){__builtin_amdgcn_rcpf(d[0]), __builtin_amdgcn_rcpf(d[1])};
}
DI __amdgpu_buffer_rsrc_t ws_rsrc(const char* ws) { return __builtin_amdgcn_make_buffer_rsrc((void*)ws, 0, 16u << 20, 0x00020000); }
DI void st_sc1(__amdgpu_buffer_rsrc_t r, unsigned off, u32x4 v) { __builtin_amdgcn_raw_buffer_store_b128(v, r, off, 0, 16); }
DI void flag_set(char* ws, size_t off, u64 tag) { __hip_atomic_store((u64*)(ws + off), tag, __ATOMIC_RELAXED, __HIP_MEMORY_SCOPE_AGENT); }
DI void flags_wait(const char* ws, unsigned off, bool on, u64 tag) {
#pragma unroll 1
    for (int spin = 0; spin < SPIN_CAP; spin++) {
        u64 v = tag;
        if (on) v = __hip_atomic_load((const u64*)(ws + off), __ATOMIC_RELAXED, __HIP_MEMORY_SCOPE_AGENT);
        if (__builtin_amdgcn_ballot_w64(v != tag) == 0) break;
        __builtin_amdgcn_s_sleep(POLL_SLEEP);
    }
    asm volatile("s_waitcnt vmcnt(0)" ::: "memory");
}
DI u64 uniform64(u64 v) {
    asm volatile("" : "+v"(v) :: "memory");
    const unsigned lo = __builtin_amdgcn_readfirstlane((unsigned)v), hi = __builtin_amdgcn_readfirstlane((unsigned)(v >> 32));
    return ((u64)hi << 32) | lo;
}
DI unsigned lds_ld(unsigned a) { return __hip_atomic_load((LDS_AS unsigned*)(size_t)a, __ATOMIC_RELAXED, __HIP_MEMORY_SCOPE_WORKGROUP); }
DI void lds_st(unsigned a, unsigned v) { __hip_atomic_store((LDS_AS unsigned*)(size_t)a, v, __ATOMIC_RELAXED, __HIP_MEMORY_SCOPE_WORKGROUP); }
DI void lds_sync4(unsigned ctr, unsigned target, int lane) {
    asm volatile("s_waitcnt lgkmcnt(0)" ::: "memory");
    LDS_AS unsigned* c = (LDS_AS unsigned*)(size_t)ctr;
    if (lane == 0) __hip_atomic_fetch_add(c, 1u, __ATOMIC_RELAXED, __HIP_MEMORY_SCOPE_WORKGROUP);
#pragma unroll 1
    for (int spin = 0; spin < SPIN_CAP; spin++) {
        unsigned v = __builtin_amdgcn_readfirstlane(__hip_atomic_load(c, __ATOMIC_RELAXED, __HIP_MEMORY_SCOPE_WORKGROUP));
        if (v >= target) break;
        __builtin_amdgcn_s_sleep(1);
    }
    asm volatile("" ::: "memory");
}

struct Params {
    const unsigned short *x, *wq, *wk, *wv, *wo, *w1, *b1, *w2, *b2, *gw;
    unsigned short* out;
    char* ws;
};

constexpr unsigned X_SLOT = 16384, L_WQ = 114688, L_RING = 143360, L_QX = L_WQ;
constexpr unsigned L_WO = 0, WO_SLICE = 14336, L_MRG = 57344, L_RED = 106496, B_AREA = 12288;
constexpr unsigned P1_GW = 159744, L_G = P1_GW + 1024;
constexpr unsigned L_CTR = L_G + 512;
constexpr unsigned C_QW = 0, C_QR = 4, C_OW = 8, C_RED = 12, C_AP = 16, C_B1 = 20, C_KVS = 24, L_XF = L_CTR + 32, L_XE = L_CTR + 48;
constexpr unsigned LDS_BYTES = L_CTR + 64;
static_assert(L_RING + 4 * 4096 <= P1_GW && L_RED + 4 * B_AREA <= P1_GW && L_MRG + 12 * 4096 <= L_RED && LDS_BYTES <= 163840 && (L_XF & 15) == 0, "LDS map");

template <int N> DI void wait_vm() { asm volatile("s_waitcnt vmcnt(%0)" ::"n"(N) : "memory"); }
DI void wait_bar() { asm volatile("s_waitcnt lgkmcnt(0)\n\ts_barrier" ::: "memory"); }
DI u32x4 lds_flags(unsigned a) { u32x4 v; asm volatile("ds_read_b128 %0, %1" : "=v"(v) : "v"(a) : "memory"); return v; }
DI void lds_wait_ge(unsigned a, unsigned need) {
#pragma unroll 1
    for (int spin = 0; spin < SPIN_CAP; spin++) {
        if ((unsigned)__builtin_amdgcn_readfirstlane(lds_ld(a)) >= need) break;
        __builtin_amdgcn_s_sleep(1);
    }
    asm volatile("" ::: "memory");
}
DI void x_wait(unsigned xf, unsigned need, int only) {
#pragma unroll 1
    for (int spin = 0; spin < SPIN_CAP; spin++) {
        unsigned m;
        if (only < 0) {
            const unsigned a = lds_ld(xf), b = lds_ld(xf + 4), c = lds_ld(xf + 8), d = lds_ld(xf + 12);
            m = min(min(a, b), min(c, d));
        } else m = lds_ld(xf + 4 * only);
        if ((unsigned)__builtin_amdgcn_readfirstlane(m) >= need) break;
        __builtin_amdgcn_s_sleep(1);
    }
    asm volatile("" ::: "memory");
}
DI unsigned x_addr(unsigned lds0, int lane, int s) {
    const int h = lane >> 5, l31 = lane & 31;
    return lds0 + l31 * 128 + (((2 * s + h) ^ ((l31 >> 1) & 7)) << 4);
}
DI unsigned w_lane(int lane) { return (lane >> 5) * 512 + ((lane & 15) >> 2) * 64 + ((lane >> 4) & 1) * 32 + (lane & 3) * 8; }
#define NR1 8
template <typename Ld>
DI void proj_loop(f32x16 (&acc)[4], unsigned lds0, unsigned ring, int lane, bool light, Ld&& ld) {
    constexpr int NR = NR1;
    u32x4 R[NR];
#define W1_DELAY 3
#define LFFN_GATE 1
    if (light) { if (LFFN_GATE) lds_wait_ge(lds0 + L_CTR + C_KVS, 2); else x_wait(lds0 + L_XE, 1, -1); }
    else if (W1_DELAY > 0) x_wait(lds0 + L_XF, W1_DELAY, -1);
#pragma unroll
    for (int s = 0; s < NR; s++) R[s] = ld(s);
    unsigned xa[4];
#pragma unroll
    for (int s = 0; s < 4; s++) xa[s] = x_addr(lds0, lane, s);
    const unsigned wr = ring + lane * 16, rd = ring + w_lane(lane), xf = lds0 + L_XF;
    bf16x8 xf_[3][4], wf[3];
#define PWRITE(s_) do { *(LDS_AS u32x4*)(size_t)(wr + ((s_) & 3) * 1024) = R[(s_) % NR]; if ((s_) + NR < 28) R[(s_) % NR] = ld((s_) + NR); } while (0)
#define PREADW(s_) do { wf[(s_) % 3] = lds_tr2(rd + ((s_) & 3) * 1024, rd + ((s_) & 3) * 1024 + 256); } while (0)
#define PREADX(s_, t_) do { xf_[(s_) % 3][t_] = lds_b128(xa[(s_) & 3] + ((s_) >> 2) * X_SLOT + (t_) * 4096); } while (0)
    PWRITE(0); PWRITE(1); PWRITE(2);
    x_wait(xf, 1, -1);
    PREADW(0); PREADX(0, 0); PREADX(0, 1); PREADX(0, 2); PREADX(0, 3);
    PREADW(1); PREADX(1, 0); PREADX(1, 1); PREADX(1, 2); PREADX(1, 3);
#pragma unroll
    for (int s = 0; s < 28; s++) {
        SB;
        if ((s & 3) == 2 && s + 2 < 28) x_wait(xf, (s + 2) / 4 + 1, -1);
        acc[0] = MFMA(wf[s % 3], xf_[s % 3][0], acc[0]); SB;
        if (s + 2 < 28) { PREADW(s + 2); PREADX(s + 2, 0); } SB;
        acc[1] = MFMA(wf[s % 3], xf_[s % 3][1], acc[1]); SB;
        if (s + 2 < 28) { PREADX(s + 2, 1); PREADX(s + 2, 2); } SB;
        acc[2] = MFMA(wf[s % 3], xf_[s % 3][2], acc[2]); SB;
        if (s + 2 < 28) PREADX(s + 2, 3);
        if (s + 3 < 28) PWRITE(s + 3);
        SB;
        acc[3] = MFMA(wf[s % 3], xf_[s % 3][3], acc[3]);
    }
    SB;
#undef PWRITE
#undef PREADW
#undef PREADX
}
DI void gelu_store(const Params& p, const f32x16& acc, const u32x2 (&bv)[4], int T, int fb, int lane) {
    f32x16 a;
#pragma unroll
    for (int q = 0; q < 4; q++) {
        a[4 * q + 0] = acc[4 * q + 0] + __uint_as_float(bv[q][0] << 16); a[4 * q + 1] = acc[4 * q + 1] + __uint_as_float(bv[q][0] & 0xffff0000u);
        a[4 * q + 2] = acc[4 * q + 2] + __uint_as_float(bv[q][1] << 16); a[4 * q + 3] = acc[4 * q + 3] + __uint_as_float(bv[q][1] & 0xffff0000u);
    }
#pragma unroll
    for (int r = 0; r < 16; r += 2) { const f32x2_ y = gelu_tanh2((f32x2_){a[r], a[r + 1]}); a[r] = y[0]; a[r + 1] = y[1]; }
    const unsigned dst = (unsigned)WS_HF + (T * 112 + 2 * fb) * 1024 + lane * 16;
    const __amdgpu_buffer_rsrc_t r_ = ws_rsrc(p.ws);
    st_sc1(r_, dst, pack8(a, 0)); st_sc1(r_, dst + 1024, pack8(a, 1));
}

DI float max16(const f32x16& s) {
    float a = __builtin_fmaxf(__builtin_fmaxf(s[0], s[1]), s[2]), b = __builtin_fmaxf(__builtin_fmaxf(s[3], s[4]), s[5]);
#pragma unroll
    for (int r = 6; r < 14; r += 4) { a = __builtin_fmaxf(__builtin_fmaxf(a, s[r]), s[r + 1]); b = __builtin_fmaxf(__builtin_fmaxf(b, s[r + 2]), s[r + 3]); }
    a = __builtin_fmaxf(__builtin_fmaxf(a, s[14]), s[15]);
    return __builtin_fmaxf(a, b);
}
DI void set_max(bf16x8& q1, float m, int h) {
    auto rr = __builtin_amdgcn_permlane32_swap(__float_as_uint(m), __float_as_uint(m), false, false);
    const float nm = -__builtin_fmaxf(__uint_as_float(rr[0]), __uint_as_float(rr[1]));
    const unsigned a = cvtpk(nm, 0.f) & 0xffffu;
    const float r1 = nm - __uint_as_float(a << 16);
    const unsigned b = cvtpk(r1, 0.f) & 0xffffu;
    const float r2 = r1 - __uint_as_float(b << 16);
    const unsigned c = cvtpk(r2, 0.f) & 0xffffu;
    u32x4 t = __builtin_bit_cast(u32x4, q1);
    if (h == 0) { t[2] = a | (b << 16); t[3] = c; }
    q1 = __builtin_bit_cast(bf16x8, t);
}
DI bf16x8 ld_sc1s(__amdgpu_buffer_rsrc_t r, unsigned off, unsigned soff) { return __builtin_bit_cast(bf16x8, __builtin_amdgcn_raw_buffer_load_b128(r, off, soff, 16)); }
DI bf16x8 ld_sc1(__amdgpu_buffer_rsrc_t r, unsigned off) { return __builtin_bit_cast(bf16x8, __builtin_amdgcn_raw_buffer_load_b128(r, off, 0, 16)); }

DI void a_wave(const Params& p, unsigned lds0, int g, int j, int w, int lane, u64 epoch_v) {
    const int h = lane >> 5, l31 = lane & 31;
    const int T = 4 * g + w;
    f32x16 accq = {};
    __builtin_amdgcn_s_setprio(A1_PRIO);
    unsigned xoff[4];
#pragma unroll
    for (int i = 0; i < 4; i++) {
        const int row = 32 * w + 8 * i + (lane >> 3);
        const int c = (lane & 7) ^ ((row >> 1) & 7);
        xoff[i] = row * 896 + c * 16;
    }
    const char* xb = (const char*)p.x + (size_t)g * 128 * 896;
    const unsigned qoff = (16 * w + (lane >> 2)) * 896 + ((lane & 3) == 3 ? 40 : (lane & 3) * 16);
    const char* qb_ = (const char*)p.wq + j * 56;
    glds16((lane < 56 ? lane : 55) * 16, p.gw, lds0 + P1_GW);
    auto dma = [&](int c) {
#pragma unroll
        for (int i = 0; i < 4; i++) glds16(xoff[i], xb + c * 128, lds0 + c * X_SLOT + (32 * w + 8 * i) * 128);
        glds16(qoff, qb_ + c * (64 * 896), lds0 + L_WQ + c * 4096 + w * 1024);
    };
    const bool light = j >= 12;
    if (light) {
        const int row = 32 * (j - 12) + 8 * w + (lane >> 3);
        const unsigned eo = row * 896 + (((lane & 7) ^ ((row >> 1) & 7)) << 4);
#pragma unroll
        for (int c = 0; c < 7; c++) glds16(eo, xb + c * 128, lds0 + c * X_SLOT + (32 * (j - 12) + 8 * w) * 128);
    }
    dma(0);
    if (!light) { dma(1); dma(2); }
    if (light) {
        wait_vm<5>();
        if (lane == 0) lds_st(lds0 + L_XE + 4 * w, 1);
        dma(1); dma(2); dma(3); dma(4); dma(5); dma(6);
    }
    u64 tag;
    const __amdgpu_buffer_rsrc_t wsr = ws_rsrc(p.ws);
    const int t0 = (4 * g + 4 * j + 16 * w) & 63;
    bf16x8 K0[4], K1[4], V0[4], V1[4];
    const unsigned kvb = (unsigned)WS_KV + lane * 16;
#define KVLOAD(set, vo) do { const unsigned o_ = (vo); K0[set] = ld_sc1(wsr, o_); K1[set] = ld_sc1(wsr, o_ + 1024); V0[set] = ld_sc1(wsr, o_ + 2048); V1[set] = ld_sc1(wsr, o_ + 3072); } while (0)
    {
        unsigned xa[4];
#pragma unroll
        for (int s = 0; s < 4; s++) xa[s] = x_addr(lds0, lane, s) + w * 4096;
        const unsigned wa = lds0 + L_WQ + w_lane(lane);
        unsigned short gwv[7];
        auto publish = [&](int c) {
            if (lane < 16) *(LDS_AS unsigned short*)(size_t)(lds0 + L_WQ + c * 4096 + w * 1024 + lane * 64 + 48) = gwv[c];
            asm volatile("s_waitcnt lgkmcnt(0)" ::: "memory");
            if (lane == 0) lds_st(lds0 + L_XF + 4 * w, c + 1);
        };
        auto qchunk = [&](int c) {
            bf16x8 xf[4], wf[4];
#pragma unroll
            for (int s = 0; s < 4; s++) { xf[s] = lds_b128(xa[s] + c * X_SLOT); wf[s] = lds_tr2(wa + c * 4096 + s * 1024, wa + c * 4096 + s * 1024 + 256); }
#pragma unroll
            for (int s = 0; s < 4; s++) accq = MFMA(wf[s], xf[s], accq);
        };
        if (light) wait_vm<30>(); else wait_vm<10>();
#pragma unroll
        for (int c = 0; c < 7; c++) gwv[c] = *(const LDS_AS unsigned short*)(size_t)(lds0 + P1_GW + (64 * c + 16 * w + (lane & 15)) * 2);
        publish(0);
        if (light) wait_vm<25>(); else { dma(3); wait_vm<10>(); }
        publish(1);
        if (light) wait_vm<20>(); else { dma(4); wait_vm<10>(); }
        publish(2);
        if (light) wait_vm<15>(); else { dma(5); wait_vm<10>(); }
        publish(3);
        if (!light) dma(6);
        wait_vm<10>(); publish(4);
        x_wait(lds0 + L_XF, 2, -1); qchunk(0); qchunk(1); SB;
        wait_vm<5>(); publish(5);
        x_wait(lds0 + L_XF, 4, -1); qchunk(2); qchunk(3); SB;
        wait_vm<0>(); publish(6);
        tag = uniform64(epoch_v) + 1;
        const unsigned fo = WS_FKV + (g * 4 + (j >> 2)) * FKV_COPY + ((t0 + (lane >> 1)) & 63) * 16 + (lane & 1) * 8;
        u64 fv = tag;
        if (lane < 32) fv = __hip_atomic_load((const u64*)(p.ws + fo), __ATOMIC_RELAXED, __HIP_MEMORY_SCOPE_AGENT);
        x_wait(lds0 + L_XF, 6, -1); qchunk(4); qchunk(5); SB;
        x_wait(lds0 + L_XF, 7, -1); qchunk(6); SB;
        asm volatile("" : "+v"(fv) :: "memory");
        if (__builtin_amdgcn_ballot_w64(fv != tag) != 0) flags_wait(p.ws, fo, lane < 32, tag);
        KVLOAD(0, kvb + t0 * 4096); KVLOAD(1, kvb + (t0 + 1) * 4096); KVLOAD(2, kvb + (t0 + 2) * 4096);
        SB;
    }
    const float zz = __uint_as_float(__builtin_amdgcn_permlane32_swap(__float_as_uint(accq[12]), __float_as_uint(accq[12]), false, false)[0]);
    const float gtok = __builtin_amdgcn_rcpf(1.0f + EX2(-1.4426950408889634f * zz));
    if (h == 0) *(LDS_AS float*)(size_t)(lds0 + L_G + (32 * w + l31) * 4) = gtok;
    bf16x8 q0[4], q1[4];
    bf16x8 qa, qb;
    {
        const float qs = 1.4426950408889634f * 0.1889822365046136f;
        f32x16 q = accq * qs;
        u32x4 a0 = pack8(q, 0), a1 = pack8(q, 1);
        if (h == 0) { a1[2] = 0; a1[3] = 0; }
        qa = __builtin_bit_cast(bf16x8, a0); qb = __builtin_bit_cast(bf16x8, a1);
    }
    asm volatile("s_waitcnt lgkmcnt(0)" ::: "memory");
    if (lane == 0) __hip_atomic_fetch_add((LDS_AS unsigned*)(size_t)(lds0 + L_CTR + C_AP), 1u, __ATOMIC_RELAXED, __HIP_MEMORY_SCOPE_WORKGROUP);
    const f32x16 zero = {};
    V1[3] = (bf16x8){}; K0[3] = V1[3]; K1[3] = V1[3]; V0[3] = V1[3];
    f32x16 S = MFMA(K0[0], qa, zero); S = MFMA(K1[0], qb, S);
    set_max(qb, max16(S), h);
    const unsigned lo = lane * 16;
    *(LDS_AS bf16x8*)(size_t)(lds0 + L_QX + w * 2048 + lo) = qa;
    *(LDS_AS bf16x8*)(size_t)(lds0 + L_QX + w * 2048 + 1024 + lo) = qb;
    lds_sync4(lds0 + L_CTR + C_QW, 4, lane);
#pragma unroll
    for (int b = 0; b < 4; b++) { q0[b] = lds_b128(lds0 + L_QX + b * 2048 + lo); q1[b] = lds_b128(lds0 + L_QX + b * 2048 + 1024 + lo); }
    asm volatile("s_waitcnt lgkmcnt(0)" ::: "memory");
    if (lane == 0) __hip_atomic_fetch_add((LDS_AS unsigned*)(size_t)(lds0 + L_CTR + C_QR), 1u, __ATOMIC_RELAXED, __HIP_MEMORY_SCOPE_WORKGROUP);
    __builtin_amdgcn_s_setprio(A2_PRIO);
    S = MFMA(K0[0], q0[0], zero); S = MFMA(K1[0], q1[0], S);
    f32x16 O[4];
#pragma unroll
    for (int b = 0; b < 4; b++) O[b] = zero;
    u32x4 P1 = {0, 0, 0, 0};
#define ASTEP(c, b) do { \
        constexpr int pc = (b) == 0 ? ((c) + 3) & 3 : (c), pb = ((b) + 3) & 3, nc = (b) == 3 ? ((c) + 1) & 3 : (c), nb = ((b) + 1) & 3; \
        O[pb] = MFMA(V1[pc], __builtin_bit_cast(bf16x8, P1), O[pb]); SB; \
        u32x4 p0, p1; \
        p0[0] = cvtpk(EX2(S[0]), EX2(S[1])); p0[1] = cvtpk(EX2(S[2]), EX2(S[3])); SB; \
        f32x16 Sn = MFMA(K0[nc], q0[nb], zero); SB; \
        if ((b) == 1) { KVLOAD(pc3(c), vnext); } \
        p0[2] = cvtpk(EX2(S[4]), EX2(S[5])); p0[3] = cvtpk(EX2(S[6]), EX2(S[7])); SB; \
        Sn = MFMA(K1[nc], q1[nb], Sn); SB; \
        p1[0] = cvtpk(EX2(S[8]), EX2(S[9])); p1[1] = cvtpk(EX2(S[10]), EX2(S[11])); SB; \
        O[b] = MFMA(V0[c], __builtin_bit_cast(bf16x8, p0), O[b]); SB; \
        p1[2] = cvtpk(EX2(S[12]), EX2(S[13])); p1[3] = cvtpk(EX2(S[14]), EX2(S[15])); SB; \
        P1 = p1; S = Sn; } while (0)
#define pc3(c) (((c) + 3) & 3)
#define ATILE(c) do { \
        const int tn = tt + (c) + 3;     \
        const unsigned vnext = lo + (tn < 16 ? (unsigned)WS_KV + ((t0 + tn) & 63) * 4096u : 0x7f000000u); \
        ASTEP(c, 0); ASTEP(c, 1); ASTEP(c, 2); ASTEP(c, 3); } while (0)
#pragma unroll 1
    for (int tt = 0; tt < 16; tt += 4) {
        ATILE(0); ATILE(1); ATILE(2); ATILE(3);
    }
    O[3] = MFMA(V1[3], __builtin_bit_cast(bf16x8, P1), O[3]);
    lds_wait_ge(lds0 + L_CTR + C_B1, 4);
#pragma unroll
    for (int b = 0; b < 4; b++) {
        if (b != w) {
            const unsigned a = lds0 + L_MRG + (b * 3 + ((w - b - 1) & 3)) * 4096 + lo;
#pragma unroll
            for (int rg = 0; rg < 4; rg++) {
                f32x4 v = {O[b][4 * rg], O[b][4 * rg + 1], O[b][4 * rg + 2], O[b][4 * rg + 3]};
                *(LDS_AS f32x4*)(size_t)(a + rg * 1024) = v;
            }
        }
    }
    f32x16 o = zero;
#pragma unroll
    for (int b = 0; b < 4; b++) if (b == w) { o = O[b]; qa = q0[b]; qb = q1[b]; }
    lds_sync4(lds0 + L_CTR + C_OW, 4, lane);
#pragma unroll
    for (int u = 0; u < 3; u++) {
        const unsigned a = lds0 + L_MRG + (w * 3 + u) * 4096 + lo;
#pragma unroll
        for (int rg = 0; rg < 4; rg++) {
            f32x4 v = *(const LDS_AS f32x4*)(size_t)(a + rg * 1024);
            o[4 * rg] += v[0]; o[4 * rg + 1] += v[1]; o[4 * rg + 2] += v[2]; o[4 * rg + 3] += v[3];
        }
    }
    auto rr = __builtin_amdgcn_permlane32_swap(__float_as_uint(o[12]), __float_as_uint(o[12]), false, false);
    float l = __uint_as_float(rr[0]);
    {
        bool ok = l < 1.152921504606846976e18f && l > 8.673617379884035e-19f;
#pragma unroll
        for (int r = 0; r < 16; r++) ok = ok && (__builtin_fabsf(o[r]) < 3.0e38f);
        if (__builtin_amdgcn_ballot_w64(!ok) != 0) {
            u32x4 tq = __builtin_bit_cast(u32x4, qb);
            if (h == 0) { tq[2] = 0; tq[3] = 0; }
            qb = __builtin_bit_cast(bf16x8, tq);
            float m = -INFINITY;
#pragma unroll 1
            for (int t = 0; t < 64; t++) {
                bf16x8 k0 = ld_sc1(wsr, kvb + t * 4096), k1 = ld_sc1(wsr, kvb + t * 4096 + 1024);
                f32x16 s = MFMA(k0, qa, zero); s = MFMA(k1, qb, s);
                m = __builtin_fmaxf(m, max16(s));
            }
            set_max(qb, m, h);
            o = zero;
#pragma unroll 1
            for (int t = 0; t < 64; t++) {
                bf16x8 k0 = ld_sc1(wsr, kvb + t * 4096), k1 = ld_sc1(wsr, kvb + t * 4096 + 1024);
                bf16x8 v0 = ld_sc1(wsr, kvb + t * 4096 + 2048), v1 = ld_sc1(wsr, kvb + t * 4096 + 3072);
                f32x16 s = MFMA(k0, qa, zero); s = MFMA(k1, qb, s);
#pragma unroll
                for (int r = 0; r < 16; r++) s[r] = EX2(s[r]);
                o = MFMA(v0, __builtin_bit_cast(bf16x8, pack8(s, 0)), o); o = MFMA(v1, __builtin_bit_cast(bf16x8, pack8(s, 1)), o);
            }
            auto r2 = __builtin_amdgcn_permlane32_swap(__float_as_uint(o[12]), __float_as_uint(o[12]), false, false);
            l = __uint_as_float(r2[0]);
        }
    }
    const float sc = gtok * __builtin_amdgcn_rcpf(l);
#pragma unroll
    for (int r = 0; r < 16; r++) o[r] *= sc;
    u32x4 o0 = pack8(o, 0), o1 = pack8(o, 1);
    if (h == 0) { o1[2] = 0; o1[3] = 0; }
    const unsigned dst = (unsigned)WS_OF + (T * 32 + 2 * j) * 1024 + lo;
    st_sc1(wsr, dst, o0); st_sc1(wsr, dst + 1024, o1);
    asm volatile("s_waitcnt vmcnt(0)" ::: "memory");
    if (lane < 7) flag_set(p.ws, WS_FO + (T * 7 + lane) * 128 + j * 8, tag);
}

DI void b_wave(const Params& p, unsigned lds0, int g, int j, int wb, int lane, u64 epoch_v) {
    const int h = lane >> 5, l31 = lane & 31;
    const bool light = j >= 12;
    const int lp = j - 12;
    const int fb = light ? 48 + 2 * lp + wb : 4 * j + wb;
    const unsigned lo = lane * 16;
    f32x16 acc[4];
#pragma unroll
    for (int b = 0; b < 4; b++) acc[b] = (f32x16){};
    __builtin_amdgcn_s_setprio(B_PRIO);
    const __amdgpu_buffer_rsrc_t wsr = ws_rsrc(p.ws);
    const unsigned ring = lds0 + L_RING + wb * 4096;
    u64 tag;
    if (light && wb >= 2) {
        __builtin_amdgcn_s_setprio(KV_PRIO);
        const int T = 4 * g + lp;
        const __amdgpu_buffer_rsrc_t rk = __builtin_amdgcn_make_buffer_rsrc((void*)(wb == 2 ? p.wk : p.wv), 0, 448 * 56, 0x00020000);
        const unsigned off = (lane >> 2) * 56 + ((lane & 3) == 3 ? 40 : (lane & 3) * 16);
        const unsigned dst = (unsigned)WS_KV + T * 4096 + lo;
        u32x4 R[28];
#pragma unroll
        for (int s = 0; s < 28; s++) R[s] = __builtin_amdgcn_raw_buffer_load_b128(rk, off, s * (16 * 56), 0);
        const unsigned wr = ring + lo, rd = ring + w_lane(lane);
        bf16x8 wf[28];
#pragma unroll
        for (int q = 0; q < 28; q += 4) {
#pragma unroll
            for (int u = 0; u < 4; u++) *(LDS_AS u32x4*)(size_t)(wr + u * 1024) = R[q + u];
#pragma unroll
            for (int u = 0; u < 4; u++) wf[q + u] = lds_tr2(rd + u * 1024, rd + u * 1024 + 256);
            SB;
        }
        x_wait(lds0 + L_XE, 1, -1);
        f32x16 a2 = {};
        {
            unsigned xa[4];
#pragma unroll
            for (int s = 0; s < 4; s++) xa[s] = x_addr(lds0, lane, s) + lp * 4096;
            bf16x8 xf[2][4];
#define KVX(q_) _Pragma("unroll") for (int u = 0; u < 4; u++) xf[(q_) & 1][u] = lds_b128(xa[u] + (q_) * X_SLOT)
            KVX(0); KVX(1);
            if (wb == 2) {
#pragma unroll
                for (int q = 0; q < 7; q++) {
                    SB;
#pragma unroll
                    for (int u = 0; u < 4; u++) { if (u & 1) a2 = MFMA(wf[4 * q + u], xf[q & 1][u], a2); else acc[0] = MFMA(wf[4 * q + u], xf[q & 1][u], acc[0]); }
                    SB;
                    if (q + 2 < 7) { KVX(q + 2); }
                }
            } else {
#pragma unroll
                for (int q = 0; q < 7; q++) {
                    SB;
#pragma unroll
                    for (int u = 0; u < 4; u++) { if (u & 1) a2 = MFMA(xf[q & 1][u], wf[4 * q + u], a2); else acc[0] = MFMA(xf[q & 1][u], wf[4 * q + u], acc[0]); }
                    SB;
                    if (q + 2 < 7) { KVX(q + 2); }
                }
            }
        }
        acc[0] += a2;
        {
            u32x4 a0 = pack8(acc[0], 0), a1 = pack8(acc[0], 1);
            if (wb == 2) {
                if (h == 0) { a1[2] = 0x3f803f80u; a1[3] = 0x00003f80u; }
                st_sc1(wsr, dst, a0); st_sc1(wsr, dst + 1024, a1);
            } else {
                if (l31 == 24) { a0 = (u32x4){0x3f803f80u, 0x3f803f80u, 0x3f803f80u, 0x3f803f80u}; a1 = a0; }
                else if (l31 > 24 && l31 < 28) { a0 = (u32x4){0, 0, 0, 0}; a1 = a0; }
                st_sc1(wsr, dst + 2048, a0); st_sc1(wsr, dst + 3072, a1);
            }
        }
        if (lane == 0) __hip_atomic_fetch_add((LDS_AS unsigned*)(size_t)(lds0 + L_CTR + C_B1), 1u, __ATOMIC_RELAXED, __HIP_MEMORY_SCOPE_WORKGROUP);
#define KVS_AT 1
        if (KVS_AT == 0 && lane == 0) __hip_atomic_fetch_add((LDS_AS unsigned*)(size_t)(lds0 + L_CTR + C_KVS), 1u, __ATOMIC_RELAXED, __HIP_MEMORY_SCOPE_WORKGROUP);
        tag = uniform64(epoch_v) + 1;
        asm volatile("s_waitcnt vmcnt(0)" ::: "memory");
        flag_set(p.ws, WS_FKV + lane * FKV_COPY + T * 16 + (wb & 1) * 8, tag);
        if (KVS_AT == 1 && lane == 0) __hip_atomic_fetch_add((LDS_AS unsigned*)(size_t)(lds0 + L_CTR + C_KVS), 1u, __ATOMIC_RELAXED, __HIP_MEMORY_SCOPE_WORKGROUP);
    } else {
        const __amdgpu_buffer_rsrc_t rw1 = __builtin_amdgcn_make_buffer_rsrc((void*)p.w1, 0, 448 * 3584, 0x00020000);
        const unsigned off = (lane >> 2) * 3584 + fb * 64 + (lane & 3) * 16;
        u32x2 bv[4];
#pragma unroll
        for (int q = 0; q < 4; q++) bv[q] = *(const u32x2*)(p.b1 + fb * 32 + 4 * h + 8 * q);
        auto ld = [&](int s) { return __builtin_amdgcn_raw_buffer_load_b128(rw1, off, s * (16 * 3584), 0); };
        proj_loop(acc, lds0, ring, lane, light, ld);
        if (lane == 0) __hip_atomic_fetch_add((LDS_AS unsigned*)(size_t)(lds0 + L_CTR + C_B1), 1u, __ATOMIC_RELAXED, __HIP_MEMORY_SCOPE_WORKGROUP);
        tag = uniform64(epoch_v) + 1;
        __builtin_amdgcn_s_setprio(G_PRIO);
#pragma unroll
        for (int t = 0; t < 4; t++) gelu_store(p, acc[t], bv, 4 * g + t, fb, lane);
        __builtin_amdgcn_s_setprio(F_PRIO);
        asm volatile("s_waitcnt vmcnt(0)" ::: "memory");
        if (lane < 14) flag_set(p.ws, WS_FH + (g * 14 + lane) * 512 + fb * 8, tag);
    }
    const bool tile = j < 14;
    if (!tile) {
        if (g == 0 && j == 15 && wb == 0) {
#pragma unroll 1
            for (int stage = 0; stage < 3; stage++) {
                const char* base = p.ws + (stage == 0 ? WS_FKV + lane * 8 : stage == 1 ? WS_FH + lane * 8 : WS_FO + (lane >> 4) * (7 * 128) + (lane & 15) * 8);
                const int n = stage == 0 ? 2 : 16;
                const unsigned step = stage == 0 ? 512 : stage == 1 ? 14 * 512 : 4 * 7 * 128;
                const bool on = stage != 1 || lane < 56;
#pragma unroll 1
                for (int spin = 0; spin < SPIN_CAP; spin++) {
                    u64 v[16];
#pragma unroll
                    for (int i = 0; i < 16; i++) v[i] = __hip_atomic_load((const u64*)(base + (i < n ? i : 0) * step), __ATOMIC_RELAXED, __HIP_MEMORY_SCOPE_AGENT);
                    unsigned bad = 0;
#pragma unroll
                    for (int i = 0; i < 16; i++) bad |= (v[i] != tag) ? 1u : 0u;
                    if (__builtin_amdgcn_ballot_w64(on && bad) == 0) break;
                    __builtin_amdgcn_s_sleep(16);
                }
            }
            if (lane == 0) __hip_atomic_store((u64*)(p.ws + WS_EPOCH), tag, __ATOMIC_RELAXED, __HIP_MEMORY_SCOPE_AGENT);
        }
        return;
    }
    const unsigned stg = lds0 + L_WO + wb * WO_SLICE;
    const int th = j & 1, cs = j >> 1, n0 = 64 * cs, T0 = 4 * g + 2 * th;
    const unsigned woff = ((lane & 31) >> 2) * 896 + (lane >> 5) * 64 + (lane & 3) * 16;
    const __amdgpu_buffer_rsrc_t rw2 = __builtin_amdgcn_make_buffer_rsrc((void*)p.w2, 0, 1792 * 896, 0x00020000);
    const unsigned w2v = woff + n0 * 2, w2s = 28 * wb * 16 * 896;
#define NBW 7
#define NBH 7
#define WO_LATE 4
#define FH1 4
    constexpr int NB = NBW, NH = NBH;
    u32x4 Wr[NB][2];
#define ROT28(st_) (st_)
#define WLOAD(st_) do { const unsigned so_ = w2s + ROT28(st_) * (16 * 896); Wr[(st_) % NB][0] = __builtin_amdgcn_raw_buffer_load_b128(rw2, w2v, so_, 0); \
        Wr[(st_) % NB][1] = __builtin_amdgcn_raw_buffer_load_b128(rw2, w2v + 8 * 896, so_, 0); } while (0)
#pragma unroll
    for (int st = 0; st < NB; st++) WLOAD(st);
    const int Tm = T0 + (wb & 1);
    const int nc = n0 + 32 * (wb >> 1) + 4 * h;
    u32x2 b2v[4];
#pragma unroll
    for (int rg = 0; rg < 4; rg++) b2v[rg] = *(const u32x2*)(p.b2 + nc + 8 * rg);
    const unsigned fho = WS_FH + (g * 14 + j) * 512 + (14 * wb + lane) * 8;
    lds_wait_ge(lds0 + L_CTR + C_AP, 4);
    lds_wait_ge(lds0 + L_CTR + C_B1, 4);
    const char* wos = (const char*)p.wo + n0 * 2;
#pragma unroll
    for (int i = WO_LATE; i < 14; i++) glds16(woff, wos + (size_t)(14 * wb + i) * (8 * 896), lds0 + L_WO + (14 * wb + i) * 1024);
    flags_wait(p.ws, fho, lane < FH1, tag);
#pragma unroll
    for (int i = 0; i < 4; i++) acc[i] = (f32x16){};
    {
        const unsigned wrd = stg + (4 * h + ((lane & 15) >> 2)) * 64 + ((lane >> 4) & 1) * 32 + (lane & 3) * 8;
        const unsigned hv = (unsigned)WS_HF + (T0 * 112 + 28 * wb) * 1024 + lo;
        bf16x8 H0[NH], H1[NH], wf0[2], wf1[2];
#define HLOAD(st_) do { const unsigned so_ = ROT28(st_) * 1024; H0[(st_) % NH] = ld_sc1s(wsr, hv, so_); H1[(st_) % NH] = ld_sc1s(wsr, hv + 112 * 1024, so_); } while (0)
#define WWRITE(st_) do { *(LDS_AS u32x4*)(size_t)(stg + ((st_) & 1) * 2048 + lo) = Wr[(st_) % NB][0]; *(LDS_AS u32x4*)(size_t)(stg + ((st_) & 1) * 2048 + 1024 + lo) = Wr[(st_) % NB][1]; } while (0)
#define WREAD(st_) do { wf0[(st_) & 1] = lds_tr2(wrd + ((st_) & 1) * 2048, wrd + ((st_) & 1) * 2048 + 1024); wf1[(st_) & 1] = lds_tr2(wrd + ((st_) & 1) * 2048 + 512, wrd + ((st_) & 1) * 2048 + 1536); } while (0)
#pragma unroll
        for (int st = 0; st < NH; st++) HLOAD(st);
        WWRITE(0); WLOAD(NB); WWRITE(1); WLOAD(NB + 1);
        WREAD(0);
#pragma unroll
        for (int st = 0; st < 28; st++) {
            SB;
            if (st + 1 < 28) WREAD(st + 1);
            if (st + 2 < 28) WWRITE(st + 2);
            if (st + 2 + NB < 28) WLOAD(st + 2 + NB);
            SB;
            acc[0] = MFMA(wf0[st & 1], H0[st % NH], acc[0]); acc[1] = MFMA(wf0[st & 1], H1[st % NH], acc[1]);
            acc[2] = MFMA(wf1[st & 1], H0[st % NH], acc[2]); acc[3] = MFMA(wf1[st & 1], H1[st % NH], acc[3]);
            SB;
            if (st + NH == 2 * FH1) {
                u64 fv = tag;
                if (lane >= FH1 && lane < 14) fv = __hip_atomic_load((const u64*)(p.ws + fho), __ATOMIC_RELAXED, __HIP_MEMORY_SCOPE_AGENT);
                if (__builtin_amdgcn_ballot_w64(fv != tag) != 0) flags_wait(p.ws, fho, lane >= FH1 && lane < 14, tag);
                asm volatile("" ::: "memory");
            }
            if (st + NH < 28) HLOAD(st + NH);
        }
    }
    asm volatile("s_waitcnt lgkmcnt(0)" ::: "memory");
#pragma unroll
    for (int i = 0; i < WO_LATE; i++) glds16(woff, wos + (size_t)(14 * wb + i) * (8 * 896), lds0 + L_WO + (14 * wb + i) * 1024);
    lds_wait_ge(lds0 + L_CTR + C_QR, 4);
    {
        const unsigned rarea = lds0 + L_RED + wb * B_AREA;
#pragma unroll
        for (int i = 0; i < 4; i++) {
            if (i != wb) {
                const unsigned a = rarea + ((i - wb - 1) & 3) * 4096 + lo;
#pragma unroll
                for (int rg = 0; rg < 4; rg++) {
                    f32x4 v = {acc[i][4 * rg], acc[i][4 * rg + 1], acc[i][4 * rg + 2], acc[i][4 * rg + 3]};
                    *(LDS_AS f32x4*)(size_t)(a + rg * 1024) = v;
                }
            }
        }
    }
    f32x16 o = {};
#pragma unroll
    for (int i = 0; i < 4; i++) if (i == wb) o = acc[i];
    asm volatile("s_waitcnt vmcnt(0)" ::: "memory");
    lds_sync4(lds0 + L_CTR + C_RED, 4, lane);
#pragma unroll
    for (int u = 0; u < 4; u++) {
        if (u != wb) {
            const unsigned a = lds0 + L_RED + u * B_AREA + ((wb - u - 1) & 3) * 4096 + lo;
#pragma unroll
            for (int rg = 0; rg < 4; rg++) {
                f32x4 v = *(const LDS_AS f32x4*)(size_t)(a + rg * 1024);
                o[4 * rg] += v[0]; o[4 * rg + 1] += v[1]; o[4 * rg + 2] += v[2]; o[4 * rg + 3] += v[3];
            }
        }
    }
    {
        const float fg = 1.0f - *(const LDS_AS float*)(size_t)(lds0 + L_G + (64 * th + 32 * (wb & 1) + l31) * 4);
#pragma unroll
        for (int rg = 0; rg < 4; rg++) {
            const u32x2 bv = b2v[rg];
            o[4 * rg] = (o[4 * rg] + __uint_as_float(bv[0] << 16)) * fg; o[4 * rg + 1] = (o[4 * rg + 1] + __uint_as_float(bv[0] & 0xffff0000u)) * fg;
            o[4 * rg + 2] = (o[4 * rg + 2] + __uint_as_float(bv[1] << 16)) * fg; o[4 * rg + 3] = (o[4 * rg + 3] + __uint_as_float(bv[1] & 0xffff0000u)) * fg;
        }
    }
    {
        const unsigned ob = (unsigned)WS_OF + Tm * 32 * 1024 + lo;
        const unsigned foo = WS_FO + (Tm * 7 + cs) * 128 + (lane & 15) * 8;
        bf16x8 of[32];
        unsigned ready = 0;
#pragma unroll
        for (int q = 0; q < 4; q++) {
#pragma unroll 1
            for (int spin = 0; spin < SPIN_CAP && ((ready >> (4 * q)) & 15u) != 15u; spin++) {
                if (spin) __builtin_amdgcn_s_sleep(POLL_SLEEP);
                u64 v = 0;
                if (lane < 16) v = __hip_atomic_load((const u64*)(p.ws + foo), __ATOMIC_RELAXED, __HIP_MEMORY_SCOPE_AGENT);
                ready = (unsigned)__builtin_amdgcn_ballot_w64(lane < 16 && v == tag);
            }
            asm volatile("" ::: "memory");
            #pragma unroll
            for (int st = 8 * q; st < 8 * q + 8; st++) of[st] = ld_sc1(wsr, ob + st * 1024);
        }
        const unsigned lp16 = lds0 + L_WO + (wb >> 1) * 512 + ((lane & 15) >> 2) * 64 + ((lane >> 4) & 1) * 32 + (lane & 3) * 8;
        const unsigned wA = lp16 + h * 256, wB = lp16 + h * 768;
        f32x16 o2 = {};
#pragma unroll
        for (int st = 0; st < 32; st++) {
            const int hd = st >> 1, s = st & 1;
            const int r1 = 28 * hd + 16 * s, r2 = 28 * hd + (s ? 20 : 8);
            const unsigned a1 = ((r1 & 4) ? wB : wA) + (r1 >> 3) * 1024 + (r1 & 7) * 64, a2 = ((r2 & 4) ? wB : wA) + (r2 >> 3) * 1024 + (r2 & 7) * 64;
            const bf16x8 w0 = lds_tr2(a1, a2);
            if (st & 1) o2 = MFMA(w0, of[st], o2); else o = MFMA(w0, of[st], o);
        }
        o += o2;
    }
    {
#define OUT_AUX 0
        const __amdgpu_buffer_rsrc_t ro = __builtin_amdgcn_make_buffer_rsrc((void*)p.out, 0, 2048 * D_ * 2, 0x00020000);
        const unsigned oo = ((Tm * 32 + l31) * D_ + nc) * 2;
#pragma unroll
        for (int rg = 0; rg < 4; rg++) {
            u32x2 ov = {cvtpk(o[4 * rg], o[4 * rg + 1]), cvtpk(o[4 * rg + 2], o[4 * rg + 3])};
            __builtin_amdgcn_raw_buffer_store_b64(ov, ro, oo + 16 * rg, 0, OUT_AUX);
        }
    }
}

__global__ void __launch_bounds__(512, 2) gated_mix_fused(Params p) {
    extern __shared__ char smem[];
    const unsigned lds0 = (unsigned)(size_t)smem;
    const int b = blockIdx.x, xcd = b & 7, idx = b >> 3;
    const int g = xcd * 2 + (idx & 1), j = idx < 8 ? 12 + (idx >> 1) : (idx - 8) >> 1;
    const int w = __builtin_amdgcn_readfirstlane(threadIdx.x >> 6), lane = threadIdx.x & 63;
    const u64 epoch = __hip_atomic_load((const u64*)(p.ws + WS_EPOCH), __ATOMIC_RELAXED, __HIP_MEMORY_SCOPE_AGENT);
    if (w == 0 && lane < 16) *(LDS_AS unsigned*)(size_t)(lds0 + L_CTR + 4 * lane) = 0;
    wait_bar();
    if (w < 4) a_wave(p, lds0, g, j, w, lane, epoch);
    else b_wave(p, lds0, g, j, w - 4, lane, epoch);
}

extern "C" void kernel_launch(void* const* d_in, const int* in_sizes, int n_in, void* d_out, int out_size, void* d_ws, size_t ws_size, hipStream_t stream) {
    Params p;
    p.x = (const unsigned short*)d_in[0]; p.wq = (const unsigned short*)d_in[1]; p.wk = (const unsigned short*)d_in[2];
    p.wv = (const unsigned short*)d_in[3]; p.wo = (const unsigned short*)d_in[4]; p.w1 = (const unsigned short*)d_in[6];
    p.b1 = (const unsigned short*)d_in[7]; p.w2 = (const unsigned short*)d_in[8]; p.b2 = (const unsigned short*)d_in[9];
    p.gw = (const unsigned short*)d_in[10]; p.out = (unsigned short*)d_out; p.ws = (char*)d_ws;
    (void)hipFuncSetAttribute((const void*)gated_mix_fused, hipFuncAttributeMaxDynamicSharedMemorySize, LDS_BYTES);
    gated_mix_fused<<<256, 512, LDS_BYTES, stream>>>(p);
}
```

```cpp
#define FH1 4
#define LFFN_GATE 1
#include <hip/hip_runtime.h>
#include <hip/hip_bf16.h>

typedef short bf16x8 __attribute__((ext_vector_type(8)));
typedef short s16x4 __attribute__((ext_vector_type(4)));
typedef __bf16 bf16x2 __attribute__((ext_vector_type(2)));
typedef float f32x16 __attribute__((ext_vector_type(16)));
typedef float f32x4 __attribute__((ext_vector_type(4)));
typedef float f32x2 __attribute__((ext_vector_type(2)));
typedef unsigned u32x4 __attribute__((ext_vector_type(4)));
typedef unsigned u32x2 __attribute__((ext_vector_type(2)));
typedef short v4i16_t __attribute__((ext_vector_type(4)));
typedef unsigned long long u64;
#define LDS_AS __attribute__((address_space(3)))
#define DI __device__ __forceinline__

constexpr int S_ = 2048, D_ = 448, F_ = 1792;
constexpr size_t WS_EPOCH = 0;
constexpr size_t WS_FKV = 65536, FKV_COPY = 1280;
constexpr size_t WS_FH = 163840;
constexpr size_t WS_FO = 294912;
constexpr size_t WS_KV = 1u << 20;
constexpr size_t WS_OF = 2u << 20;
constexpr size_t WS_HF = 4u << 20;

#define STAMP(i) do {} while (0)
#define MFMA(a, b, c) __builtin_amdgcn_mfma_f32_32x32x16_bf16(a, b, c, 0, 0, 0)
#define SB __builtin_amdgcn_sched_barrier(0)
#define EX2(x) __builtin_amdgcn_exp2f(x)
constexpr int SPIN_CAP = 1 << 21;
#define POLL_SLEEP 2
#define G_PRIO 3
#define F_PRIO 0
#define KV_PRIO 3
#define A1_PRIO 2
#define A2_PRIO 0
#define B_PRIO 2

DI unsigned cvtpk(float lo, float hi) { f32x2 v = {lo, hi}; return __builtin_bit_cast(unsigned, __builtin_convertvector(v, bf16x2)); }
DI void glds16(unsigned voff, const void* sbase, unsigned lds_dst) {
    unsigned keep;
    asm volatile("s_mov_b32 %0, m0\n\ts_mov_b32 m0, %3\n\ts_nop 0\n\tglobal_load_lds_dwordx4 %1, %2\n\ts_mov_b32 m0, %0"
                 : "=&s"(keep) : "v"(voff), "s"(sbase), "s"(lds_dst) : "memory");
}
DI void glds16_sc1(unsigned voff, const void* sbase, unsigned lds_dst) {
    unsigned keep;
    asm volatile("s_mov_b32 %0, m0\n\ts_mov_b32 m0, %3\n\ts_nop 0\n\tglobal_load_lds_dwordx4 %1, %2 sc1\n\ts_mov_b32 m0, %0"
                 : "=&s"(keep) : "v"(voff), "s"(sbase), "s"(lds_dst) : "memory");
}
DI bf16x8 lds_b128(unsigned a) { return *(const LDS_AS bf16x8*)(size_t)a; }
DI s16x4 lds_tr(unsigned a) { return __builtin_bit_cast(s16x4, __builtin_amdgcn_ds_read_tr16_b64_v4i16((LDS_AS v4i16_t*)(size_t)a)); }
DI bf16x8 lds_tr2(unsigned a0, unsigned a1) { s16x4 lo = lds_tr(a0), hi = lds_tr(a1); return __builtin_shufflevector(lo, hi, 0, 1, 2, 3, 4, 5, 6, 7); }
DI float dot2(unsigned a, unsigned b, float c) { return __builtin_amdgcn_fdot2_f32_bf16(__builtin_bit_cast(bf16x2, a), __builtin_bit_cast(bf16x2, b), c, false); }
DI u32x4 pack8(const f32x16& a, int s) {
    u32x4 r;
    r[0] = cvtpk(a[8 * s + 0], a[8 * s + 1]); r[1] = cvtpk(a[8 * s + 2], a[8 * s + 3]);
    r[2] = cvtpk(a[8 * s + 4], a[8 * s + 5]); r[3] = cvtpk(a[8 * s + 6], a[8 * s + 7]);
    return r;
}
DI float gelu_tanh(float x) {
    const float c1 = -2.0f * 1.4426950408889634f * 0.7978845608028654f, c2 = c1 * 0.044715f;
    float t = x * x;
    float e = EX2(x * __builtin_fmaf(t, c2, c1));
    return x * __builtin_amdgcn_rcpf(1.0f + e);
}
typedef float f32x2_ __attribute__((ext_vector_type(2)));
DI f32x2_ gelu_tanh2(f32x2_ x) {
    const float c1 = -2.0f * 1.4426950408889634f * 0.7978845608028654f, c2 = c1 * 0.044715f;
    const f32x2_ z = x * __builtin_elementwise_fma(x * x, (f32x2_){c2, c2}, (f32x2_){c1, c1});
    const f32x2_ d = (f32x2_){EX2(z[0]), EX2(z[1])} + 1.0f;
    return x * (f32x2_){__builtin_amdgcn_rcpf(d[0]), __builtin_amdgcn_rcpf(d[1])};
}
DI __amdgpu_buffer_rsrc_t ws_rsrc(const char* ws) { return __builtin_amdgcn_make_buffer_rsrc((void*)ws, 0, 16u << 20, 0x00020000); }
DI void st_sc1(__amdgpu_buffer_rsrc_t r, unsigned off, u32x4 v) { __builtin_amdgcn_raw_buffer_store_b128(v, r, off, 0, 16); }
DI void flag_set(char* ws, size_t off, u64 tag) { __hip_atomic_store((u64*)(ws + off), tag, __ATOMIC_RELAXED, __HIP_MEMORY_SCOPE_AGENT); }
DI void flags_wait(const char* ws, unsigned off, bool on, u64 tag) {
#pragma unroll 1
    for (int spin = 0; spin < SPIN_CAP; spin++) {
        u64 v = tag;
        if (on) v = __hip_atomic_load((const u64*)(ws + off), __ATOMIC_RELAXED, __HIP_MEMORY_SCOPE_AGENT);
        if (__builtin_amdgcn_ballot_w64(v != tag) == 0) break;
        __builtin_amdgcn_s_sleep(POLL_SLEEP);
    }
    asm volatile("s_waitcnt vmcnt(0)" ::: "memory");
}
DI u64 uniform64(u64 v) {
    asm volatile("" : "+v"(v) :: "memory");
    const unsigned lo = __builtin_amdgcn_readfirstlane((unsigned)v), hi = __builtin_amdgcn_readfirstlane((unsigned)(v >> 32));
    return ((u64)hi << 32) | lo;
}
DI unsigned lds_ld(unsigned a) { return __hip_atomic_load((LDS_AS unsigned*)(size_t)a, __ATOMIC_RELAXED, __HIP_MEMORY_SCOPE_WORKGROUP); }
DI void lds_st(unsigned a, unsigned v) { __hip_atomic_store((LDS_AS unsigned*)(size_t)a, v, __ATOMIC_RELAXED, __HIP_MEMORY_SCOPE_WORKGROUP); }
DI void lds_sync4(unsigned ctr, unsigned target, int lane) {
    asm volatile("s_waitcnt lgkmcnt(0)" ::: "memory");
    LDS_AS unsigned* c = (LDS_AS unsigned*)(size_t)ctr;
    if (lane == 0) __hip_atomic_fetch_add(c, 1u, __ATOMIC_RELAXED, __HIP_MEMORY_SCOPE_WORKGROUP);
#pragma unroll 1
    for (int spin = 0; spin < SPIN_CAP; spin++) {
        unsigned v = __builtin_amdgcn_readfirstlane(__hip_atomic_load(c, __ATOMIC_RELAXED, __HIP_MEMORY_SCOPE_WORKGROUP));
        if (v >= target) break;
        __builtin_amdgcn_s_sleep(1);
    }
    asm volatile("" ::: "memory");
}

struct Params {
    const unsigned short *x, *wq, *wk, *wv, *wo, *w1, *b1, *w2, *b2, *gw;
    unsigned short* out;
    char* ws;
};

constexpr unsigned X_SLOT = 16384, L_WQ = 114688, L_RING = 143360, L_QX = L_WQ;
constexpr unsigned L_WO = 0, WO_SLICE = 14336, L_MRG = 57344, L_RED = 106496, B_AREA = 12288;
constexpr unsigned P1_GW = 159744, L_G = P1_GW + 1024;
constexpr unsigned L_CTR = L_G + 512;
constexpr unsigned C_QW = 0, C_QR = 4, C_OW = 8, C_RED = 12, C_AP = 16, C_B1 = 20, C_KVS = 24, L_XF = L_CTR + 32, L_XE = L_CTR + 48;
constexpr unsigned LDS_BYTES = L_CTR + 64;
static_assert(L_RING + 4 * 4096 <= P1_GW && L_RED + 4 * B_AREA <= P1_GW && L_MRG + 12 * 4096 <= L_RED && LDS_BYTES <= 163840 && (L_XF & 15) == 0, "LDS map");

template <int N> DI void wait_vm() { asm volatile("s_waitcnt vmcnt(%0)" ::"n"(N) : "memory"); }
DI void wait_bar() { asm volatile("s_waitcnt lgkmcnt(0)\n\ts_barrier" ::: "memory"); }
DI u32x4 lds_flags(unsigned a) { u32x4 v; asm volatile("ds_read_b128 %0, %1" : "=v"(v) : "v"(a) : "memory"); return v; }
DI void lds_wait_ge(unsigned a, unsigned need) {
#pragma unroll 1
    for (int spin = 0; spin < SPIN_CAP; spin++) {
        if ((unsigned)__builtin_amdgcn_readfirstlane(lds_ld(a)) >= need) break;
        __builtin_amdgcn_s_sleep(1);
    }
    asm volatile("" ::: "memory");
}
DI void x_wait(unsigned xf, unsigned need, int only) {
#pragma unroll 1
    for (int spin = 0; spin < SPIN_CAP; spin++) {
        unsigned m;
        if (only < 0) {
            const unsigned a = lds_ld(xf), b = lds_ld(xf + 4), c = lds_ld(xf + 8), d = lds_ld(xf + 12);
            m = min(min(a, b), min(c, d));
        } else m = lds_ld(xf + 4 * only);
        if ((unsigned)__builtin_amdgcn_readfirstlane(m) >= need) break;
        __builtin_amdgcn_s_sleep(1);
    }
    asm volatile("" ::: "memory");
}
DI unsigned x_addr(unsigned lds0, int lane, int s) {
    const int h = lane >> 5, l31 = lane & 31;
    return lds0 + l31 * 128 + (((2 * s + h) ^ ((l31 >> 1) & 7)) << 4);
}
DI unsigned w_lane(int lane) { return (lane >> 5) * 512 + ((lane & 15) >> 2) * 64 + ((lane >> 4) & 1) * 32 + (lane & 3) * 8; }
#define NR1 8
template <typename Ld>
DI void proj_loop(f32x16 (&acc)[4], unsigned lds0, unsigned ring, int lane, bool light, Ld&& ld, unsigned long long* ptb) {
    constexpr int NR = NR1;
    u32x4 R[NR];
#define W1_DELAY 3
    if (light) { if (LFFN_GATE) lds_wait_ge(lds0 + L_CTR + C_KVS, 2); else x_wait(lds0 + L_XE, 1, -1); }
    else if (W1_DELAY > 0) x_wait(lds0 + L_XF, W1_DELAY, -1);
#pragma unroll
    for (int s = 0; s < NR; s++) R[s] = ld(s);
    unsigned xa[4];
#pragma unroll
    for (int s = 0; s < 4; s++) xa[s] = x_addr(lds0, lane, s);
    const unsigned wr = ring + lane * 16, rd = ring + w_lane(lane), xf = lds0 + L_XF;
    bf16x8 xf_[3][4], wf[3];
#define PWRITE(s_) do { *(LDS_AS u32x4*)(size_t)(wr + ((s_) & 3) * 1024) = R[(s_) % NR]; if ((s_) + NR < 28) R[(s_) % NR] = ld((s_) + NR); } while (0)
#define PREADW(s_) do { wf[(s_) % 3] = lds_tr2(rd + ((s_) & 3) * 1024, rd + ((s_) & 3) * 1024 + 256); } while (0)
#define PREADX(s_, t_) do { xf_[(s_) % 3][t_] = lds_b128(xa[(s_) & 3] + ((s_) >> 2) * X_SLOT + (t_) * 4096); } while (0)
    PWRITE(0); PWRITE(1); PWRITE(2);
    x_wait(xf, 1, -1);
    PREADW(0); PREADX(0, 0); PREADX(0, 1); PREADX(0, 2); PREADX(0, 3);
    PREADW(1); PREADX(1, 0); PREADX(1, 1); PREADX(1, 2); PREADX(1, 3);
#pragma unroll
    for (int s = 0; s < 28; s++) {
        SB;
        if ((s & 3) == 2 && s + 2 < 28) x_wait(xf, (s + 2) / 4 + 1, -1);
        acc[0] = MFMA(wf[s % 3], xf_[s % 3][0], acc[0]); SB;
        if (s + 2 < 28) { PREADW(s + 2); PREADX(s + 2, 0); } SB;
        acc[1] = MFMA(wf[s % 3], xf_[s % 3][1], acc[1]); SB;
        if (s + 2 < 28) { PREADX(s + 2, 1); PREADX(s + 2, 2); } SB;
        acc[2] = MFMA(wf[s % 3], xf_[s % 3][2], acc[2]); SB;
        if (s + 2 < 28) PREADX(s + 2, 3);
        if (s + 3 < 28) PWRITE(s + 3);
        SB;
        acc[3] = MFMA(wf[s % 3], xf_[s % 3][3], acc[3]);
    }
    SB;
#undef PWRITE
#undef PREADW
#undef PREADX
}
DI void gelu_store(const Params& p, const f32x16& acc, const u32x2 (&bv)[4], int T, int fb, int lane) {
    f32x16 a;
#pragma unroll
    for (int q = 0; q < 4; q++) {
        a[4 * q + 0] = acc[4 * q + 0] + __uint_as_float(bv[q][0] << 16); a[4 * q + 1] = acc[4 * q + 1] + __uint_as_float(bv[q][0] & 0xffff0000u);
        a[4 * q + 2] = acc[4 * q + 2] + __uint_as_float(bv[q][1] << 16); a[4 * q + 3] = acc[4 * q + 3] + __uint_as_float(bv[q][1] & 0xffff0000u);
    }
#pragma unroll
    for (int r = 0; r < 16; r += 2) { const f32x2_ y = gelu_tanh2((f32x2_){a[r], a[r + 1]}); a[r] = y[0]; a[r + 1] = y[1]; }
    const unsigned dst = (unsigned)WS_HF + (T * 112 + 2 * fb) * 1024 + lane * 16;
    const __amdgpu_buffer_rsrc_t r_ = ws_rsrc(p.ws);
    st_sc1(r_, dst, pack8(a, 0)); st_sc1(r_, dst + 1024, pack8(a, 1));
}

DI float max16(const f32x16& s) {
    float a = __builtin_fmaxf(__builtin_fmaxf(s[0], s[1]), s[2]), b = __builtin_fmaxf(__builtin_fmaxf(s[3], s[4]), s[5]);
#pragma unroll
    for (int r = 6; r < 14; r += 4) { a = __builtin_fmaxf(__builtin_fmaxf(a, s[r]), s[r + 1]); b = __builtin_fmaxf(__builtin_fmaxf(b, s[r + 2]), s[r + 3]); }
    a = __builtin_fmaxf(__builtin_fmaxf(a, s[14]), s[15]);
    return __builtin_fmaxf(a, b);
}
DI void set_max(bf16x8& q1, float m, int h) {
    auto rr = __builtin_amdgcn_permlane32_swap(__float_as_uint(m), __float_as_uint(m), false, false);
    const float nm = -__builtin_fmaxf(__uint_as_float(rr[0]), __uint_as_float(rr[1]));
    const unsigned a = cvtpk(nm, 0.f) & 0xffffu;
    const float r1 = nm - __uint_as_float(a << 16);
    const unsigned b = cvtpk(r1, 0.f) & 0xffffu;
    const float r2 = r1 - __uint_as_float(b << 16);
    const unsigned c = cvtpk(r2, 0.f) & 0xffffu;
    u32x4 t = __builtin_bit_cast(u32x4, q1);
    if (h == 0) { t[2] = a | (b << 16); t[3] = c; }
    q1 = __builtin_bit_cast(bf16x8, t);
}
DI bf16x8 ld_sc1s(__amdgpu_buffer_rsrc_t r, unsigned off, unsigned soff) { return __builtin_bit_cast(bf16x8, __builtin_amdgcn_raw_buffer_load_b128(r, off, soff, 16)); }
DI bf16x8 ld_sc1(__amdgpu_buffer_rsrc_t r, unsigned off) { return __builtin_bit_cast(bf16x8, __builtin_amdgcn_raw_buffer_load_b128(r, off, 0, 16)); }

DI void a_wave(const Params& p, unsigned lds0, int g, int j, int w, int lane, u64 epoch_v) {
    const int h = lane >> 5, l31 = lane & 31;
    const int T = 4 * g + w;
    f32x16 accq = {};
    STAMP(0);
    __builtin_amdgcn_s_setprio(A1_PRIO);
    unsigned xoff[4];
#pragma unroll
    for (int i = 0; i < 4; i++) {
        const int row = 32 * w + 8 * i + (lane >> 3);
        const int c = (lane & 7) ^ ((row >> 1) & 7);
        xoff[i] = row * 896 + c * 16;
    }
    const char* xb = (const char*)p.x + (size_t)g * 128 * 896;
    const unsigned qoff = (16 * w + (lane >> 2)) * 896 + ((lane & 3) == 3 ? 40 : (lane & 3) * 16);
    const char* qb_ = (const char*)p.wq + j * 56;
    glds16((lane < 56 ? lane : 55) * 16, p.gw, lds0 + P1_GW);
    auto dma = [&](int c) {
#pragma unroll
        for (int i = 0; i < 4; i++) glds16(xoff[i], xb + c * 128, lds0 + c * X_SLOT + (32 * w + 8 * i) * 128);
        glds16(qoff, qb_ + c * (64 * 896), lds0 + L_WQ + c * 4096 + w * 1024);
    };
    const bool light = j >= 12;
    if (light) {
        const int row = 32 * (j - 12) + 8 * w + (lane >> 3);
        const unsigned eo = row * 896 + (((lane & 7) ^ ((row >> 1) & 7)) << 4);
#pragma unroll
        for (int c = 0; c < 7; c++) glds16(eo, xb + c * 128, lds0 + c * X_SLOT + (32 * (j - 12) + 8 * w) * 128);
    }
    dma(0);
    if (!light) { dma(1); dma(2); }
    wait_bar();
    if (light) {
        wait_vm<5>();
        if (lane == 0) lds_st(lds0 + L_XE + 4 * w, 1);
        dma(1); dma(2); dma(3); dma(4); dma(5); dma(6);
    }
    u64 tag;
    const __amdgpu_buffer_rsrc_t wsr = ws_rsrc(p.ws);
    const int t0 = (4 * g + 4 * j + 16 * w) & 63;
    bf16x8 K0[4], K1[4], V0[4], V1[4];
    const unsigned kvb = (unsigned)WS_KV + lane * 16;
#define KVLOAD(set, vo) do { const unsigned o_ = (vo); K0[set] = ld_sc1(wsr, o_); K1[set] = ld_sc1(wsr, o_ + 1024); V0[set] = ld_sc1(wsr, o_ + 2048); V1[set] = ld_sc1(wsr, o_ + 3072); } while (0)
    {
        unsigned xa[4];
#pragma unroll
        for (int s = 0; s < 4; s++) xa[s] = x_addr(lds0, lane, s) + w * 4096;
        const unsigned wa = lds0 + L_WQ + w_lane(lane);
        unsigned short gwv[7];
        auto publish = [&](int c) {
            if (lane < 16) *(LDS_AS unsigned short*)(size_t)(lds0 + L_WQ + c * 4096 + w * 1024 + lane * 64 + 48) = gwv[c];
            asm volatile("s_waitcnt lgkmcnt(0)" ::: "memory");
            if (lane == 0) lds_st(lds0 + L_XF + 4 * w, c + 1);
        };
        auto qchunk = [&](int c) {
            bf16x8 xf[4], wf[4];
#pragma unroll
            for (int s = 0; s < 4; s++) { xf[s] = lds_b128(xa[s] + c * X_SLOT); wf[s] = lds_tr2(wa + c * 4096 + s * 1024, wa + c * 4096 + s * 1024 + 256); }
#pragma unroll
            for (int s = 0; s < 4; s++) accq = MFMA(wf[s], xf[s], accq);
        };
#define TC(c)
        if (light) wait_vm<30>(); else wait_vm<10>();
        TC(0);
#pragma unroll
        for (int c = 0; c < 7; c++) gwv[c] = *(const LDS_AS unsigned short*)(size_t)(lds0 + P1_GW + (64 * c + 16 * w + (lane & 15)) * 2);
        publish(0);
        if (light) wait_vm<25>(); else { dma(3); wait_vm<10>(); }
        TC(1); publish(1);
        if (light) wait_vm<20>(); else { dma(4); wait_vm<10>(); }
        TC(2); publish(2);
        if (light) wait_vm<15>(); else { dma(5); wait_vm<10>(); }
        TC(3); publish(3);
        if (!light) dma(6);
        wait_vm<10>(); TC(4); publish(4);
        x_wait(lds0 + L_XF, 2, -1); qchunk(0); qchunk(1); SB;
        wait_vm<5>(); TC(5); publish(5);
        x_wait(lds0 + L_XF, 4, -1); qchunk(2); qchunk(3); SB;
        wait_vm<0>(); TC(6); publish(6);
        tag = uniform64(epoch_v) + 1;
        const unsigned fo = WS_FKV + (g * 4 + (j >> 2)) * FKV_COPY + ((t0 + (lane >> 1)) & 63) * 16 + (lane & 1) * 8;
        u64 fv = tag;
        if (lane < 32) fv = __hip_atomic_load((const u64*)(p.ws + fo), __ATOMIC_RELAXED, __HIP_MEMORY_SCOPE_AGENT);
        x_wait(lds0 + L_XF, 6, -1); TC(8); qchunk(4); qchunk(5); SB; TC(9);
        x_wait(lds0 + L_XF, 7, -1); TC(11); qchunk(6); SB; TC(12);
        asm volatile("" : "+v"(fv) :: "memory");
        if (__builtin_amdgcn_ballot_w64(fv != tag) != 0) flags_wait(p.ws, fo, lane < 32, tag);
        TC(10);
        KVLOAD(0, kvb + t0 * 4096); KVLOAD(1, kvb + (t0 + 1) * 4096); KVLOAD(2, kvb + (t0 + 2) * 4096);
        SB;
    }
    const float zz = __uint_as_float(__builtin_amdgcn_permlane32_swap(__float_as_uint(accq[12]), __float_as_uint(accq[12]), false, false)[0]);
    const float gtok = __builtin_amdgcn_rcpf(1.0f + EX2(-1.4426950408889634f * zz));
    if (h == 0) *(LDS_AS float*)(size_t)(lds0 + L_G + (32 * w + l31) * 4) = gtok;
    bf16x8 q0[4], q1[4];
    bf16x8 qa, qb;
    {
        const float qs = 1.4426950408889634f * 0.1889822365046136f;
        f32x16 q = accq * qs;
        u32x4 a0 = pack8(q, 0), a1 = pack8(q, 1);
        if (h == 0) { a1[2] = 0; a1[3] = 0; }
        qa = __builtin_bit_cast(bf16x8, a0); qb = __builtin_bit_cast(bf16x8, a1);
    }
    asm volatile("s_waitcnt lgkmcnt(0)" ::: "memory");
    if (lane == 0) __hip_atomic_fetch_add((LDS_AS unsigned*)(size_t)(lds0 + L_CTR + C_AP), 1u, __ATOMIC_RELAXED, __HIP_MEMORY_SCOPE_WORKGROUP);
    const f32x16 zero = {};
    V1[3] = (bf16x8){}; K0[3] = V1[3]; K1[3] = V1[3]; V0[3] = V1[3];
    f32x16 S = MFMA(K0[0], qa, zero); S = MFMA(K1[0], qb, S);
    set_max(qb, max16(S), h);
    const unsigned lo = lane * 16;
    *(LDS_AS bf16x8*)(size_t)(lds0 + L_QX + w * 2048 + lo) = qa;
    *(LDS_AS bf16x8*)(size_t)(lds0 + L_QX + w * 2048 + 1024 + lo) = qb;
    lds_sync4(lds0 + L_CTR + C_QW, 4, lane);
#pragma unroll
    for (int b = 0; b < 4; b++) { q0[b] = lds_b128(lds0 + L_QX + b * 2048 + lo); q1[b] = lds_b128(lds0 + L_QX + b * 2048 + 1024 + lo); }
    asm volatile("s_waitcnt lgkmcnt(0)" ::: "memory");
    if (lane == 0) __hip_atomic_fetch_add((LDS_AS unsigned*)(size_t)(lds0 + L_CTR + C_QR), 1u, __ATOMIC_RELAXED, __HIP_MEMORY_SCOPE_WORKGROUP);
    TC(13);
    __builtin_amdgcn_s_setprio(A2_PRIO);
    S = MFMA(K0[0], q0[0], zero); S = MFMA(K1[0], q1[0], S);
    f32x16 O[4];
#pragma unroll
    for (int b = 0; b < 4; b++) O[b] = zero;
    u32x4 P1 = {0, 0, 0, 0};
#define ASTEP(c, b) do { \
        constexpr int pc = (b) == 0 ? ((c) + 3) & 3 : (c), pb = ((b) + 3) & 3, nc = (b) == 3 ? ((c) + 1) & 3 : (c), nb = ((b) + 1) & 3; \
        O[pb] = MFMA(V1[pc], __builtin_bit_cast(bf16x8, P1), O[pb]); SB; \
        u32x4 p0, p1; \
        p0[0] = cvtpk(EX2(S[0]), EX2(S[1])); p0[1] = cvtpk(EX2(S[2]), EX2(S[3])); SB; \
        f32x16 Sn = MFMA(K0[nc], q0[nb], zero); SB; \
        if ((b) == 1) { KVLOAD(pc3(c), vnext); } \
        p0[2] = cvtpk(EX2(S[4]), EX2(S[5])); p0[3] = cvtpk(EX2(S[6]), EX2(S[7])); SB; \
        Sn = MFMA(K1[nc], q1[nb], Sn); SB; \
        p1[0] = cvtpk(EX2(S[8]), EX2(S[9])); p1[1] = cvtpk(EX2(S[10]), EX2(S[11])); SB; \
        O[b] = MFMA(V0[c], __builtin_bit_cast(bf16x8, p0), O[b]); SB; \
        p1[2] = cvtpk(EX2(S[12]), EX2(S[13])); p1[3] = cvtpk(EX2(S[14]), EX2(S[15])); SB; \
        P1 = p1; S = Sn; } while (0)
#define pc3(c) (((c) + 3) & 3)
#define ATILE(c) do { \
        const int tn = tt + (c) + 3;     \
        const unsigned vnext = lo + (tn < 16 ? (unsigned)WS_KV + ((t0 + tn) & 63) * 4096u : 0x7f000000u); \
        ASTEP(c, 0); ASTEP(c, 1); ASTEP(c, 2); ASTEP(c, 3); } while (0)
#pragma unroll 1
    for (int tt = 0; tt < 16; tt += 4) {
        ATILE(0); ATILE(1); ATILE(2); ATILE(3);
    }
    O[3] = MFMA(V1[3], __builtin_bit_cast(bf16x8, P1), O[3]);
    STAMP(5);
    lds_wait_ge(lds0 + L_CTR + C_B1, 4);
#pragma unroll
    for (int b = 0; b < 4; b++) {
        if (b != w) {
            const unsigned a = lds0 + L_MRG + (b * 3 + ((w - b - 1) & 3)) * 4096 + lo;
#pragma unroll
            for (int rg = 0; rg < 4; rg++) {
                f32x4 v = {O[b][4 * rg], O[b][4 * rg + 1], O[b][4 * rg + 2], O[b][4 * rg + 3]};
                *(LDS_AS f32x4*)(size_t)(a + rg * 1024) = v;
            }
        }
    }
    f32x16 o = zero;
#pragma unroll
    for (int b = 0; b < 4; b++) if (b == w) { o = O[b]; qa = q0[b]; qb = q1[b]; }
    lds_sync4(lds0 + L_CTR + C_OW, 4, lane);
#pragma unroll
    for (int u = 0; u < 3; u++) {
        const unsigned a = lds0 + L_MRG + (w * 3 + u) * 4096 + lo;
#pragma unroll
        for (int rg = 0; rg < 4; rg++) {
            f32x4 v = *(const LDS_AS f32x4*)(size_t)(a + rg * 1024);
            o[4 * rg] += v[0]; o[4 * rg + 1] += v[1]; o[4 * rg + 2] += v[2]; o[4 * rg + 3] += v[3];
        }
    }
    STAMP(6);
    auto rr = __builtin_amdgcn_permlane32_swap(__float_as_uint(o[12]), __float_as_uint(o[12]), false, false);
    float l = __uint_as_float(rr[0]);
    {
        bool ok = l < 1.152921504606846976e18f && l > 8.673617379884035e-19f;
#pragma unroll
        for (int r = 0; r < 16; r++) ok = ok && (__builtin_fabsf(o[r]) < 3.0e38f);
        if (__builtin_amdgcn_ballot_w64(!ok) != 0) {
            u32x4 tq = __builtin_bit_cast(u32x4, qb);
            if (h == 0) { tq[2] = 0; tq[3] = 0; }
            qb = __builtin_bit_cast(bf16x8, tq);
            float m = -INFINITY;
#pragma unroll 1
            for (int t = 0; t < 64; t++) {
                bf16x8 k0 = ld_sc1(wsr, kvb + t * 4096), k1 = ld_sc1(wsr, kvb + t * 4096 + 1024);
                f32x16 s = MFMA(k0, qa, zero); s = MFMA(k1, qb, s);
                m = __builtin_fmaxf(m, max16(s));
            }
            set_max(qb, m, h);
            o = zero;
#pragma unroll 1
            for (int t = 0; t < 64; t++) {
                bf16x8 k0 = ld_sc1(wsr, kvb + t * 4096), k1 = ld_sc1(wsr, kvb + t * 4096 + 1024);
                bf16x8 v0 = ld_sc1(wsr, kvb + t * 4096 + 2048), v1 = ld_sc1(wsr, kvb + t * 4096 + 3072);
                f32x16 s = MFMA(k0, qa, zero); s = MFMA(k1, qb, s);
#pragma unroll
                for (int r = 0; r < 16; r++) s[r] = EX2(s[r]);
                o = MFMA(v0, __builtin_bit_cast(bf16x8, pack8(s, 0)), o); o = MFMA(v1, __builtin_bit_cast(bf16x8, pack8(s, 1)), o);
            }
            auto r2 = __builtin_amdgcn_permlane32_swap(__float_as_uint(o[12]), __float_as_uint(o[12]), false, false);
            l = __uint_as_float(r2[0]);
        }
    }
    const float sc = gtok * __builtin_amdgcn_rcpf(l);
#pragma unroll
    for (int r = 0; r < 16; r++) o[r] *= sc;
    u32x4 o0 = pack8(o, 0), o1 = pack8(o, 1);
    if (h == 0) { o1[2] = 0; o1[3] = 0; }
    const unsigned dst = (unsigned)WS_OF + (T * 32 + 2 * j) * 1024 + lo;
    st_sc1(wsr, dst, o0); st_sc1(wsr, dst + 1024, o1);
    asm volatile("s_waitcnt vmcnt(0)" ::: "memory");
    if (lane < 7) flag_set(p.ws, WS_FO + (T * 7 + lane) * 128 + j * 8, tag);
    STAMP(7);
}

DI void b_wave(const Params& p, unsigned lds0, int g, int j, int wb, int lane, u64 epoch_v) {
    const int h = lane >> 5, l31 = lane & 31;
    const bool light = j >= 12;
    const int lp = j - 12;
    const int fb = light ? 48 + 2 * lp + wb : 4 * j + wb;
    const unsigned lo = lane * 16;
    f32x16 acc[4];
#pragma unroll
    for (int b = 0; b < 4; b++) acc[b] = (f32x16){};
    STAMP(0);
    __builtin_amdgcn_s_setprio(B_PRIO);
    const __amdgpu_buffer_rsrc_t wsr = ws_rsrc(p.ws);
    const unsigned ring = lds0 + L_RING + wb * 4096;
    u64 tag;
    if (light && wb >= 2) {
        __builtin_amdgcn_s_setprio(KV_PRIO);
        const int T = 4 * g + lp;
        const __amdgpu_buffer_rsrc_t rk = __builtin_amdgcn_make_buffer_rsrc((void*)(wb == 2 ? p.wk : p.wv), 0, 448 * 56, 0x00020000);
        const unsigned off = (lane >> 2) * 56 + ((lane & 3) == 3 ? 40 : (lane & 3) * 16);
        const unsigned dst = (unsigned)WS_KV + T * 4096 + lo;
        u32x4 R[28];
#pragma unroll
        for (int s = 0; s < 28; s++) R[s] = __builtin_amdgcn_raw_buffer_load_b128(rk, off, s * (16 * 56), 0);
        SB; wait_bar(); SB;
        const unsigned wr = ring + lo, rd = ring + w_lane(lane);
        bf16x8 wf[28];
#pragma unroll
        for (int q = 0; q < 28; q += 4) {
#pragma unroll
            for (int u = 0; u < 4; u++) *(LDS_AS u32x4*)(size_t)(wr + u * 1024) = R[q + u];
#pragma unroll
            for (int u = 0; u < 4; u++) wf[q + u] = lds_tr2(rd + u * 1024, rd + u * 1024 + 256);
            SB;
        }
        STAMP(10);
        x_wait(lds0 + L_XE, 1, -1);
        STAMP(11);
        f32x16 a2 = {};
        {
            unsigned xa[4];
#pragma unroll
            for (int s = 0; s < 4; s++) xa[s] = x_addr(lds0, lane, s) + lp * 4096;
            bf16x8 xf[2][4];
#define KVX(q_) _Pragma("unroll") for (int u = 0; u < 4; u++) xf[(q_) & 1][u] = lds_b128(xa[u] + (q_) * X_SLOT)
            KVX(0); KVX(1);
            if (wb == 2) {
#pragma unroll
                for (int q = 0; q < 7; q++) {
                    SB;
#pragma unroll
                    for (int u = 0; u < 4; u++) { if (u & 1) a2 = MFMA(wf[4 * q + u], xf[q & 1][u], a2); else acc[0] = MFMA(wf[4 * q + u], xf[q & 1][u], acc[0]); }
                    SB;
                    if (q + 2 < 7) { KVX(q + 2); }
                }
            } else {
#pragma unroll
                for (int q = 0; q < 7; q++) {
                    SB;
#pragma unroll
                    for (int u = 0; u < 4; u++) { if (u & 1) a2 = MFMA(xf[q & 1][u], wf[4 * q + u], a2); else acc[0] = MFMA(xf[q & 1][u], wf[4 * q + u], acc[0]); }
                    SB;
                    if (q + 2 < 7) { KVX(q + 2); }
                }
            }
        }
        acc[0] += a2;
        STAMP(13);
        {
            u32x4 a0 = pack8(acc[0], 0), a1 = pack8(acc[0], 1);
            if (wb == 2) {
                if (h == 0) { a1[2] = 0x3f803f80u; a1[3] = 0x00003f80u; }
                st_sc1(wsr, dst, a0); st_sc1(wsr, dst + 1024, a1);
            } else {
                if (l31 == 24) { a0 = (u32x4){0x3f803f80u, 0x3f803f80u, 0x3f803f80u, 0x3f803f80u}; a1 = a0; }
                else if (l31 > 24 && l31 < 28) { a0 = (u32x4){0, 0, 0, 0}; a1 = a0; }
                st_sc1(wsr, dst + 2048, a0); st_sc1(wsr, dst + 3072, a1);
            }
        }
        STAMP(1);
        if (lane == 0) __hip_atomic_fetch_add((LDS_AS unsigned*)(size_t)(lds0 + L_CTR + C_B1), 1u, __ATOMIC_RELAXED, __HIP_MEMORY_SCOPE_WORKGROUP);
#define KVS_AT 1
        if (KVS_AT == 0 && lane == 0) __hip_atomic_fetch_add((LDS_AS unsigned*)(size_t)(lds0 + L_CTR + C_KVS), 1u, __ATOMIC_RELAXED, __HIP_MEMORY_SCOPE_WORKGROUP);
        tag = uniform64(epoch_v) + 1;
        asm volatile("s_waitcnt vmcnt(0)" ::: "memory");
        STAMP(14);
        flag_set(p.ws, WS_FKV + lane * FKV_COPY + T * 16 + (wb & 1) * 8, tag);
        if (KVS_AT == 1 && lane == 0) __hip_atomic_fetch_add((LDS_AS unsigned*)(size_t)(lds0 + L_CTR + C_KVS), 1u, __ATOMIC_RELAXED, __HIP_MEMORY_SCOPE_WORKGROUP);
        STAMP(15);
    } else {
        const __amdgpu_buffer_rsrc_t rw1 = __builtin_amdgcn_make_buffer_rsrc((void*)p.w1, 0, 448 * 3584, 0x00020000);
        const unsigned off = (lane >> 2) * 3584 + fb * 64 + (lane & 3) * 16;
        u32x2 bv[4];
#pragma unroll
        for (int q = 0; q < 4; q++) bv[q] = *(const u32x2*)(p.b1 + fb * 32 + 4 * h + 8 * q);
        wait_bar();
        auto ld = [&](int s) { return __builtin_amdgcn_raw_buffer_load_b128(rw1, off, s * (16 * 3584), 0); };
        unsigned long long ptb[8];
        proj_loop(acc, lds0, ring, lane, light, ld, ptb);
        STAMP(1);
        if (lane == 0) __hip_atomic_fetch_add((LDS_AS unsigned*)(size_t)(lds0 + L_CTR + C_B1), 1u, __ATOMIC_RELAXED, __HIP_MEMORY_SCOPE_WORKGROUP);
        tag = uniform64(epoch_v) + 1;
        __builtin_amdgcn_s_setprio(G_PRIO);
#pragma unroll
        for (int t = 0; t < 4; t++) gelu_store(p, acc[t], bv, 4 * g + t, fb, lane);
        __builtin_amdgcn_s_setprio(F_PRIO);
        STAMP(12);
        asm volatile("s_waitcnt vmcnt(0)" ::: "memory");
        if (lane < 14) flag_set(p.ws, WS_FH + (g * 14 + lane) * 512 + fb * 8, tag);
        STAMP(3);

    }
    const bool tile = j < 14;
    if (!tile) {
        if (g == 0 && j == 15 && wb == 0) {
#pragma unroll 1
            for (int stage = 0; stage < 3; stage++) {
                const char* base = p.ws + (stage == 0 ? WS_FKV + lane * 8 : stage == 1 ? WS_FH + lane * 8 : WS_FO + (lane >> 4) * (7 * 128) + (lane & 15) * 8);
                const int n = stage == 0 ? 2 : 16;
                const unsigned step = stage == 0 ? 512 : stage == 1 ? 14 * 512 : 4 * 7 * 128;
                const bool on = stage != 1 || lane < 56;
#pragma unroll 1
                for (int spin = 0; spin < SPIN_CAP; spin++) {
                    u64 v[16];
#pragma unroll
                    for (int i = 0; i < 16; i++) v[i] = __hip_atomic_load((const u64*)(base + (i < n ? i : 0) * step), __ATOMIC_RELAXED, __HIP_MEMORY_SCOPE_AGENT);
                    unsigned bad = 0;
#pragma unroll
                    for (int i = 0; i < 16; i++) bad |= (v[i] != tag) ? 1u : 0u;
                    if (__builtin_amdgcn_ballot_w64(on && bad) == 0) break;
                    __builtin_amdgcn_s_sleep(16);
                }
            }
            if (lane == 0) __hip_atomic_store((u64*)(p.ws + WS_EPOCH), tag, __ATOMIC_RELAXED, __HIP_MEMORY_SCOPE_AGENT);
        }
        return;
    }
    const unsigned stg = lds0 + L_WO + wb * WO_SLICE;
    const int th = j & 1, cs = j >> 1, n0 = 64 * cs, T0 = 4 * g + 2 * th;
    const unsigned woff = ((lane & 31) >> 2) * 896 + (lane >> 5) * 64 + (lane & 3) * 16;
    const __amdgpu_buffer_rsrc_t rw2 = __builtin_amdgcn_make_buffer_rsrc((void*)p.w2, 0, 1792 * 896, 0x00020000);
    const unsigned w2v = woff + n0 * 2, w2s = 28 * wb * 16 * 896;
#define NBW 7
#define NBH 7
#define WO_LATE 4
    constexpr int NB = NBW, NH = NBH;
    u32x4 Wr[NB][2];
#define ROT28(st_) (st_)
#define WLOAD(st_) do { const unsigned so_ = w2s + ROT28(st_) * (16 * 896); Wr[(st_) % NB][0] = __builtin_amdgcn_raw_buffer_load_b128(rw2, w2v, so_, 0); \
        Wr[(st_) % NB][1] = __builtin_amdgcn_raw_buffer_load_b128(rw2, w2v + 8 * 896, so_, 0); } while (0)
#pragma unroll
    for (int st = 0; st < NB; st++) WLOAD(st);
    const int Tm = T0 + (wb & 1);
    const int nc = n0 + 32 * (wb >> 1) + 4 * h;
    u32x2 b2v[4];
#pragma unroll
    for (int rg = 0; rg < 4; rg++) b2v[rg] = *(const u32x2*)(p.b2 + nc + 8 * rg);
    const unsigned fho = WS_FH + (g * 14 + j) * 512 + (14 * wb + lane) * 8;
    lds_wait_ge(lds0 + L_CTR + C_AP, 4);
    lds_wait_ge(lds0 + L_CTR + C_B1, 4);
    const char* wos = (const char*)p.wo + n0 * 2;
#pragma unroll
    for (int i = WO_LATE; i < 14; i++) glds16(woff, wos + (size_t)(14 * wb + i) * (8 * 896), lds0 + L_WO + (14 * wb + i) * 1024);
    flags_wait(p.ws, fho, lane < FH1, tag);
    STAMP(4);
#pragma unroll
    for (int i = 0; i < 4; i++) acc[i] = (f32x16){};
    {
        const unsigned wrd = stg + (4 * h + ((lane & 15) >> 2)) * 64 + ((lane >> 4) & 1) * 32 + (lane & 3) * 8;
        const unsigned hv = (unsigned)WS_HF + (T0 * 112 + 28 * wb) * 1024 + lo;
        bf16x8 H0[NH], H1[NH], wf0[2], wf1[2];
#define HLOAD(st_) do { const unsigned so_ = ROT28(st_) * 1024; H0[(st_) % NH] = ld_sc1s(wsr, hv, so_); H1[(st_) % NH] = ld_sc1s(wsr, hv + 112 * 1024, so_); } while (0)
#define WWRITE(st_) do { *(LDS_AS u32x4*)(size_t)(stg + ((st_) & 1) * 2048 + lo) = Wr[(st_) % NB][0]; *(LDS_AS u32x4*)(size_t)(stg + ((st_) & 1) * 2048 + 1024 + lo) = Wr[(st_) % NB][1]; } while (0)
#define WREAD(st_) do { wf0[(st_) & 1] = lds_tr2(wrd + ((st_) & 1) * 2048, wrd + ((st_) & 1) * 2048 + 1024); wf1[(st_) & 1] = lds_tr2(wrd + ((st_) & 1) * 2048 + 512, wrd + ((st_) & 1) * 2048 + 1536); } while (0)
#pragma unroll
        for (int st = 0; st < NH; st++) HLOAD(st);
        WWRITE(0); WLOAD(NB); WWRITE(1); WLOAD(NB + 1);
        WREAD(0);
#pragma unroll
        for (int st = 0; st < 28; st++) {
            SB;
            if (st + 1 < 28) WREAD(st + 1);
            if (st + 2 < 28) WWRITE(st + 2);
            if (st + 2 + NB < 28) WLOAD(st + 2 + NB);
            SB;
            acc[0] = MFMA(wf0[st & 1], H0[st % NH], acc[0]); acc[1] = MFMA(wf0[st & 1], H1[st % NH], acc[1]);
            acc[2] = MFMA(wf1[st & 1], H0[st % NH], acc[2]); acc[3] = MFMA(wf1[st & 1], H1[st % NH], acc[3]);
            SB;
            if (st + NH == 2 * FH1) {
                u64 fv = tag;
                if (lane >= FH1 && lane < 14) fv = __hip_atomic_load((const u64*)(p.ws + fho), __ATOMIC_RELAXED, __HIP_MEMORY_SCOPE_AGENT);
                if (__builtin_amdgcn_ballot_w64(fv != tag) != 0) flags_wait(p.ws, fho, lane >= FH1 && lane < 14, tag);
                asm volatile("" ::: "memory");
            }
            if (st + NH < 28) HLOAD(st + NH);
        }
    }
    STAMP(5);
    asm volatile("s_waitcnt lgkmcnt(0)" ::: "memory");
#pragma unroll
    for (int i = 0; i < WO_LATE; i++) glds16(woff, wos + (size_t)(14 * wb + i) * (8 * 896), lds0 + L_WO + (14 * wb + i) * 1024);
    lds_wait_ge(lds0 + L_CTR + C_QR, 4);
    {
        const unsigned rarea = lds0 + L_RED + wb * B_AREA;
#pragma unroll
        for (int i = 0; i < 4; i++) {
            if (i != wb) {
                const unsigned a = rarea + ((i - wb - 1) & 3) * 4096 + lo;
#pragma unroll
                for (int rg = 0; rg < 4; rg++) {
                    f32x4 v = {acc[i][4 * rg], acc[i][4 * rg + 1], acc[i][4 * rg + 2], acc[i][4 * rg + 3]};
                    *(LDS_AS f32x4*)(size_t)(a + rg * 1024) = v;
                }
            }
        }
    }
    f32x16 o = {};
#pragma unroll
    for (int i = 0; i < 4; i++) if (i == wb) o = acc[i];
    asm volatile("s_waitcnt vmcnt(0)" ::: "memory");
    lds_sync4(lds0 + L_CTR + C_RED, 4, lane);
#pragma unroll
    for (int u = 0; u < 4; u++) {
        if (u != wb) {
            const unsigned a = lds0 + L_RED + u * B_AREA + ((wb - u - 1) & 3) * 4096 + lo;
#pragma unroll
            for (int rg = 0; rg < 4; rg++) {
                f32x4 v = *(const LDS_AS f32x4*)(size_t)(a + rg * 1024);
                o[4 * rg] += v[0]; o[4 * rg + 1] += v[1]; o[4 * rg + 2] += v[2]; o[4 * rg + 3] += v[3];
            }
        }
    }
    {
        const float fg = 1.0f - *(const LDS_AS float*)(size_t)(lds0 + L_G + (64 * th + 32 * (wb & 1) + l31) * 4);
#pragma unroll
        for (int rg = 0; rg < 4; rg++) {
            const u32x2 bv = b2v[rg];
            o[4 * rg] = (o[4 * rg] + __uint_as_float(bv[0] << 16)) * fg; o[4 * rg + 1] = (o[4 * rg + 1] + __uint_as_float(bv[0] & 0xffff0000u)) * fg;
            o[4 * rg + 2] = (o[4 * rg + 2] + __uint_as_float(bv[1] << 16)) * fg; o[4 * rg + 3] = (o[4 * rg + 3] + __uint_as_float(bv[1] & 0xffff0000u)) * fg;
        }
    }
    STAMP(6);
    {
        const unsigned ob = (unsigned)WS_OF + Tm * 32 * 1024 + lo;
        const unsigned foo = WS_FO + (Tm * 7 + cs) * 128 + (lane & 15) * 8;
        bf16x8 of[32];
        unsigned ready = 0;
#pragma unroll
        for (int q = 0; q < 4; q++) {
#pragma unroll 1
            for (int spin = 0; spin < SPIN_CAP && ((ready >> (4 * q)) & 15u) != 15u; spin++) {
                if (spin) __builtin_amdgcn_s_sleep(POLL_SLEEP);
                u64 v = 0;
                if (lane < 16) v = __hip_atomic_load((const u64*)(p.ws + foo), __ATOMIC_RELAXED, __HIP_MEMORY_SCOPE_AGENT);
                ready = (unsigned)__builtin_amdgcn_ballot_w64(lane < 16 && v == tag);
            }
            asm volatile("" ::: "memory");
            if (q == 3) STAMP(7);
#pragma unroll
            for (int st = 8 * q; st < 8 * q + 8; st++) of[st] = ld_sc1(wsr, ob + st * 1024);
        }
        const unsigned lp16 = lds0 + L_WO + (wb >> 1) * 512 + ((lane & 15) >> 2) * 64 + ((lane >> 4) & 1) * 32 + (lane & 3) * 8;
        const unsigned wA = lp16 + h * 256, wB = lp16 + h * 768;
        f32x16 o2 = {};
#pragma unroll
        for (int st = 0; st < 32; st++) {
            const int hd = st >> 1, s = st & 1;
            const int r1 = 28 * hd + 16 * s, r2 = 28 * hd + (s ? 20 : 8);
            const unsigned a1 = ((r1 & 4) ? wB : wA) + (r1 >> 3) * 1024 + (r1 & 7) * 64, a2 = ((r2 & 4) ? wB : wA) + (r2 >> 3) * 1024 + (r2 & 7) * 64;
            const bf16x8 w0 = lds_tr2(a1, a2);
            if (st & 1) o2 = MFMA(w0, of[st], o2); else o = MFMA(w0, of[st], o);
        }
        o += o2;
    }
    STAMP(8);
    {
#define OUT_AUX 0
        const __amdgpu_buffer_rsrc_t ro = __builtin_amdgcn_make_buffer_rsrc((void*)p.out, 0, 2048 * D_ * 2, 0x00020000);
        const unsigned oo = ((Tm * 32 + l31) * D_ + nc) * 2;
#pragma unroll
        for (int rg = 0; rg < 4; rg++) {
            u32x2 ov = {cvtpk(o[4 * rg], o[4 * rg + 1]), cvtpk(o[4 * rg + 2], o[4 * rg + 3])};
            __builtin_amdgcn_raw_buffer_store_b64(ov, ro, oo + 16 * rg, 0, OUT_AUX);
        }
    }
    STAMP(9);
}

__global__ void __launch_bounds__(512, 2) gated_mix_fused(Params p) {
    extern __shared__ char smem[];
    const unsigned lds0 = (unsigned)(size_t)smem;
    const int b = blockIdx.x, xcd = b & 7, idx = b >> 3;
    const int g = xcd * 2 + (idx & 1), j = idx < 8 ? 12 + (idx >> 1) : (idx - 8) >> 1;
    const int w = __builtin_amdgcn_readfirstlane(threadIdx.x >> 6), lane = threadIdx.x & 63;
    const u64 epoch = __hip_atomic_load((const u64*)(p.ws + WS_EPOCH), __ATOMIC_RELAXED, __HIP_MEMORY_SCOPE_AGENT);
    if (w == 0 && lane < 16) *(LDS_AS unsigned*)(size_t)(lds0 + L_CTR + 4 * lane) = 0;
    if (w < 4) a_wave(p, lds0, g, j, w, lane, epoch);
    else b_wave(p, lds0, g, j, w - 4, lane, epoch);
}

extern "C" void kernel_launch(void* const* d_in, const int* in_sizes, int n_in, void* d_out, int out_size, void* d_ws, size_t ws_size, hipStream_t stream) {
    Params p;
    p.x = (const unsigned short*)d_in[0]; p.wq = (const unsigned short*)d_in[1]; p.wk = (const unsigned short*)d_in[2];
    p.wv = (const unsigned short*)d_in[3]; p.wo = (const unsigned short*)d_in[4]; p.w1 = (const unsigned short*)d_in[6];
    p.b1 = (const unsigned short*)d_in[7]; p.w2 = (const unsigned short*)d_in[8]; p.b2 = (const unsigned short*)d_in[9];
    p.gw = (const unsigned short*)d_in[10]; p.out = (unsigned short*)d_out; p.ws = (char*)d_ws;
    (void)hipFuncSetAttribute((const void*)gated_mix_fused, hipFuncAttributeMaxDynamicSharedMemorySize, LDS_BYTES);
    gated_mix_fused<<<256, 512, LDS_BYTES, stream>>>(p);
}
```

```cpp
#include <hip/hip_runtime.h>
#include <hip/hip_bf16.h>

typedef short bf16x8 __attribute__((ext_vector_type(8)));
typedef short s16x4 __attribute__((ext_vector_type(4)));
typedef __bf16 bf16x2 __attribute__((ext_vector_type(2)));
typedef float f32x16 __attribute__((ext_vector_type(16)));
typedef float f32x4 __attribute__((ext_vector_type(4)));
typedef float f32x2 __attribute__((ext_vector_type(2)));
typedef unsigned u32x4 __attribute__((ext_vector_type(4)));
typedef unsigned u32x2 __attribute__((ext_vector_type(2)));
typedef short v4i16_t __attribute__((ext_vector_type(4)));
typedef unsigned long long u64;
#define LDS_AS __attribute__((address_space(3)))
#define DI __device__ __forceinline__

constexpr int S_ = 2048, D_ = 448, F_ = 1792;
constexpr size_t WS_EPOCH = 0;
constexpr size_t WS_FKV = 65536, FKV_COPY = 1280;
constexpr size_t WS_FH = 163840;
constexpr size_t WS_FO = 294912;
constexpr size_t WS_KV = 1u << 20;
constexpr size_t WS_OF = 2u << 20;
constexpr size_t WS_HF = 4u << 20;

#define MFMA(a, b, c) __builtin_amdgcn_mfma_f32_32x32x16_bf16(a, b, c, 0, 0, 0)
#define SB __builtin_amdgcn_sched_barrier(0)
#define EX2(x) __builtin_amdgcn_exp2f(x)
constexpr int SPIN_CAP = 1 << 21;
#define POLL_SLEEP 2
#define G_PRIO 3
#define F_PRIO 0
#define KV_PRIO 3
#define A1_PRIO 2
#define A2_PRIO 0
#define B_PRIO 2

DI unsigned cvtpk(float lo, float hi) { f32x2 v = {lo, hi}; return __builtin_bit_cast(unsigned, __builtin_convertvector(v, bf16x2)); }
DI void glds16(unsigned voff, const void* sbase, unsigned lds_dst) {
    unsigned keep;
    asm volatile("s_mov_b32 %0, m0\n\ts_mov_b32 m0, %3\n\ts_nop 0\n\tglobal_load_lds_dwordx4 %1, %2\n\ts_mov_b32 m0, %0"
                 : "=&s"(keep) : "v"(voff), "s"(sbase), "s"(lds_dst) : "memory");
}
DI void glds16_sc1(unsigned voff, const void* sbase, unsigned lds_dst) {
    unsigned keep;
    asm volatile("s_mov_b32 %0, m0\n\ts_mov_b32 m0, %3\n\ts_nop 0\n\tglobal_load_lds_dwordx4 %1, %2 sc1\n\ts_mov_b32 m0, %0"
                 : "=&s"(keep) : "v"(voff), "s"(sbase), "s"(lds_dst) : "memory");
}
DI bf16x8 lds_b128(unsigned a) { return *(const LDS_AS bf16x8*)(size_t)a; }
DI s16x4 lds_tr(unsigned a) { return __builtin_bit_cast(s16x4, __builtin_amdgcn_ds_read_tr16_b64_v4i16((LDS_AS v4i16_t*)(size_t)a)); }
DI bf16x8 lds_tr2(unsigned a0, unsigned a1) { s16x4 lo = lds_tr(a0), hi = lds_tr(a1); return __builtin_shufflevector(lo, hi, 0, 1, 2, 3, 4, 5, 6, 7); }
DI float dot2(unsigned a, unsigned b, float c) { return __builtin_amdgcn_fdot2_f32_bf16(__builtin_bit_cast(bf16x2, a), __builtin_bit_cast(bf16x2, b), c, false); }
DI u32x4 pack8(const f32x16& a, int s) {
    u32x4 r;
    r[0] = cvtpk(a[8 * s + 0], a[8 * s + 1]); r[1] = cvtpk(a[8 * s + 2], a[8 * s + 3]);
    r[2] = cvtpk(a[8 * s + 4], a[8 * s + 5]); r[3] = cvtpk(a[8 * s + 6], a[8 * s + 7]);
    return r;
}
DI float gelu_tanh(float x) {
    const float c1 = -2.0f * 1.4426950408889634f * 0.7978845608028654f, c2 = c1 * 0.044715f;
    float t = x * x;
    float e = EX2(x * __builtin_fmaf(t, c2, c1));
    return x * __builtin_amdgcn_rcpf(1.0f + e);
}
typedef float f32x2_ __attribute__((ext_vector_type(2)));
DI f32x2_ gelu_tanh2(f32x2_ x) {
    const float c1 = -2.0f * 1.4426950408889634f * 0.7978845608028654f, c2 = c1 * 0.044715f;
    const f32x2_ z = x * __builtin_elementwise_fma(x * x, (f32x2_){c2, c2}, (f32x2_){c1, c1});
    const f32x2_ d = (f32x2_){EX2(z[0]), EX2(z[1])} + 1.0f;
    return x * (f32x2_){__builtin_amdgcn_rcpf(d[0]), __builtin_amdgcn_rcpf(d[1])};
}
DI __amdgpu_buffer_rsrc_t ws_rsrc(const char* ws) { return __builtin_amdgcn_make_buffer_rsrc((void*)ws, 0, 16u << 20, 0x00020000); }
DI void st_sc1(__amdgpu_buffer_rsrc_t r, unsigned off, u32x4 v) { __builtin_amdgcn_raw_buffer_store_b128(v, r, off, 0, 16); }
DI void flag_set(char* ws, size_t off, u64 tag) { __hip_atomic_store((u64*)(ws + off), tag, __ATOMIC_RELAXED, __HIP_MEMORY_SCOPE_AGENT); }
DI void flags_wait(const char* ws, unsigned off, bool on, u64 tag) {
#pragma unroll 1
    for (int spin = 0; spin < SPIN_CAP; spin++) {
        u64 v = tag;
        if (on) v = __hip_atomic_load((const u64*)(ws + off), __ATOMIC_RELAXED, __HIP_MEMORY_SCOPE_AGENT);
        if (__builtin_amdgcn_ballot_w64(v != tag) == 0) break;
        __builtin_amdgcn_s_sleep(POLL_SLEEP);
    }
    asm volatile("s_waitcnt vmcnt(0)" ::: "memory");
}
DI u64 uniform64(u64 v) {
    asm volatile("" : "+v"(v) :: "memory");
    const unsigned lo = __builtin_amdgcn_readfirstlane((unsigned)v), hi = __builtin_amdgcn_readfirstlane((unsigned)(v >> 32));
    return ((u64)hi << 32) | lo;
}
DI unsigned lds_ld(unsigned a) { return __hip_atomic_load((LDS_AS unsigned*)(size_t)a, __ATOMIC_RELAXED, __HIP_MEMORY_SCOPE_WORKGROUP); }
DI void lds_st(unsigned a, unsigned v) { __hip_atomic_store((LDS_AS unsigned*)(size_t)a, v, __ATOMIC_RELAXED, __HIP_MEMORY_SCOPE_WORKGROUP); }
DI void lds_sync4(unsigned ctr, unsigned target, int lane) {
    asm volatile("s_waitcnt lgkmcnt(0)" ::: "memory");
    LDS_AS unsigned* c = (LDS_AS unsigned*)(size_t)ctr;
    if (lane == 0) __hip_atomic_fetch_add(c, 1u, __ATOMIC_RELAXED, __HIP_MEMORY_SCOPE_WORKGROUP);
#pragma unroll 1
    for (int spin = 0; spin < SPIN_CAP; spin++) {
        unsigned v = __builtin_amdgcn_readfirstlane(__hip_atomic_load(c, __ATOMIC_RELAXED, __HIP_MEMORY_SCOPE_WORKGROUP));
        if (v >= target) break;
        __builtin_amdgcn_s_sleep(1);
    }
    asm volatile("" ::: "memory");
}

struct Params {
    const unsigned short *x, *wq, *wk, *wv, *wo, *w1, *b1, *w2, *b2, *gw;
    unsigned short* out;
    char* ws;
};

constexpr unsigned X_SLOT = 16384, L_WQ = 114688, L_RING = 143360, L_QX = L_WQ;
constexpr unsigned L_WO = 0, WO_SLICE = 14336, L_MRG = 57344, L_RED = 106496, B_AREA = 12288;
constexpr unsigned P1_GW = 159744, L_G = P1_GW + 1024;
constexpr unsigned L_CTR = L_G + 512;
constexpr unsigned C_QW = 0, C_QR = 4, C_OW = 8, C_RED = 12, C_AP = 16, C_B1 = 20, C_KVS = 24, L_XF = L_CTR + 32, L_XE = L_CTR + 48;
constexpr unsigned LDS_BYTES = L_CTR + 64;
static_assert(L_RING + 4 * 4096 <= P1_GW && L_RED + 4 * B_AREA <= P1_GW && L_MRG + 12 * 4096 <= L_RED && LDS_BYTES <= 163840 && (L_XF & 15) == 0, "LDS map");

template <int N> DI void wait_vm() { asm volatile("s_waitcnt vmcnt(%0)" ::"n"(N) : "memory"); }
DI void wait_bar() { asm volatile("s_waitcnt lgkmcnt(0)\n\ts_barrier" ::: "memory"); }
DI u32x4 lds_flags(unsigned a) { u32x4 v; asm volatile("ds_read_b128 %0, %1" : "=v"(v) : "v"(a) : "memory"); return v; }
DI void lds_wait_ge(unsigned a, unsigned need) {
#pragma unroll 1
    for (int spin = 0; spin < SPIN_CAP; spin++) {
        if ((unsigned)__builtin_amdgcn_readfirstlane(lds_ld(a)) >= need) break;
        __builtin_amdgcn_s_sleep(1);
    }
    asm volatile("" ::: "memory");
}
DI void x_wait(unsigned xf, unsigned need, int only) {
#pragma unroll 1
    for (int spin = 0; spin < SPIN_CAP; spin++) {
        unsigned m;
        if (only < 0) {
            const unsigned a = lds_ld(xf), b = lds_ld(xf + 4), c = lds_ld(xf + 8), d = lds_ld(xf + 12);
            m = min(min(a, b), min(c, d));
        } else m = lds_ld(xf + 4 * only);
        if ((unsigned)__builtin_amdgcn_readfirstlane(m) >= need) break;
        __builtin_amdgcn_s_sleep(1);
    }
    asm volatile("" ::: "memory");
}
DI unsigned x_addr(unsigned lds0, int lane, int s) {
    const int h = lane >> 5, l31 = lane & 31;
    return lds0 + l31 * 128 + (((2 * s + h) ^ ((l31 >> 1) & 7)) << 4);
}
DI unsigned w_lane(int lane) { return (lane >> 5) * 512 + ((lane & 15) >> 2) * 64 + ((lane >> 4) & 1) * 32 + (lane & 3) * 8; }
#define NR1 8
template <typename Ld>
DI void proj_loop(f32x16 (&acc)[4], unsigned lds0, unsigned ring, int lane, bool light, Ld&& ld) {
    constexpr int NR = NR1;
    u32x4 R[NR];
#define W1_DELAY 3
#define LFFN_GATE 1
    if (light) { if (LFFN_GATE) lds_wait_ge(lds0 + L_CTR + C_KVS, 2); else x_wait(lds0 + L_XE, 1, -1); }
    else if (W1_DELAY > 0) x_wait(lds0 + L_XF, W1_DELAY, -1);
#pragma unroll
    for (int s = 0; s < NR; s++) R[s] = ld(s);
    unsigned xa[4];
#pragma unroll
    for (int s = 0; s < 4; s++) xa[s] = x_addr(lds0, lane, s);
    const unsigned wr = ring + lane * 16, rd = ring + w_lane(lane), xf = lds0 + L_XF;
    bf16x8 xf_[3][4], wf[3];
#define PWRITE(s_) do { *(LDS_AS u32x4*)(size_t)(wr + ((s_) & 3) * 1024) = R[(s_) % NR]; if ((s_) + NR < 28) R[(s_) % NR] = ld((s_) + NR); } while (0)
#define PREADW(s_) do { wf[(s_) % 3] = lds_tr2(rd + ((s_) & 3) * 1024, rd + ((s_) & 3) * 1024 + 256); } while (0)
#define PREADX(s_, t_) do { xf_[(s_) % 3][t_] = lds_b128(xa[(s_) & 3] + ((s_) >> 2) * X_SLOT + (t_) * 4096); } while (0)
    PWRITE(0); PWRITE(1); PWRITE(2);
    x_wait(xf, 1, -1);
    PREADW(0); PREADX(0, 0); PREADX(0, 1); PREADX(0, 2); PREADX(0, 3);
    PREADW(1); PREADX(1, 0); PREADX(1, 1); PREADX(1, 2); PREADX(1, 3);
#pragma unroll
    for (int s = 0; s < 28; s++) {
        SB;
        if ((s & 3) == 2 && s + 2 < 28) x_wait(xf, (s + 2) / 4 + 1, -1);
        acc[0] = MFMA(wf[s % 3], xf_[s % 3][0], acc[0]); SB;
        if (s + 2 < 28) { PREADW(s + 2); PREADX(s + 2, 0); } SB;
        acc[1] = MFMA(wf[s % 3], xf_[s % 3][1], acc[1]); SB;
        if (s + 2 < 28) { PREADX(s + 2, 1); PREADX(s + 2, 2); } SB;
        acc[2] = MFMA(wf[s % 3], xf_[s % 3][2], acc[2]); SB;
        if (s + 2 < 28) PREADX(s + 2, 3);
        if (s + 3 < 28) PWRITE(s + 3);
        SB;
        acc[3] = MFMA(wf[s % 3], xf_[s % 3][3], acc[3]);
    }
    SB;
#undef PWRITE
#undef PREADW
#undef PREADX
}
DI void gelu_store(const Params& p, const f32x16& acc, const u32x2 (&bv)[4], int T, int fb, int lane) {
    f32x16 a;
#pragma unroll
    for (int q = 0; q < 4; q++) {
        a[4 * q + 0] = acc[4 * q + 0] + __uint_as_float(bv[q][0] << 16); a[4 * q + 1] = acc[4 * q + 1] + __uint_as_float(bv[q][0] & 0xffff0000u);
        a[4 * q + 2] = acc[4 * q + 2] + __uint_as_float(bv[q][1] << 16); a[4 * q + 3] = acc[4 * q + 3] + __uint_as_float(bv[q][1] & 0xffff0000u);
    }
#pragma unroll
    for (int r = 0; r < 16; r += 2) { const f32x2_ y = gelu_tanh2((f32x2_){a[r], a[r + 1]}); a[r] = y[0]; a[r + 1] = y[1]; }
    const unsigned dst = (unsigned)WS_HF + (T * 112 + 2 * fb) * 1024 + lane * 16;
    const __amdgpu_buffer_rsrc_t r_ = ws_rsrc(p.ws);
    st_sc1(r_, dst, pack8(a, 0)); st_sc1(r_, dst + 1024, pack8(a, 1));
}

DI float max16(const f32x16& s) {
    float a = __builtin_fmaxf(__builtin_fmaxf(s[0], s[1]), s[2]), b = __builtin_fmaxf(__builtin_fmaxf(s[3], s[4]), s[5]);
#pragma unroll
    for (int r = 6; r < 14; r += 4) { a = __builtin_fmaxf(__builtin_fmaxf(a, s[r]), s[r + 1]); b = __builtin_fmaxf(__builtin_fmaxf(b, s[r + 2]), s[r + 3]); }
    a = __builtin_fmaxf(__builtin_fmaxf(a, s[14]), s[15]);
    return __builtin_fmaxf(a, b);
}
DI void set_max(bf16x8& q1, float m, int h) {
    auto rr = __builtin_amdgcn_permlane32_swap(__float_as_uint(m), __float_as_uint(m), false, false);
    const float nm = -__builtin_fmaxf(__uint_as_float(rr[0]), __uint_as_float(rr[1]));
    const unsigned a = cvtpk(nm, 0.f) & 0xffffu;
    const float r1 = nm - __uint_as_float(a << 16);
    const unsigned b = cvtpk(r1, 0.f) & 0xffffu;
    const float r2 = r1 - __uint_as_float(b << 16);
    const unsigned c = cvtpk(r2, 0.f) & 0xffffu;
    u32x4 t = __builtin_bit_cast(u32x4, q1);
    if (h == 0) { t[2] = a | (b << 16); t[3] = c; }
    q1 = __builtin_bit_cast(bf16x8, t);
}
DI bf16x8 ld_sc1s(__amdgpu_buffer_rsrc_t r, unsigned off, unsigned soff) { return __builtin_bit_cast(bf16x8, __builtin_amdgcn_raw_buffer_load_b128(r, off, soff, 16)); }
DI bf16x8 ld_sc1(__amdgpu_buffer_rsrc_t r, unsigned off) { return __builtin_bit_cast(bf16x8, __builtin_amdgcn_raw_buffer_load_b128(r, off, 0, 16)); }

DI void a_wave(const Params& p, unsigned lds0, int g, int j, int w, int lane, u64 epoch_v) {
    const int h = lane >> 5, l31 = lane & 31;
    const int T = 4 * g + w;
    f32x16 accq = {};
    __builtin_amdgcn_s_setprio(A1_PRIO);
    unsigned xoff[4];
#pragma unroll
    for (int i = 0; i < 4; i++) {
        const int row = 32 * w + 8 * i + (lane >> 3);
        const int c = (lane & 7) ^ ((row >> 1) & 7);
        xoff[i] = row * 896 + c * 16;
    }
    const char* xb = (const char*)p.x + (size_t)g * 128 * 896;
    const unsigned qoff = (16 * w + (lane >> 2)) * 896 + ((lane & 3) == 3 ? 40 : (lane & 3) * 16);
    const char* qb_ = (const char*)p.wq + j * 56;
    glds16((lane < 56 ? lane : 55) * 16, p.gw, lds0 + P1_GW);
    auto dma = [&](int c) {
#pragma unroll
        for (int i = 0; i < 4; i++) glds16(xoff[i], xb + c * 128, lds0 + c * X_SLOT + (32 * w + 8 * i) * 128);
        glds16(qoff, qb_ + c * (64 * 896), lds0 + L_WQ + c * 4096 + w * 1024);
    };
    const bool light = j >= 12;
    if (light) {
        const int row = 32 * (j - 12) + 8 * w + (lane >> 3);
        const unsigned eo = row * 896 + (((lane & 7) ^ ((row >> 1) & 7)) << 4);
#pragma unroll
        for (int c = 0; c < 7; c++) glds16(eo, xb + c * 128, lds0 + c * X_SLOT + (32 * (j - 12) + 8 * w) * 128);
    }
    dma(0);
    if (!light) { dma(1); dma(2); }
    wait_bar();
    if (light) {
        wait_vm<5>();
        if (lane == 0) lds_st(lds0 + L_XE + 4 * w, 1);
        dma(1); dma(2); dma(3); dma(4); dma(5); dma(6);
    }
    u64 tag;
    const __amdgpu_buffer_rsrc_t wsr = ws_rsrc(p.ws);
    const int t0 = (4 * g + 4 * j + 16 * w) & 63;
    bf16x8 K0[4], K1[4], V0[4], V1[4];
    const unsigned kvb = (unsigned)WS_KV + lane * 16;
#define KVLOAD(set, vo) do { const unsigned o_ = (vo); K0[set] = ld_sc1(wsr, o_); K1[set] = ld_sc1(wsr, o_ + 1024); V0[set] = ld_sc1(wsr, o_ + 2048); V1[set] = ld_sc1(wsr, o_ + 3072); } while (0)
    {
        unsigned xa[4];
#pragma unroll
        for (int s = 0; s < 4; s++) xa[s] = x_addr(lds0, lane, s) + w * 4096;
        const unsigned wa = lds0 + L_WQ + w_lane(lane);
        unsigned short gwv[7];
        auto publish = [&](int c) {
            if (lane < 16) *(LDS_AS unsigned short*)(size_t)(lds0 + L_WQ + c * 4096 + w * 1024 + lane * 64 + 48) = gwv[c];
            asm volatile("s_waitcnt lgkmcnt(0)" ::: "memory");
            if (lane == 0) lds_st(lds0 + L_XF + 4 * w, c + 1);
        };
        auto qchunk = [&](int c) {
            bf16x8 xf[4], wf[4];
#pragma unroll
            for (int s = 0; s < 4; s++) { xf[s] = lds_b128(xa[s] + c * X_SLOT); wf[s] = lds_tr2(wa + c * 4096 + s * 1024, wa + c * 4096 + s * 1024 + 256); }
#pragma unroll
            for (int s = 0; s < 4; s++) accq = MFMA(wf[s], xf[s], accq);
        };
        if (light) wait_vm<30>(); else wait_vm<10>();
#pragma unroll
        for (int c = 0; c < 7; c++) gwv[c] = *(const LDS_AS unsigned short*)(size_t)(lds0 + P1_GW + (64 * c + 16 * w + (lane & 15)) * 2);
        publish(0);
        if (light) wait_vm<25>(); else { dma(3); wait_vm<10>(); }
        publish(1);
        if (light) wait_vm<20>(); else { dma(4); wait_vm<10>(); }
        publish(2);
        if (light) wait_vm<15>(); else { dma(5); wait_vm<10>(); }
        publish(3);
        if (!light) dma(6);
        wait_vm<10>(); publish(4);
        x_wait(lds0 + L_XF, 2, -1); qchunk(0); qchunk(1); SB;
        wait_vm<5>(); publish(5);
        x_wait(lds0 + L_XF, 4, -1); qchunk(2); qchunk(3); SB;
        wait_vm<0>(); publish(6);
        tag = uniform64(epoch_v) + 1;
        const unsigned fo = WS_FKV + (g * 4 + (j >> 2)) * FKV_COPY + ((t0 + (lane >> 1)) & 63) * 16 + (lane & 1) * 8;
        u64 fv = tag;
        if (lane < 32) fv = __hip_atomic_load((const u64*)(p.ws + fo), __ATOMIC_RELAXED, __HIP_MEMORY_SCOPE_AGENT);
        x_wait(lds0 + L_XF, 6, -1); qchunk(4); qchunk(5); SB;
        x_wait(lds0 + L_XF, 7, -1); qchunk(6); SB;
        asm volatile("" : "+v"(fv) :: "memory");
        if (__builtin_amdgcn_ballot_w64(fv != tag) != 0) flags_wait(p.ws, fo, lane < 32, tag);
        KVLOAD(0, kvb + t0 * 4096); KVLOAD(1, kvb + (t0 + 1) * 4096); KVLOAD(2, kvb + (t0 + 2) * 4096);
        SB;
    }
    const float zz = __uint_as_float(__builtin_amdgcn_permlane32_swap(__float_as_uint(accq[12]), __float_as_uint(accq[12]), false, false)[0]);
    const float gtok = __builtin_amdgcn_rcpf(1.0f + EX2(-1.4426950408889634f * zz));
    if (h == 0) *(LDS_AS float*)(size_t)(lds0 + L_G + (32 * w + l31) * 4) = gtok;
    bf16x8 q0[4], q1[4];
    bf16x8 qa, qb;
    {
        const float qs = 1.4426950408889634f * 0.1889822365046136f;
        f32x16 q = accq * qs;
        u32x4 a0 = pack8(q, 0), a1 = pack8(q, 1);
        if (h == 0) { a1[2] = 0; a1[3] = 0; }
        qa = __builtin_bit_cast(bf16x8, a0); qb = __builtin_bit_cast(bf16x8, a1);
    }
    asm volatile("s_waitcnt lgkmcnt(0)" ::: "memory");
    if (lane == 0) __hip_atomic_fetch_add((LDS_AS unsigned*)(size_t)(lds0 + L_CTR + C_AP), 1u, __ATOMIC_RELAXED, __HIP_MEMORY_SCOPE_WORKGROUP);
    const f32x16 zero = {};
    V1[3] = (bf16x8){}; K0[3] = V1[3]; K1[3] = V1[3]; V0[3] = V1[3];
    f32x16 S = MFMA(K0[0], qa, zero); S = MFMA(K1[0], qb, S);
    set_max(qb, max16(S), h);
    const unsigned lo = lane * 16;
    *(LDS_AS bf16x8*)(size_t)(lds0 + L_QX + w * 2048 + lo) = qa;
    *(LDS_AS bf16x8*)(size_t)(lds0 + L_QX + w * 2048 + 1024 + lo) = qb;
    lds_sync4(lds0 + L_CTR + C_QW, 4, lane);
#pragma unroll
    for (int b = 0; b < 4; b++) { q0[b] = lds_b128(lds0 + L_QX + b * 2048 + lo); q1[b] = lds_b128(lds0 + L_QX + b * 2048 + 1024 + lo); }
    asm volatile("s_waitcnt lgkmcnt(0)" ::: "memory");
    if (lane == 0) __hip_atomic_fetch_add((LDS_AS unsigned*)(size_t)(lds0 + L_CTR + C_QR), 1u, __ATOMIC_RELAXED, __HIP_MEMORY_SCOPE_WORKGROUP);
    __builtin_amdgcn_s_setprio(A2_PRIO);
    S = MFMA(K0[0], q0[0], zero); S = MFMA(K1[0], q1[0], S);
    f32x16 O[4];
#pragma unroll
    for (int b = 0; b < 4; b++) O[b] = zero;
    u32x4 P1 = {0, 0, 0, 0};
#define ASTEP(c, b) do { \
        constexpr int pc = (b) == 0 ? ((c) + 3) & 3 : (c), pb = ((b) + 3) & 3, nc = (b) == 3 ? ((c) + 1) & 3 : (c), nb = ((b) + 1) & 3; \
        O[pb] = MFMA(V1[pc], __builtin_bit_cast(bf16x8, P1), O[pb]); SB; \
        u32x4 p0, p1; \
        p0[0] = cvtpk(EX2(S[0]), EX2(S[1])); p0[1] = cvtpk(EX2(S[2]), EX2(S[3])); SB; \
        f32x16 Sn = MFMA(K0[nc], q0[nb], zero); SB; \
        if ((b) == 1) { KVLOAD(pc3(c), vnext); } \
        p0[2] = cvtpk(EX2(S[4]), EX2(S[5])); p0[3] = cvtpk(EX2(S[6]), EX2(S[7])); SB; \
        Sn = MFMA(K1[nc], q1[nb], Sn); SB; \
        p1[0] = cvtpk(EX2(S[8]), EX2(S[9])); p1[1] = cvtpk(EX2(S[10]), EX2(S[11])); SB; \
        O[b] = MFMA(V0[c], __builtin_bit_cast(bf16x8, p0), O[b]); SB; \
        p1[2] = cvtpk(EX2(S[12]), EX2(S[13])); p1[3] = cvtpk(EX2(S[14]), EX2(S[15])); SB; \
        P1 = p1; S = Sn; } while (0)
#define pc3(c) (((c) + 3) & 3)
#define ATILE(c) do { \
        const int tn = tt + (c) + 3;     \
        const unsigned vnext = lo + (tn < 16 ? (unsigned)WS_KV + ((t0 + tn) & 63) * 4096u : 0x7f000000u); \
        ASTEP(c, 0); ASTEP(c, 1); ASTEP(c, 2); ASTEP(c, 3); } while (0)
#pragma unroll 1
    for (int tt = 0; tt < 16; tt += 4) {
        ATILE(0); ATILE(1); ATILE(2); ATILE(3);
    }
    O[3] = MFMA(V1[3], __builtin_bit_cast(bf16x8, P1), O[3]);
    lds_wait_ge(lds0 + L_CTR + C_B1, 4);
#pragma unroll
    for (int b = 0; b < 4; b++) {
        if (b != w) {
            const unsigned a = lds0 + L_MRG + (b * 3 + ((w - b - 1) & 3)) * 4096 + lo;
#pragma unroll
            for (int rg = 0; rg < 4; rg++) {
                f32x4 v = {O[b][4 * rg], O[b][4 * rg + 1], O[b][4 * rg + 2], O[b][4 * rg + 3]};
                *(LDS_AS f32x4*)(size_t)(a + rg * 1024) = v;
            }
        }
    }
    f32x16 o = zero;
#pragma unroll
    for (int b = 0; b < 4; b++) if (b == w) { o = O[b]; qa = q0[b]; qb = q1[b]; }
    lds_sync4(lds0 + L_CTR + C_OW, 4, lane);
#pragma unroll
    for (int u = 0; u < 3; u++) {
        const unsigned a = lds0 + L_MRG + (w * 3 + u) * 4096 + lo;
#pragma unroll
        for (int rg = 0; rg < 4; rg++) {
            f32x4 v = *(const LDS_AS f32x4*)(size_t)(a + rg * 1024);
            o[4 * rg] += v[0]; o[4 * rg + 1] += v[1]; o[4 * rg + 2] += v[2]; o[4 * rg + 3] += v[3];
        }
    }
    auto rr = __builtin_amdgcn_permlane32_swap(__float_as_uint(o[12]), __float_as_uint(o[12]), false, false);
    float l = __uint_as_float(rr[0]);
    {
        bool ok = l < 1.152921504606846976e18f && l > 8.673617379884035e-19f;
#pragma unroll
        for (int r = 0; r < 16; r++) ok = ok && (__builtin_fabsf(o[r]) < 3.0e38f);
        if (__builtin_amdgcn_ballot_w64(!ok) != 0) {
            u32x4 tq = __builtin_bit_cast(u32x4, qb);
            if (h == 0) { tq[2] = 0; tq[3] = 0; }
            qb = __builtin_bit_cast(bf16x8, tq);
            float m = -INFINITY;
#pragma unroll 1
            for (int t = 0; t < 64; t++) {
                bf16x8 k0 = ld_sc1(wsr, kvb + t * 4096), k1 = ld_sc1(wsr, kvb + t * 4096 + 1024);
                f32x16 s = MFMA(k0, qa, zero); s = MFMA(k1, qb, s);
                m = __builtin_fmaxf(m, max16(s));
            }
            set_max(qb, m, h);
            o = zero;
#pragma unroll 1
            for (int t = 0; t < 64; t++) {
                bf16x8 k0 = ld_sc1(wsr, kvb + t * 4096), k1 = ld_sc1(wsr, kvb + t * 4096 + 1024);
                bf16x8 v0 = ld_sc1(wsr, kvb + t * 4096 + 2048), v1 = ld_sc1(wsr, kvb + t * 4096 + 3072);
                f32x16 s = MFMA(k0, qa, zero); s = MFMA(k1, qb, s);
#pragma unroll
                for (int r = 0; r < 16; r++) s[r] = EX2(s[r]);
                o = MFMA(v0, __builtin_bit_cast(bf16x8, pack8(s, 0)), o); o = MFMA(v1, __builtin_bit_cast(bf16x8, pack8(s, 1)), o);
            }
            auto r2 = __builtin_amdgcn_permlane32_swap(__float_as_uint(o[12]), __float_as_uint(o[12]), false, false);
            l = __uint_as_float(r2[0]);
        }
    }
    const float sc = gtok * __builtin_amdgcn_rcpf(l);
#pragma unroll
    for (int r = 0; r < 16; r++) o[r] *= sc;
    u32x4 o0 = pack8(o, 0), o1 = pack8(o, 1);
    if (h == 0) { o1[2] = 0; o1[3] = 0; }
    const unsigned dst = (unsigned)WS_OF + (T * 32 + 2 * j) * 1024 + lo;
    st_sc1(wsr, dst, o0); st_sc1(wsr, dst + 1024, o1);
    asm volatile("s_waitcnt vmcnt(0)" ::: "memory");
    if (lane < 7) flag_set(p.ws, WS_FO + (T * 7 + lane) * 128 + j * 8, tag);
}

DI void b_wave(const Params& p, unsigned lds0, int g, int j, int wb, int lane, u64 epoch_v) {
    const int h = lane >> 5, l31 = lane & 31;
    const bool light = j >= 12;
    const int lp = j - 12;
    const int fb = light ? 48 + 2 * lp + wb : 4 * j + wb;
    const unsigned lo = lane * 16;
    f32x16 acc[4];
#pragma unroll
    for (int b = 0; b < 4; b++) acc[b] = (f32x16){};
    __builtin_amdgcn_s_setprio(B_PRIO);
    const __amdgpu_buffer_rsrc_t wsr = ws_rsrc(p.ws);
    const unsigned ring = lds0 + L_RING + wb * 4096;
    u64 tag;
    if (light && wb >= 2) {
        __builtin_amdgcn_s_setprio(KV_PRIO);
        const int T = 4 * g + lp;
        const __amdgpu_buffer_rsrc_t rk = __builtin_amdgcn_make_buffer_rsrc((void*)(wb == 2 ? p.wk : p.wv), 0, 448 * 56, 0x00020000);
        const unsigned off = (lane >> 2) * 56 + ((lane & 3) == 3 ? 40 : (lane & 3) * 16);
        const unsigned dst = (unsigned)WS_KV + T * 4096 + lo;
        u32x4 R[28];
#pragma unroll
        for (int s = 0; s < 28; s++) R[s] = __builtin_amdgcn_raw_buffer_load_b128(rk, off, s * (16 * 56), 0);
        SB; wait_bar(); SB;
        const unsigned wr = ring + lo, rd = ring + w_lane(lane);
        bf16x8 wf[28];
#pragma unroll
        for (int q = 0; q < 28; q += 4) {
#pragma unroll
            for (int u = 0; u < 4; u++) *(LDS_AS u32x4*)(size_t)(wr + u * 1024) = R[q + u];
#pragma unroll
            for (int u = 0; u < 4; u++) wf[q + u] = lds_tr2(rd + u * 1024, rd + u * 1024 + 256);
            SB;
        }
        x_wait(lds0 + L_XE, 1, -1);
        f32x16 a2 = {};
        {
            unsigned xa[4];
#pragma unroll
            for (int s = 0; s < 4; s++) xa[s] = x_addr(lds0, lane, s) + lp * 4096;
            bf16x8 xf[2][4];
#define KVX(q_) _Pragma("unroll") for (int u = 0; u < 4; u++) xf[(q_) & 1][u] = lds_b128(xa[u] + (q_) * X_SLOT)
            KVX(0); KVX(1);
            if (wb == 2) {
#pragma unroll
                for (int q = 0; q < 7; q++) {
                    SB;
#pragma unroll
                    for (int u = 0; u < 4; u++) { if (u & 1) a2 = MFMA(wf[4 * q + u], xf[q & 1][u], a2); else acc[0] = MFMA(wf[4 * q + u], xf[q & 1][u], acc[0]); }
                    SB;
                    if (q + 2 < 7) { KVX(q + 2); }
                }
            } else {
#pragma unroll
                for (int q = 0; q < 7; q++) {
                    SB;
#pragma unroll
                    for (int u = 0; u < 4; u++) { if (u & 1) a2 = MFMA(xf[q & 1][u], wf[4 * q + u], a2); else acc[0] = MFMA(xf[q & 1][u], wf[4 * q + u], acc[0]); }
                    SB;
                    if (q + 2 < 7) { KVX(q + 2); }
                }
            }
        }
        acc[0] += a2;
        {
            u32x4 a0 = pack8(acc[0], 0), a1 = pack8(acc[0], 1);
            if (wb == 2) {
                if (h == 0) { a1[2] = 0x3f803f80u; a1[3] = 0x00003f80u; }
                st_sc1(wsr, dst, a0); st_sc1(wsr, dst + 1024, a1);
            } else {
                if (l31 == 24) { a0 = (u32x4){0x3f803f80u, 0x3f803f80u, 0x3f803f80u, 0x3f803f80u}; a1 = a0; }
                else if (l31 > 24 && l31 < 28) { a0 = (u32x4){0, 0, 0, 0}; a1 = a0; }
                st_sc1(wsr, dst + 2048, a0); st_sc1(wsr, dst + 3072, a1);
            }
        }
        if (lane == 0) __hip_atomic_fetch_add((LDS_AS unsigned*)(size_t)(lds0 + L_CTR + C_B1), 1u, __ATOMIC_RELAXED, __HIP_MEMORY_SCOPE_WORKGROUP);
#define KVS_AT 1
        if (KVS_AT == 0 && lane == 0) __hip_atomic_fetch_add((LDS_AS unsigned*)(size_t)(lds0 + L_CTR + C_KVS), 1u, __ATOMIC_RELAXED, __HIP_MEMORY_SCOPE_WORKGROUP);
        tag = uniform64(epoch_v) + 1;
        asm volatile("s_waitcnt vmcnt(0)" ::: "memory");
        flag_set(p.ws, WS_FKV + lane * FKV_COPY + T * 16 + (wb & 1) * 8, tag);
        if (KVS_AT == 1 && lane == 0) __hip_atomic_fetch_add((LDS_AS unsigned*)(size_t)(lds0 + L_CTR + C_KVS), 1u, __ATOMIC_RELAXED, __HIP_MEMORY_SCOPE_WORKGROUP);
    } else {
        const __amdgpu_buffer_rsrc_t rw1 = __builtin_amdgcn_make_buffer_rsrc((void*)p.w1, 0, 448 * 3584, 0x00020000);
        const unsigned off = (lane >> 2) * 3584 + fb * 64 + (lane & 3) * 16;
        u32x2 bv[4];
#pragma unroll
        for (int q = 0; q < 4; q++) bv[q] = *(const u32x2*)(p.b1 + fb * 32 + 4 * h + 8 * q);
        wait_bar();
        auto ld = [&](int s) { return __builtin_amdgcn_raw_buffer_load_b128(rw1, off, s * (16 * 3584), 0); };
        proj_loop(acc, lds0, ring, lane, light, ld);
        if (lane == 0) __hip_atomic_fetch_add((LDS_AS unsigned*)(size_t)(lds0 + L_CTR + C_B1), 1u, __ATOMIC_RELAXED, __HIP_MEMORY_SCOPE_WORKGROUP);
        tag = uniform64(epoch_v) + 1;
        __builtin_amdgcn_s_setprio(G_PRIO);
#pragma unroll
        for (int t = 0; t < 4; t++) gelu_store(p, acc[t], bv, 4 * g + t, fb, lane);
        __builtin_amdgcn_s_setprio(F_PRIO);
        asm volatile("s_waitcnt vmcnt(0)" ::: "memory");
        if (lane < 14) flag_set(p.ws, WS_FH + (g * 14 + lane) * 512 + fb * 8, tag);
    }
    const bool tile = j < 14;
    if (!tile) {
        if (g == 0 && j == 15 && wb == 0) {
#pragma unroll 1
            for (int stage = 0; stage < 3; stage++) {
                const char* base = p.ws + (stage == 0 ? WS_FKV + lane * 8 : stage == 1 ? WS_FH + lane * 8 : WS_FO + (lane >> 4) * (7 * 128) + (lane & 15) * 8);
                const int n = stage == 0 ? 2 : 16;
                const unsigned step = stage == 0 ? 512 : stage == 1 ? 14 * 512 : 4 * 7 * 128;
                const bool on = stage != 1 || lane < 56;
#pragma unroll 1
                for (int spin = 0; spin < SPIN_CAP; spin++) {
                    u64 v[16];
#pragma unroll
                    for (int i = 0; i < 16; i++) v[i] = __hip_atomic_load((const u64*)(base + (i < n ? i : 0) * step), __ATOMIC_RELAXED, __HIP_MEMORY_SCOPE_AGENT);
                    unsigned bad = 0;
#pragma unroll
                    for (int i = 0; i < 16; i++) bad |= (v[i] != tag) ? 1u : 0u;
                    if (__builtin_amdgcn_ballot_w64(on && bad) == 0) break;
                    __builtin_amdgcn_s_sleep(16);
                }
            }
            if (lane == 0) __hip_atomic_store((u64*)(p.ws + WS_EPOCH), tag, __ATOMIC_RELAXED, __HIP_MEMORY_SCOPE_AGENT);
        }
        return;
    }
    const unsigned stg = lds0 + L_WO + wb * WO_SLICE;
    const int th = j & 1, cs = j >> 1, n0 = 64 * cs, T0 = 4 * g + 2 * th;
    const unsigned woff = ((lane & 31) >> 2) * 896 + (lane >> 5) * 64 + (lane & 3) * 16;
    const __amdgpu_buffer_rsrc_t rw2 = __builtin_amdgcn_make_buffer_rsrc((void*)p.w2, 0, 1792 * 896, 0x00020000);
    const unsigned w2v = woff + n0 * 2, w2s = 28 * wb * 16 * 896;
#define NBW 7
#define NBH 7
#define WO_LATE 4
#define FH1 4
    constexpr int NB = NBW, NH = NBH;
    u32x4 Wr[NB][2];
#define ROT28(st_) (st_)
#define WLOAD(st_) do { const unsigned so_ = w2s + ROT28(st_) * (16 * 896); Wr[(st_) % NB][0] = __builtin_amdgcn_raw_buffer_load_b128(rw2, w2v, so_, 0); \
        Wr[(st_) % NB][1] = __builtin_amdgcn_raw_buffer_load_b128(rw2, w2v + 8 * 896, so_, 0); } while (0)
#pragma unroll
    for (int st = 0; st < NB; st++) WLOAD(st);
    const int Tm = T0 + (wb & 1);
    const int nc = n0 + 32 * (wb >> 1) + 4 * h;
    u32x2 b2v[4];
#pragma unroll
    for (int rg = 0; rg < 4; rg++) b2v[rg] = *(const u32x2*)(p.b2 + nc + 8 * rg);
    const unsigned fho = WS_FH + (g * 14 + j) * 512 + (14 * wb + lane) * 8;
    lds_wait_ge(lds0 + L_CTR + C_AP, 4);
    lds_wait_ge(lds0 + L_CTR + C_B1, 4);
    const char* wos = (const char*)p.wo + n0 * 2;
#pragma unroll
    for (int i = WO_LATE; i < 14; i++) glds16(woff, wos + (size_t)(14 * wb + i) * (8 * 896), lds0 + L_WO + (14 * wb + i) * 1024);
    flags_wait(p.ws, fho, lane < FH1, tag);
#pragma unroll
    for (int i = 0; i < 4; i++) acc[i] = (f32x16){};
    {
        const unsigned wrd = stg + (4 * h + ((lane & 15) >> 2)) * 64 + ((lane >> 4) & 1) * 32 + (lane & 3) * 8;
        const unsigned hv = (unsigned)WS_HF + (T0 * 112 + 28 * wb) * 1024 + lo;
        bf16x8 H0[NH], H1[NH], wf0[2], wf1[2];
#define HLOAD(st_) do { const unsigned so_ = ROT28(st_) * 1024; H0[(st_) % NH] = ld_sc1s(wsr, hv, so_); H1[(st_) % NH] = ld_sc1s(wsr, hv + 112 * 1024, so_); } while (0)
#define WWRITE(st_) do { *(LDS_AS u32x4*)(size_t)(stg + ((st_) & 1) * 2048 + lo) = Wr[(st_) % NB][0]; *(LDS_AS u32x4*)(size_t)(stg + ((st_) & 1) * 2048 + 1024 + lo) = Wr[(st_) % NB][1]; } while (0)
#define WREAD(st_) do { wf0[(st_) & 1] = lds_tr2(wrd + ((st_) & 1) * 2048, wrd + ((st_) & 1) * 2048 + 1024); wf1[(st_) & 1] = lds_tr2(wrd + ((st_) & 1) * 2048 + 512, wrd + ((st_) & 1) * 2048 + 1536); } while (0)
#pragma unroll
        for (int st = 0; st < NH; st++) HLOAD(st);
        WWRITE(0); WLOAD(NB); WWRITE(1); WLOAD(NB + 1);
        WREAD(0);
#pragma unroll
        for (int st = 0; st < 28; st++) {
            SB;
            if (st + 1 < 28) WREAD(st + 1);
            if (st + 2 < 28) WWRITE(st + 2);
            if (st + 2 + NB < 28) WLOAD(st + 2 + NB);
            SB;
            acc[0] = MFMA(wf0[st & 1], H0[st % NH], acc[0]); acc[1] = MFMA(wf0[st & 1], H1[st % NH], acc[1]);
            acc[2] = MFMA(wf1[st & 1], H0[st % NH], acc[2]); acc[3] = MFMA(wf1[st & 1], H1[st % NH], acc[3]);
            SB;
            if (st + NH == 2 * FH1) {
                u64 fv = tag;
                if (lane >= FH1 && lane < 14) fv = __hip_atomic_load((const u64*)(p.ws + fho), __ATOMIC_RELAXED, __HIP_MEMORY_SCOPE_AGENT);
                if (__builtin_amdgcn_ballot_w64(fv != tag) != 0) flags_wait(p.ws, fho, lane >= FH1 && lane < 14, tag);
                asm volatile("" ::: "memory");
            }
            if (st + NH < 28) HLOAD(st + NH);
        }
    }
    asm volatile("s_waitcnt lgkmcnt(0)" ::: "memory");
#pragma unroll
    for (int i = 0; i < WO_LATE; i++) glds16(woff, wos + (size_t)(14 * wb + i) * (8 * 896), lds0 + L_WO + (14 * wb + i) * 1024);
    lds_wait_ge(lds0 + L_CTR + C_QR, 4);
    {
        const unsigned rarea = lds0 + L_RED + wb * B_AREA;
#pragma unroll
        for (int i = 0; i < 4; i++) {
            if (i != wb) {
                const unsigned a = rarea + ((i - wb - 1) & 3) * 4096 + lo;
#pragma unroll
                for (int rg = 0; rg < 4; rg++) {
                    f32x4 v = {acc[i][4 * rg], acc[i][4 * rg + 1], acc[i][4 * rg + 2], acc[i][4 * rg + 3]};
                    *(LDS_AS f32x4*)(size_t)(a + rg * 1024) = v;
                }
            }
        }
    }
    f32x16 o = {};
#pragma unroll
    for (int i = 0; i < 4; i++) if (i == wb) o = acc[i];
    asm volatile("s_waitcnt vmcnt(0)" ::: "memory");
    lds_sync4(lds0 + L_CTR + C_RED, 4, lane);
#pragma unroll
    for (int u = 0; u < 4; u++) {
        if (u != wb) {
            const unsigned a = lds0 + L_RED + u * B_AREA + ((wb - u - 1) & 3) * 4096 + lo;
#pragma unroll
            for (int rg = 0; rg < 4; rg++) {
                f32x4 v = *(const LDS_AS f32x4*)(size_t)(a + rg * 1024);
                o[4 * rg] += v[0]; o[4 * rg + 1] += v[1]; o[4 * rg + 2] += v[2]; o[4 * rg + 3] += v[3];
            }
        }
    }
    {
        const float fg = 1.0f - *(const LDS_AS float*)(size_t)(lds0 + L_G + (64 * th + 32 * (wb & 1) + l31) * 4);
#pragma unroll
        for (int rg = 0; rg < 4; rg++) {
            const u32x2 bv = b2v[rg];
            o[4 * rg] = (o[4 * rg] + __uint_as_float(bv[0] << 16)) * fg; o[4 * rg + 1] = (o[4 * rg + 1] + __uint_as_float(bv[0] & 0xffff0000u)) * fg;
            o[4 * rg + 2] = (o[4 * rg + 2] + __uint_as_float(bv[1] << 16)) * fg; o[4 * rg + 3] = (o[4 * rg + 3] + __uint_as_float(bv[1] & 0xffff0000u)) * fg;
        }
    }
    {
        const unsigned ob = (unsigned)WS_OF + Tm * 32 * 1024 + lo;
        const unsigned foo = WS_FO + (Tm * 7 + cs) * 128 + (lane & 15) * 8;
        bf16x8 of[32];
        unsigned ready = 0;
#pragma unroll
        for (int q = 0; q < 4; q++) {
#pragma unroll 1
            for (int spin = 0; spin < SPIN_CAP && ((ready >> (4 * q)) & 15u) != 15u; spin++) {
                if (spin) __builtin_amdgcn_s_sleep(POLL_SLEEP);
                u64 v = 0;
                if (lane < 16) v = __hip_atomic_load((const u64*)(p.ws + foo), __ATOMIC_RELAXED, __HIP_MEMORY_SCOPE_AGENT);
                ready = (unsigned)__builtin_amdgcn_ballot_w64(lane < 16 && v == tag);
            }
            asm volatile("" ::: "memory");
            #pragma unroll
            for (int st = 8 * q; st < 8 * q + 8; st++) of[st] = ld_sc1(wsr, ob + st * 1024);
        }
        const unsigned lp16 = lds0 + L_WO + (wb >> 1) * 512 + ((lane & 15) >> 2) * 64 + ((lane >> 4) & 1) * 32 + (lane & 3) * 8;
        const unsigned wA = lp16 + h * 256, wB = lp16 + h * 768;
        f32x16 o2 = {};
#pragma unroll
        for (int st = 0; st < 32; st++) {
            const int hd = st >> 1, s = st & 1;
            const int r1 = 28 * hd + 16 * s, r2 = 28 * hd + (s ? 20 : 8);
            const unsigned a1 = ((r1 & 4) ? wB : wA) + (r1 >> 3) * 1024 + (r1 & 7) * 64, a2 = ((r2 & 4) ? wB : wA) + (r2 >> 3) * 1024 + (r2 & 7) * 64;
            const bf16x8 w0 = lds_tr2(a1, a2);
            if (st & 1) o2 = MFMA(w0, of[st], o2); else o = MFMA(w0, of[st], o);
        }
        o += o2;
    }
    {
#define OUT_AUX 0
        const __amdgpu_buffer_rsrc_t ro = __builtin_amdgcn_make_buffer_rsrc((void*)p.out, 0, 2048 * D_ * 2, 0x00020000);
        const unsigned oo = ((Tm * 32 + l31) * D_ + nc) * 2;
#pragma unroll
        for (int rg = 0; rg < 4; rg++) {
            u32x2 ov = {cvtpk(o[4 * rg], o[4 * rg + 1]), cvtpk(o[4 * rg + 2], o[4 * rg + 3])};
            __builtin_amdgcn_raw_buffer_store_b64(ov, ro, oo + 16 * rg, 0, OUT_AUX);
        }
    }
}

__global__ void __launch_bounds__(512, 2) gated_mix_fused(Params p) {
    extern __shared__ char smem[];
    const unsigned lds0 = (unsigned)(size_t)smem;
    const int b = blockIdx.x, xcd = b & 7, idx = b >> 3;
    const int g = xcd * 2 + (idx & 1), j = idx < 8 ? 12 + (idx >> 1) : (idx - 8) >> 1;
    const int w = __builtin_amdgcn_readfirstlane(threadIdx.x >> 6), lane = threadIdx.x & 63;
    const u64 epoch = __hip_atomic_load((const u64*)(p.ws + WS_EPOCH), __ATOMIC_RELAXED, __HIP_MEMORY_SCOPE_AGENT);
    if (w == 0 && lane < 16) *(LDS_AS unsigned*)(size_t)(lds0 + L_CTR + 4 * lane) = 0;
    if (w < 4) a_wave(p, lds0, g, j, w, lane, epoch);
    else b_wave(p, lds0, g, j, w - 4, lane, epoch);
}

extern "C" void kernel_launch(void* const* d_in, const int* in_sizes, int n_in, void* d_out, int out_size, void* d_ws, size_t ws_size, hipStream_t stream) {
    Params p;
    p.x = (const unsigned short*)d_in[0]; p.wq = (const unsigned short*)d_in[1]; p.wk = (const unsigned short*)d_in[2];
    p.wv = (const unsigned short*)d_in[3]; p.wo = (const unsigned short*)d_in[4]; p.w1 = (const unsigned short*)d_in[6];
    p.b1 = (const unsigned short*)d_in[7]; p.w2 = (const unsigned short*)d_in[8]; p.b2 = (const unsigned short*)d_in[9];
    p.gw = (const unsigned short*)d_in[10]; p.out = (unsigned short*)d_out; p.ws = (char*)d_ws;
    (void)hipFuncSetAttribute((const void*)gated_mix_fused, hipFuncAttributeMaxDynamicSharedMemorySize, LDS_BYTES);
    gated_mix_fused<<<256, 512, LDS_BYTES, stream>>>(p);
}
```
